```python
import torch

def _softmax(x, axis=-1):
    return torch.softmax(x, dim=axis)
def _gelu(x):
    return torch.nn.functional.gelu(x, approximate="tanh")
def _silu(x):
    return torch.nn.functional.silu(x)
def _sigmoid(x):
    return torch.sigmoid(x)
def _layernorm(x, axis=-1, eps=1e-5):
    m = x.mean(dim=axis, keepdim=True)
    v = x.var(dim=axis, keepdim=True, unbiased=False)
    return (x - m) / torch.sqrt(v + eps)
def _scatter_add(tgt, idx, src, axis=0):
    return tgt.clone().index_add_(axis, idx, src)
def _segment_sum(src, seg, n):
    out = torch.zeros((n,) + src.shape[1:], dtype=src.dtype)
    return out.index_add_(0, seg, src)
def _pad(x, pw):
    flat = tuple(v for pair in reversed(pw) for v in pair)
    return torch.nn.functional.pad(x, flat)

kernel_name = 'parallel_sum__mha_block__glu_gated_swiglu__prompt'

def setup_inputs():

    _w = (lambda a: (lambda t: t.to(torch.bfloat16) if t.is_floating_point() else t)(torch.as_tensor(a)))
    def _A_setup():
        B, S, D, H = 1, 2048, 1024, 32
        return {
            "x": _w((lambda *s: torch.randn(*s, dtype=torch.bfloat16) * 0.05)(B, S, D)),
            "wqkv": _w((lambda *s: torch.randn(*s, dtype=torch.bfloat16) * 0.05)(D, 3 * D)),
            "wo": _w((lambda *s: torch.randn(*s, dtype=torch.bfloat16) * 0.05)(D, D)),
        }
    _A_in = _A_setup()

    def _B_setup():
        B, S, D, F = 1, 2048, 1024, 3584
        return {
            "x": _w((lambda *s: torch.randn(*s, dtype=torch.bfloat16) * 0.05)(B, S, D)),
            "w1": _w((lambda *s: torch.randn(*s, dtype=torch.bfloat16) * 0.8)(D, F) * (2.0 / D) ** 0.5),
            "w3": _w((lambda *s: torch.randn(*s, dtype=torch.bfloat16) * 0.8)(D, F) * (2.0 / D) ** 0.5),
            "w2": _w((lambda *s: torch.randn(*s, dtype=torch.bfloat16) * 0.8)(F, D) * (2.0 / F) ** 0.5),
        }
    _B_in = _B_setup()

    return {
        "A_x": _A_in["x"],
        "A_wqkv": _A_in["wqkv"],
        "A_wo": _A_in["wo"],
        "B_x": _B_in["x"],
        "B_w1": _B_in["w1"],
        "B_w3": _B_in["w3"],
        "B_w2": _B_in["w2"],
    }

def reference(A_x, A_wqkv, A_wo, B_x, B_w1, B_w3, B_w2):
    def _A_ref(x, wqkv, wo):
        B, S, D = x.shape
        H = 32
        Dh = D // H
        qkv = (lambda x, *d: x.permute(*d))((x @ wqkv).reshape(B, S, 3, H, Dh), 2, 0, 3, 1, 4)
        q, k, v = qkv[0], qkv[1], qkv[2]
        scores = torch.einsum("bhqd,bhkd->bhqk", q, k) * (Dh ** -0.5)
        p = _softmax(scores, axis=-1)
        o = torch.einsum("bhqk,bhkd->bhqd", p, v)
        return (lambda x, *d: x.permute(*d))(o, 0, 2, 1, 3).reshape(B, S, D) @ wo

    def _B_ref(x, w1, w3, w2):
        gate = torch.einsum("bsd,df->bsf", x, w1)
        up = torch.einsum("bsd,df->bsf", x, w3)
        h = _silu(gate) * up
        out = torch.einsum("bsf,fd->bsd", h, w2)
        return out

    x = A_x
    xn = _layernorm(x, axis=-1)
    return x + _A_ref(xn, A_wqkv, A_wo) + _B_ref(xn, B_w1, B_w3, B_w2)
```

```cpp
#include <hip/hip_runtime.h>
#include <hip/hip_bf16.h>
#include <stdint.h>

#define LAS __attribute__((address_space(3)))
#define GAS __attribute__((address_space(1)))
#define DI __device__ __forceinline__
typedef unsigned short u16;
typedef short bf16x8 __attribute__((ext_vector_type(8)));
typedef short s16x4 __attribute__((ext_vector_type(4)));
typedef float f32x2 __attribute__((ext_vector_type(2)));
typedef float f32x4 __attribute__((ext_vector_type(4)));
typedef float f32x16 __attribute__((ext_vector_type(16)));
typedef unsigned u32x2 __attribute__((ext_vector_type(2)));
typedef unsigned u32x4 __attribute__((ext_vector_type(4)));
typedef __bf16 bf16x2_t __attribute__((ext_vector_type(2)));

constexpr int SEQ = 2048, DM = 1024, FF = 3584, NH = 32;
constexpr int KB_X = DM / 32;
constexpr int KB_AH = (DM + FF) / 32;
constexpr size_t WS_XN = 0;
constexpr size_t WS_Q = WS_XN + (size_t)SEQ * DM * 2;
constexpr size_t WS_K = WS_Q + (size_t)SEQ * DM * 2;
constexpr size_t WS_V = WS_K + (size_t)SEQ * DM * 2;
constexpr size_t WS_AH = WS_V + (size_t)SEQ * DM * 2;
constexpr size_t WS_PART = WS_AH + (size_t)SEQ * (DM + FF) * 2;
constexpr size_t WS_FLAG = WS_PART + (size_t)128 * 65536;
constexpr int N_FLAG_WORDS = 256 * 16;

DI unsigned cvtpk(float lo, float hi) { f32x2 v = {lo, hi}; bf16x2_t b = __builtin_convertvector(v, bf16x2_t); return __builtin_bit_cast(unsigned, b); }
DI float bf_lo(unsigned u) { return __uint_as_float(u << 16); }
DI float bf_hi(unsigned u) { return __uint_as_float(u & 0xffff0000u); }
DI float rbf(float f) { return __uint_as_float(cvtpk(f, f) << 16); }
DI s16x4 trd(const LAS char* p) { return __builtin_amdgcn_ds_read_tr16_b64_v4i16((LAS s16x4*)p); }
DI bf16x8 cat8(s16x4 a, s16x4 b) { return __builtin_shufflevector(a, b, 0, 1, 2, 3, 4, 5, 6, 7); }
template <int OFF> DI void glds(const char* base, unsigned voff, unsigned dst) {
    asm volatile("s_mov_b32 m0, %2\n\ts_nop 0\n\tglobal_load_lds_dwordx4 %0, %1 offset:%3" :: "v"(voff), "s"(base), "s"(dst), "i"(OFF) : "memory"); }
DI void glds_pair(const char* base, unsigned voff, unsigned dst) {
    asm volatile("s_mov_b32 m0, %2\n\ts_nop 0\n\tglobal_load_lds_dwordx4 %0, %1\n\tglobal_load_lds_dwordx4 %0, %1 offset:1024" :: "v"(voff), "s"(base), "s"(dst) : "memory"); }
#define WAIT_V(n) asm volatile("s_waitcnt vmcnt(" #n ")" ::: "memory")
#define WAIT_L(n) asm volatile("s_waitcnt lgkmcnt(" #n ")" ::: "memory")
#define BAR __builtin_amdgcn_s_barrier()
#define SCHED __builtin_amdgcn_sched_barrier(0)
#define MFMA16(a, b, c) __builtin_amdgcn_mfma_f32_16x16x32_bf16(a, b, c, 0, 0, 0)
#define MFMA32(a, b, c) __builtin_amdgcn_mfma_f32_32x32x16_bf16(a, b, c, 0, 0, 0)

__global__ __launch_bounds__(512) void k_ln(const u16* __restrict__ x, char* __restrict__ ws) {
    __shared__ float red[2][8][8];
    const int tid = threadIdx.x, w = tid >> 6, l = tid & 63, r = l & 7, g = (l >> 3) & 3, kbl = l >> 5;
    const int row = blockIdx.x * 8 + r;
    if (blockIdx.x == 0) { unsigned* fl = (unsigned*)(ws + WS_FLAG); for (int i = tid; i < N_FLAG_WORDS; i += 512) fl[i] = 0u; }
    u32x2 v[2][2];
#pragma unroll
    for (int c = 0; c < 2; ++c) { const u16* p = x + (size_t)row * DM + (4 * w + 2 * c + kbl) * 32 + 4 * g;
        v[c][0] = *(const u32x2*)p; v[c][1] = *(const u32x2*)(p + 16); }
    float f[16];
#pragma unroll
    for (int c = 0; c < 2; ++c)
#pragma unroll
        for (int h = 0; h < 2; ++h) { f[c * 8 + h * 4 + 0] = bf_lo(v[c][h][0]); f[c * 8 + h * 4 + 1] = bf_hi(v[c][h][0]); f[c * 8 + h * 4 + 2] = bf_lo(v[c][h][1]); f[c * 8 + h * 4 + 3] = bf_hi(v[c][h][1]); }
    float s = 0.f;
#pragma unroll
    for (int i = 0; i < 16; ++i) s += f[i];
    s += __shfl_xor(s, 8); s += __shfl_xor(s, 16); s += __shfl_xor(s, 32);
    if (l < 8) red[0][w][l] = s;
    __syncthreads();
    float mean = 0.f;
#pragma unroll
    for (int i = 0; i < 8; ++i) mean += red[0][i][r];
    mean *= (1.0f / DM);
    float q = 0.f;
#pragma unroll
    for (int i = 0; i < 16; ++i) { const float d = f[i] - mean; q += d * d; }
    q += __shfl_xor(q, 8); q += __shfl_xor(q, 16); q += __shfl_xor(q, 32);
    if (l < 8) red[1][w][l] = q;
    __syncthreads();
    float var = 0.f;
#pragma unroll
    for (int i = 0; i < 8; ++i) var += red[1][i][r];
    var *= (1.0f / DM);
    const float mb = rbf(mean), sd = rbf(sqrtf(rbf(rbf(var) + 1e-5f))), inv = 1.0f / sd;
#pragma unroll
    for (int c = 0; c < 2; ++c) { u32x4 o;
#pragma unroll
        for (int i = 0; i < 4; ++i) o[i] = cvtpk(rbf(f[c * 8 + 2 * i] - mb) * inv, rbf(f[c * 8 + 2 * i + 1] - mb) * inv);
        const int kb = 4 * w + 2 * c + kbl;
        *(u32x4*)(ws + WS_XN + ((size_t)(row >> 4) * KB_X + kb) * 1024 + (g * 16 + (row & 15)) * 16) = o; }
}

DI float silu_mul(float g, float u) { return g * u * __builtin_amdgcn_rcpf(1.0f + __builtin_amdgcn_exp2f(g * -1.4426950408889634f)); }
DI u32x4 pack8(const f32x4& a, const f32x4& b) { u32x4 o; o[0] = cvtpk(a[0], a[1]); o[1] = cvtpk(a[2], a[3]); o[2] = cvtpk(b[0], b[1]); o[3] = cvtpk(b[2], b[3]); return o; }

struct G128 { const char* a; size_t a_rb; const char* b0; const char* b1; const char* c0; const char* c1; int nswitch; unsigned ldb2; int nt; };
DI void g128_issue(LAS char* lds, const G128& g, int t, int lw, unsigned voffA, unsigned voffB) {
    const unsigned slot = (unsigned)(size_t)lds + (t & 3) * 32768;
    const char* a = g.a + (size_t)t * 2048; const size_t ko = (size_t)t * 64 * g.ldb2;
    const char* b0 = (t < g.nswitch ? g.b0 : g.c0) + ko; const char* b1 = (t < g.nswitch ? g.b1 : g.c1) + ko;
#pragma unroll
    for (int i = 0; i < 2; ++i) { const char* ar = a + (size_t)i * g.a_rb;
        glds_pair(ar, voffA, slot + (lw * 4 + i * 2) * 1024); }
#pragma unroll
    for (int i = 0; i < 2; ++i) { const size_t ro = (size_t)i * 8 * g.ldb2;
        glds<0>(b0 + ro, voffB, slot + 16384 + (lw * 2 + i) * 1024); glds<0>(b1 + ro, voffB, slot + 24576 + (lw * 2 + i) * 1024); }
}
DI void g128_loader_offs(const G128& g, int lw, int lane, unsigned& voffA, unsigned& voffB) {
    voffA = (unsigned)(lw * 2 * g.a_rb) + lane * 16;
    voffB = (unsigned)(lw * 16 + (lane >> 3)) * g.ldb2 + (((lane & 7) ^ (2 * ((lane >> 4) & 3))) * 16);
}
DI void g128_loader_loop(LAS char* lds, const G128& g, int lw, unsigned voffA, unsigned voffB) {
    WAIT_V(24); BAR;
    WAIT_V(16);
    for (int t = 0; t < g.nt; ++t) {
        BAR;
        if (t + 4 < g.nt) { g128_issue(lds, g, t + 4, lw, voffA, voffB); WAIT_V(16); }
        else if (t + 3 < g.nt) { WAIT_V(8); } else { WAIT_V(0); }
    }
}
DI void g128_compute(LAS char* lds, int nt, int cw, int lane, f32x4 (&acc)[4][4]) {
    const int wm = cw >> 1, wn = cw & 1, g = lane >> 4, q = (lane & 15) >> 2, p = lane & 3, swz = (2 * g + (q >> 1)) & 3;
    const int aoff = wm * 8192 + lane * 16;
    const int boff0 = 16384 + (4 * g + q) * 128 + (((2 * wn) ^ swz) * 32) + p * 8, boff1 = boff0 ^ 32;
    bf16x8 AX[4], BX[4], AY[4], BY[4];
#define G_RD(A_, B_, slot, kk) do { const LAS char* s_ = lds + (slot); \
        _Pragma("unroll") for (int n = 0; n < 4; ++n) { const LAS char* b_ = s_ + ((n & 1) ? boff1 : boff0) + (n >> 1) * 8192 + (kk) * 4096; B_[n] = cat8(trd(b_), trd(b_ + 2048)); } \
        _Pragma("unroll") for (int m = 0; m < 4; ++m) A_[m] = *(const LAS bf16x8*)(s_ + aoff + (m * 2 + (kk)) * 1024); } while (0)
#define G_MM(A_, B_) do { _Pragma("unroll") for (int m = 0; m < 4; ++m) _Pragma("unroll") for (int n = 0; n < 4; ++n) acc[m][n] = MFMA16(B_[n], A_[m], acc[m][n]); } while (0)
#define G_IL() do { _Pragma("unroll") for (int i_ = 0; i_ < 12; ++i_) { __builtin_amdgcn_sched_group_barrier(0x100, 1, 0); __builtin_amdgcn_sched_group_barrier(0x8, 1, 0); } __builtin_amdgcn_sched_group_barrier(0x8, 4, 0); } while (0)
#pragma unroll
    for (int m = 0; m < 4; ++m)
#pragma unroll
        for (int n = 0; n < 4; ++n) acc[m][n] = (f32x4){0.f, 0.f, 0.f, 0.f};
    BAR;
    G_RD(AX, BX, 0, 0);
    for (int t = 0; t < nt - 1; ++t) {
        G_RD(AY, BY, (t & 3) * 32768, 1); G_MM(AX, BX); G_IL();
        SCHED; WAIT_L(0); BAR; SCHED;
        G_RD(AX, BX, ((t + 1) & 3) * 32768, 0); G_MM(AY, BY); G_IL();
        SCHED;
    }
    G_RD(AY, BY, ((nt - 1) & 3) * 32768, 1); G_MM(AX, BX);
    SCHED; WAIT_L(0); BAR; SCHED;
    G_MM(AY, BY);
#undef G_RD
#undef G_MM
#undef G_IL
}

constexpr float QSCALE = 0.17677669529663687f * 1.4426950408889634f;
__global__ __launch_bounds__(512, 2) void k_gemm1(const u16* __restrict__ wqkv, const u16* __restrict__ w1, const u16* __restrict__ w3, char* __restrict__ ws) {
    extern __shared__ __attribute__((aligned(16))) char smem[];
    LAS char* lds = (LAS char*)smem;
    const int tid = threadIdx.x, wid = __builtin_amdgcn_readfirstlane(tid >> 6), lane = tid & 63, wr = wid >> 2, wc = wid & 3;
    const int xcd = blockIdx.x & 7, idx = blockIdx.x >> 3;
    const int ct = xcd * 4 + (idx >> 3), rt = idx & 7;
    const bool qkv = ct < 12;
    const unsigned ldb2 = qkv ? 3 * DM * 2 : FF * 2;
    const char* cB0 = qkv ? (const char*)wqkv + ct * 512 : (const char*)w1 + (ct - 12) * 256;
    const char* cB1 = qkv ? cB0 + 256 : (const char*)w3 + (ct - 12) * 256;
    const char* cA = ws + WS_XN + (size_t)rt * 16 * KB_X * 1024;
    const unsigned voffA = wid * (KB_X * 1024) + lane * 16;
    const unsigned voffB = (unsigned)(wid * 8 + (lane >> 3)) * ldb2 + (((lane & 7) ^ (2 * ((lane >> 4) & 3))) * 16);
    const size_t kstepB = (size_t)64 * ldb2; const unsigned lds0 = (unsigned)(size_t)lds;
    f32x4 acc[2][2][4][2];
    {
        const int g = lane >> 4, q = (lane & 15) >> 2, p = lane & 3, swz = (2 * g + (q >> 1)) & 3;
        const int aoff = wr * 8192 + lane * 16;
        const int boff0 = 65536 + (wc >> 1) * 8192 + (4 * g + q) * 128 + (((2 * (wc & 1)) ^ swz) * 32) + p * 8, boff1 = boff0 ^ 32;
#define SA(b, h) (((b) * 2 + (h)) * 16384)
#define SB(b, h) (((b) * 2 + (h)) * 16384)
#define STA(b, h, kt) do { const char* s_ = cA + (size_t)(h) * 8 * KB_X * 1024 + (size_t)(kt) * 2048; \
        glds_pair(s_, voffA, lds0 + SA(b, h) + wid * 2048); } while (0)
#define STB(b, h, kt) do { const char* s_ = ((h) ? cB1 : cB0) + (size_t)(kt) * kstepB; \
        glds<0>(s_, voffB, lds0 + 65536 + SB(b, h) + wid * 1024); glds<128>(s_, voffB, lds0 + 65536 + SB(b, h) + 8192 - 128 + wid * 1024); } while (0)
#define LDA(dst, b, h) do { _Pragma("unroll") for (int m = 0; m < 4; ++m) _Pragma("unroll") for (int k = 0; k < 2; ++k) \
        dst[m][k] = *(const LAS bf16x8*)(lds + aoff + SA(b, h) + (m * 2 + k) * 1024); } while (0)
#define LDB(dst, b, h) do { _Pragma("unroll") for (int n = 0; n < 2; ++n) _Pragma("unroll") for (int k = 0; k < 2; ++k) { \
        const LAS char* b_ = lds + (n ? boff1 : boff0) + SB(b, h) + k * 4096; dst[n][k] = cat8(trd(b_), trd(b_ + 2048)); } } while (0)
#define MMA(ai, bj, At, Bt) do { __builtin_amdgcn_s_setprio(1); _Pragma("unroll") for (int m = 0; m < 4; ++m) _Pragma("unroll") for (int n = 0; n < 2; ++n) _Pragma("unroll") for (int k = 0; k < 2; ++k) \
        acc[ai][bj][m][n] = MFMA16(Bt[n][k], At[m][k], acc[ai][bj][m][n]); __builtin_amdgcn_s_setprio(0); } while (0)
#pragma unroll
        for (int a = 0; a < 2; ++a)
#pragma unroll
            for (int b = 0; b < 2; ++b)
#pragma unroll
                for (int m = 0; m < 4; ++m)
#pragma unroll
                    for (int n = 0; n < 2; ++n) acc[a][b][m][n] = (f32x4){0.f, 0.f, 0.f, 0.f};
        bf16x8 At[4][2], B0[2][2], B1[2][2];
        constexpr int nt = DM / 64;
        STB(0, 0, 0); STB(0, 1, 0); STA(0, 0, 0); STA(0, 1, 0);
        if (wr == 1) BAR;
        WAIT_V(2); BAR;
        STB(1, 0, 1); STA(1, 0, 1); STB(1, 1, 1);
        WAIT_V(6); BAR;
        for (int t = 0; t < nt; t += 2) {
            const int t2 = (t + 2 < nt) ? t + 2 : 0, t3 = t2 + 1;
            LDB(B0, 0, 0); LDB(B1, 0, 1); SCHED; LDA(At, 0, 0); STA(1, 1, t + 1);
            WAIT_V(8); WAIT_L(0); BAR; MMA(0, 0, At, B0); MMA(0, 1, At, B1); BAR; SCHED;
            LDA(At, 0, 1); STB(0, 0, t2); STB(0, 1, t2); STA(0, 0, t2);
            WAIT_V(8); WAIT_L(0); BAR; MMA(1, 0, At, B0); MMA(1, 1, At, B1); BAR; SCHED;
            LDB(B0, 1, 0); LDB(B1, 1, 1); SCHED; LDA(At, 1, 0); STA(0, 1, t2);
            WAIT_V(8); WAIT_L(0); BAR; MMA(0, 0, At, B0); MMA(0, 1, At, B1); BAR; SCHED;
            LDA(At, 1, 1); STB(1, 0, t3); STB(1, 1, t3); STA(1, 0, t3);
            WAIT_V(8); WAIT_L(0); BAR; MMA(1, 0, At, B0); MMA(1, 1, At, B1); BAR; SCHED;
        }
        WAIT_V(0);
        if (wr == 0) BAR;
        BAR;
#undef SA
#undef SB
#undef STA
#undef STB
#undef LDA
#undef LDB
#undef MMA
    }
    int tz = threadIdx.x; asm volatile("" : "+v"(tz));
    const int ln = tz & 63, fr = ln & 15, fq = ln >> 4;
    const int cb2 = xcd * 2 + (idx >> 4), rb2 = idx & 15;
    G128 g2; g2.a = ws + WS_XN + (size_t)rb2 * 8 * KB_X * 1024; g2.a_rb = KB_X * 1024;
    g2.b0 = g2.c0 = (const char*)w1 + (2560 + cb2 * 64) * 2; g2.b1 = g2.c1 = (const char*)w3 + (2560 + cb2 * 64) * 2; g2.nswitch = 0; g2.ldb2 = FF * 2; g2.nt = DM / 64;
    unsigned vA2 = 0, vB2 = 0;
    if (wid >= 4) { g128_loader_offs(g2, wid - 4, ln, vA2, vB2);
#pragma unroll
        for (int t = 0; t < 4; ++t) g128_issue(lds, g2, t, wid - 4, vA2, vB2); }
    if (qkv) {
        const int kind = ct >> 2;
        const float sc = kind == 0 ? QSCALE : 1.0f;
        char* base = ws + WS_Q + (size_t)kind * (SEQ * DM * 2) + (size_t)((ct & 3) * 8 + wc) * 131072 + (rt * 256 + wr * 64) * 64;
        const unsigned lo0 = kind == 2 ? fr * 64 + fq * 16 : fr * 16 + fq * 512, lo1 = lo0 + (kind == 2 ? 1024 : 256);
#pragma unroll
        for (int bj = 0; bj < 2; ++bj)
#pragma unroll
            for (int ai = 0; ai < 2; ++ai)
#pragma unroll
                for (int m = 0; m < 4; ++m)
                    *(u32x4*)(base + bj * 4 * 131072 + ai * 8192 + (m >> 1) * 2048 + ((m & 1) ? lo1 : lo0)) = pack8(acc[ai][bj][m][0] * sc, acc[ai][bj][m][1] * sc);
    } else {
        const int kb = KB_X + (ct - 12) * 4 + wc;
#pragma unroll
        for (int ai = 0; ai < 2; ++ai)
#pragma unroll
            for (int m = 0; m < 4; ++m) { const int rb = rt * 16 + ai * 8 + wr * 4 + m; f32x4 h0, h1;
#pragma unroll
                for (int j = 0; j < 4; ++j) { h0[j] = silu_mul(acc[ai][0][m][0][j], acc[ai][1][m][0][j]); h1[j] = silu_mul(acc[ai][0][m][1][j], acc[ai][1][m][1][j]); }
                *(u32x4*)(ws + WS_AH + ((size_t)rb * KB_AH + kb) * 1024 + ln * 16) = pack8(h0, h1); }
    }
    if (wid >= 4) { g128_loader_loop(lds, g2, wid - 4, vA2, vB2); }
    else {
        f32x4 c2[4][4];
        g128_compute(lds, g2.nt, wid, ln, c2);
        const int kb = KB_X + (2560 + cb2 * 64) / 32 + (wid & 1);
#pragma unroll
        for (int m = 0; m < 4; ++m) { const int rb = rb2 * 8 + (wid >> 1) * 4 + m; f32x4 h0, h1;
#pragma unroll
            for (int j = 0; j < 4; ++j) { h0[j] = silu_mul(c2[m][0][j], c2[m][2][j]); h1[j] = silu_mul(c2[m][1][j], c2[m][3][j]); }
            *(u32x4*)(ws + WS_AH + ((size_t)rb * KB_AH + kb) * 1024 + ln * 16) = pack8(h0, h1); }
    }
}

constexpr int AT_STAGE = 32768, AT_NS = 3, AT_LDS = AT_STAGE * AT_NS;
__global__ __launch_bounds__(1024) void k_attn(char* __restrict__ ws) {
    extern __shared__ __attribute__((aligned(16))) char smem[];
    LAS char* lds = (LAS char*)smem;
    const int tid = threadIdx.x, wid = __builtin_amdgcn_readfirstlane(tid >> 6), lane = tid & 63, qi = wid & 7, half = wid >> 3, hi = lane >> 5;
    const int xcd = blockIdx.x & 7, idx = blockIdx.x >> 3, head = xcd * 4 + (idx >> 3), qb = idx & 7;
    const char* Kg = ws + WS_K + (size_t)head * 131072; const char* Vg = ws + WS_V + (size_t)head * 131072;
    const unsigned soff = half * 65536 + qi * 1024 + lane * 16;
    const unsigned sdst = (unsigned)(size_t)lds + half * 16384 + qi * 1024;
#define AT_ISSUE(s) do { glds<0>(Kg + (s) * 8192, soff, sdst + ((s) % AT_NS) * AT_STAGE); glds<0>(Vg + (s) * 8192, soff, sdst + ((s) % AT_NS) * AT_STAGE + 8192); } while (0)
    const char* Qg = ws + WS_Q + ((size_t)head * 64 + qb * 8 + qi) * 2048 + lane * 16;
    bf16x8 q0 = *(const bf16x8*)Qg, q1 = *(const bf16x8*)(Qg + 1024);
    AT_ISSUE(0); AT_ISSUE(1);
    bf16x8 sel; { const short one = ((lane & 15) == ((lane >> 4) & 1)) ? (short)0x3f80 : (short)0;
#pragma unroll
        for (int i = 0; i < 8; ++i) sel[i] = one; }
    const int koff = half * 16384 + lane * 16;
    const int voff = half * 16384 + 8192 + (4 * hi + ((lane & 15) >> 2)) * 64 + ((lane >> 4) & 1) * 32 + (lane & 3) * 8;
    f32x16 O, negm; f32x4 L = {0.f, 0.f, 0.f, 0.f}; float mrow;
#pragma unroll
    for (int i = 0; i < 16; ++i) { O[i] = 0.f; negm[i] = 0.f; }
    asm volatile("s_waitcnt vmcnt(2)" ::: "memory"); BAR;
    {
        const bf16x8 k0 = *(const LAS bf16x8*)(lds + koff), k1 = *(const LAS bf16x8*)(lds + koff + 1024);
        f32x16 S = MFMA32(k0, q0, negm); S = MFMA32(k1, q1, S);
        float m = S[0];
#pragma unroll
        for (int i = 1; i < 16; ++i) m = fmaxf(m, S[i]);
        m = fmaxf(m, __shfl_xor(m, 32)); mrow = m;
#pragma unroll
        for (int i = 0; i < 16; ++i) negm[i] = -m;
        asm volatile("" : "+v"(negm));
    }
    for (int s = 0; s < 8; ++s) {
        if (s > 0) { if (s < 7) { WAIT_V(2); } else { WAIT_V(0); } WAIT_L(0); BAR; }
        if (s + 2 < 8) AT_ISSUE(s + 2);
        const LAS char* kp = lds + (s % AT_NS) * AT_STAGE + koff; const LAS char* vp = lds + (s % AT_NS) * AT_STAGE + voff;
        bf16x8 k0 = *(const LAS bf16x8*)(kp), k1 = *(const LAS bf16x8*)(kp + 1024);
#pragma unroll
        for (int j = 0; j < 4; ++j) {
            const bf16x8 v0 = cat8(trd(vp + j * 2048), trd(vp + j * 2048 + 512)), v1 = cat8(trd(vp + j * 2048 + 1024), trd(vp + j * 2048 + 1536));
            f32x16 S = MFMA32(k0, q0, negm); S = MFMA32(k1, q1, S);
            SCHED;
            if (j < 3) { k0 = *(const LAS bf16x8*)(kp + (j + 1) * 2048); k1 = *(const LAS bf16x8*)(kp + (j + 1) * 2048 + 1024); }
            u32x4 p0, p1;
#pragma unroll
            for (int i = 0; i < 8; ++i) S[i] = __builtin_amdgcn_exp2f(S[i]);
#pragma unroll
            for (int i = 0; i < 4; ++i) p0[i] = cvtpk(S[2 * i], S[2 * i + 1]);
            O = MFMA32(v0, __builtin_bit_cast(bf16x8, p0), O); L = MFMA16(sel, __builtin_bit_cast(bf16x8, p0), L);
#pragma unroll
            for (int i = 8; i < 16; ++i) S[i] = __builtin_amdgcn_exp2f(S[i]);
#pragma unroll
            for (int i = 0; i < 4; ++i) p1[i] = cvtpk(S[8 + 2 * i], S[9 + 2 * i]);
            O = MFMA32(v1, __builtin_bit_cast(bf16x8, p1), O); L = MFMA16(sel, __builtin_bit_cast(bf16x8, p1), L);
            SCHED;
        }
    }
    float lrow; { const float a = __shfl(L[0], lane & 15), b = __shfl(L[1], lane & 15); lrow = (lane & 16) ? b : a; }
    if (__any(!(lrow < 1.0e18f))) {
        float m = -INFINITY, l = 0.f;
#pragma unroll
        for (int i = 0; i < 16; ++i) { O[i] = 0.f; negm[i] = 0.f; }
        const u16* Vs = (const u16*)Vg;
        for (int t = 0; t < 32; ++t) { const int key0 = half * 1024 + t * 32;
            const bf16x8 k0 = *(const bf16x8*)(Kg + key0 * 64 + lane * 16), k1 = *(const bf16x8*)(Kg + key0 * 64 + 1024 + lane * 16);
            bf16x8 v0, v1;
#pragma unroll
            for (int j = 0; j < 8; ++j) { const int kk = key0 + 8 * (j >> 2) + 4 * hi + (j & 3); v0[j] = (short)Vs[kk * 32 + (lane & 31)]; v1[j] = (short)Vs[(kk + 16) * 32 + (lane & 31)]; }
            f32x16 S = MFMA32(k0, q0, negm); S = MFMA32(k1, q1, S);
            float mt = S[0];
#pragma unroll
            for (int i = 1; i < 16; ++i) mt = fmaxf(mt, S[i]);
            mt = fmaxf(mt, __shfl_xor(mt, 32));
            const float mn = fmaxf(m, mt), al = __builtin_amdgcn_exp2f(m - mn); m = mn;
            u32x4 p0, p1; float ps = 0.f;
#pragma unroll
            for (int i = 0; i < 16; ++i) { S[i] = rbf(__builtin_amdgcn_exp2f(S[i] - mn)); ps += S[i]; O[i] *= al; }
            l = l * al + ps + __shfl_xor(ps, 32);
#pragma unroll
            for (int i = 0; i < 4; ++i) { p0[i] = cvtpk(S[2 * i], S[2 * i + 1]); p1[i] = cvtpk(S[8 + 2 * i], S[9 + 2 * i]); }
            O = MFMA32(v0, __builtin_bit_cast(bf16x8, p0), O); O = MFMA32(v1, __builtin_bit_cast(bf16x8, p1), O);
        }
        mrow = m; lrow = l;
    }
    WAIT_L(0); BAR;
    LAS float* xo = (LAS float*)(lds + qi * 5120);
    if (half == 1) {
#pragma unroll
        for (int i = 0; i < 16; ++i) xo[i * 64 + lane] = O[i];
        xo[16 * 64 + lane] = mrow; xo[17 * 64 + lane] = lrow;
    }
    WAIT_L(0); BAR;
    if (half == 0) {
        const float m2 = xo[16 * 64 + lane], l2 = xo[17 * 64 + lane], mm = fmaxf(mrow, m2);
        const float a1 = __builtin_amdgcn_exp2f(mrow - mm), a2 = __builtin_amdgcn_exp2f(m2 - mm), inv = 1.0f / (lrow * a1 + l2 * a2), s1 = a1 * inv, s2 = a2 * inv;
        unsigned pk[8];
#pragma unroll
        for (int i = 0; i < 8; ++i) pk[i] = cvtpk(O[2 * i] * s1 + xo[(2 * i) * 64 + lane] * s2, O[2 * i + 1] * s1 + xo[(2 * i + 1) * 64 + lane] * s2);
        char* dst = ws + WS_AH + ((size_t)(qb * 16 + qi * 2 + ((lane >> 4) & 1)) * KB_AH + head) * 1024 + (lane & 15) * 16;
#pragma unroll
        for (int c = 0; c < 2; ++c) {
            auto r0 = __builtin_amdgcn_permlane32_swap(pk[4 * c + 0], pk[4 * c + 2], false, false);
            auto r1 = __builtin_amdgcn_permlane32_swap(pk[4 * c + 1], pk[4 * c + 3], false, false);
            *(u32x4*)(dst + (2 * c + hi) * 256) = (u32x4){r0[0], r1[0], r0[1], r1[1]};
        }
    }
#undef AT_ISSUE
}

__global__ __launch_bounds__(512, 2) void k_gemm2(const u16* __restrict__ x, const u16* __restrict__ wo, const u16* __restrict__ w2, char* __restrict__ ws, u16* __restrict__ out) {
    extern __shared__ __attribute__((aligned(16))) char smem[];
    LAS char* lds = (LAS char*)smem;
    const int tid = threadIdx.x, wid = __builtin_amdgcn_readfirstlane(tid >> 6), lane = tid & 63;
    const int xcd = blockIdx.x & 7, idx = blockIdx.x >> 3;
    const int kh = xcd & 1, rt = ((xcd >> 1) & 1) * 8 + (idx & 7), ct = (xcd >> 2) * 4 + (idx >> 3), tile = rt * 8 + ct;
    constexpr int NT = 36;
    G128 g; g.a = ws + WS_AH + ((size_t)rt * 8 * KB_AH + kh * NT * 2) * 1024; g.a_rb = KB_AH * 1024; g.ldb2 = DM * 2; g.nt = NT;
    g.b0 = (const char*)wo + ct * 256; g.b1 = g.b0 + 128; g.nswitch = kh ? 0 : 16;
    g.c0 = (const char*)w2 + ct * 256 + ((ptrdiff_t)kh * NT - 16) * 64 * (DM * 2); g.c1 = g.c0 + 128;
    GAS unsigned* ticket = (GAS unsigned*)(ws + WS_FLAG) + tile * 16; GAS unsigned* flag = ticket + 128 * 16;
    LAS unsigned* tk = (LAS unsigned*)(lds + 131072);
    f32x4 acc[4][4];
    if (wid >= 4) {
        unsigned vA, vB; g128_loader_offs(g, wid - 4, lane, vA, vB);
#pragma unroll
        for (int t = 0; t < 4; ++t) g128_issue(lds, g, t, wid - 4, vA, vB);
        g128_loader_loop(lds, g, wid - 4, vA, vB);
        if (tid == 256) { *tk = __hip_atomic_fetch_add(ticket, 1u, __ATOMIC_RELAXED, __HIP_MEMORY_SCOPE_AGENT); }
    } else g128_compute(lds, NT, wid, lane, acc);
    asm volatile("s_waitcnt vmcnt(0) lgkmcnt(0)" ::: "memory"); BAR; asm volatile("" ::: "memory");
    const unsigned first = *tk;
    const __amdgpu_buffer_rsrc_t rs = __builtin_amdgcn_make_buffer_rsrc((void*)(ws + WS_PART + (size_t)tile * 65536), 0, 65536, 0x00020000);
    if (first == 0u) {
        if (wid < 4) {
#pragma unroll
            for (int m = 0; m < 4; ++m)
#pragma unroll
                for (int n = 0; n < 4; ++n) __builtin_amdgcn_raw_buffer_store_b128(__builtin_bit_cast(u32x4, acc[m][n]), rs, ((m * 4 + n) * 256 + tid) * 16, 0, 16);
        }
        WAIT_V(0); BAR;
        if (tid == 0) __hip_atomic_store(flag, 1u, __ATOMIC_RELAXED, __HIP_MEMORY_SCOPE_AGENT);
        return;
    }
    if (wid == 4) {
        while (__builtin_amdgcn_readfirstlane(__hip_atomic_load(flag, __ATOMIC_RELAXED, __HIP_MEMORY_SCOPE_AGENT)) == 0u) __builtin_amdgcn_s_sleep(1);
        __builtin_amdgcn_fence(__ATOMIC_ACQUIRE, "agent");
        WAIT_V(0);
    }
    asm volatile("" ::: "memory"); BAR; asm volatile("" ::: "memory");
    if (wid < 4) {
        const int fr = lane & 15, fq = lane >> 4, wm = wid >> 1, wn = wid & 1;
#pragma unroll
        for (int m = 0; m < 4; ++m)
#pragma unroll
            for (int n = 0; n < 4; ++n) acc[m][n] += __builtin_bit_cast(f32x4, __builtin_amdgcn_raw_buffer_load_b128(rs, ((m * 4 + n) * 256 + tid) * 16, 0, 0));
#pragma unroll
        for (int m = 0; m < 4; ++m) { const size_t rowo = (size_t)(rt * 128 + wm * 64 + m * 16 + fr) * DM + ct * 128 + wn * 32 + (fq & 1) * 16 + (fq >> 1) * 8;
#pragma unroll
            for (int s = 0; s < 2; ++s) { const u32x4 xv = *(const u32x4*)(x + rowo + s * 64); float lo[4], hi4[4];
#pragma unroll
                for (int i = 0; i < 4; ++i) { auto r = __builtin_amdgcn_permlane16_swap(__float_as_uint(acc[m][2 * s][i]), __float_as_uint(acc[m][2 * s + 1][i]), false, false);
                    lo[i] = __uint_as_float(r[0]); hi4[i] = __uint_as_float(r[1]); }
                u32x4 o; o[0] = cvtpk(lo[0] + bf_lo(xv[0]), lo[1] + bf_hi(xv[0])); o[1] = cvtpk(lo[2] + bf_lo(xv[1]), lo[3] + bf_hi(xv[1]));
                o[2] = cvtpk(hi4[0] + bf_lo(xv[2]), hi4[1] + bf_hi(xv[2])); o[3] = cvtpk(hi4[2] + bf_lo(xv[3]), hi4[3] + bf_hi(xv[3]));
                *(u32x4*)(out + rowo + s * 64) = o; } }
    }
}

extern "C" void kernel_launch(void* const* d_in, const int* in_sizes, int n_in, void* d_out, int out_size, void* d_ws, size_t ws_size, hipStream_t stream) {
    const u16* x = (const u16*)d_in[0]; const u16* wqkv = (const u16*)d_in[1]; const u16* wo = (const u16*)d_in[2];
    const u16* w1 = (const u16*)d_in[4]; const u16* w3 = (const u16*)d_in[5]; const u16* w2 = (const u16*)d_in[6];
    char* ws = (char*)d_ws;
    (void)hipFuncSetAttribute((const void*)k_gemm1, hipFuncAttributeMaxDynamicSharedMemorySize, 131072);
    (void)hipFuncSetAttribute((const void*)k_attn, hipFuncAttributeMaxDynamicSharedMemorySize, AT_LDS);
    (void)hipFuncSetAttribute((const void*)k_gemm2, hipFuncAttributeMaxDynamicSharedMemorySize, 131072 + 16);
    k_ln<<<256, 512, 0, stream>>>(x, ws);
    k_gemm1<<<256, 512, 131072, stream>>>(wqkv, w1, w3, ws);
    k_attn<<<256, 1024, AT_LDS, stream>>>(ws);
    k_gemm2<<<256, 512, 131072 + 16, stream>>>(x, wo, w2, ws, (u16*)d_out);
}
```

```cpp
#include <hip/hip_runtime.h>
#include <hip/hip_bf16.h>
#include <stdint.h>

#define LAS __attribute__((address_space(3)))
#define GAS __attribute__((address_space(1)))
#define DI __device__ __forceinline__
typedef unsigned short u16;
typedef short bf16x8 __attribute__((ext_vector_type(8)));
typedef short s16x4 __attribute__((ext_vector_type(4)));
typedef float f32x2 __attribute__((ext_vector_type(2)));
typedef float f32x4 __attribute__((ext_vector_type(4)));
typedef float f32x16 __attribute__((ext_vector_type(16)));
typedef unsigned u32x2 __attribute__((ext_vector_type(2)));
typedef unsigned u32x4 __attribute__((ext_vector_type(4)));
typedef __bf16 bf16x2_t __attribute__((ext_vector_type(2)));

constexpr int SEQ = 2048, DM = 1024, FF = 3584, NH = 32;
constexpr int KB_X = DM / 32;
constexpr int KB_AH = (DM + FF) / 32;
constexpr size_t WS_XN = 0;
constexpr size_t WS_Q = WS_XN + (size_t)SEQ * DM * 2;
constexpr size_t WS_K = WS_Q + (size_t)SEQ * DM * 2;
constexpr size_t WS_V = WS_K + (size_t)SEQ * DM * 2;
constexpr size_t WS_AH = WS_V + (size_t)SEQ * DM * 2;
constexpr size_t WS_PART = WS_AH + (size_t)SEQ * (DM + FF) * 2;
constexpr size_t WS_FLAG = WS_PART + (size_t)128 * 65536;
constexpr int N_FLAG_WORDS = 256 * 16;

#define TS_DECL
#define TS(i)
#define TS_OUT(kid)
DI unsigned cvtpk(float lo, float hi) { f32x2 v = {lo, hi}; bf16x2_t b = __builtin_convertvector(v, bf16x2_t); return __builtin_bit_cast(unsigned, b); }
DI float bf_lo(unsigned u) { return __uint_as_float(u << 16); }
DI float bf_hi(unsigned u) { return __uint_as_float(u & 0xffff0000u); }
DI float rbf(float f) { return __uint_as_float(cvtpk(f, f) << 16); }
DI s16x4 trd(const LAS char* p) { return __builtin_amdgcn_ds_read_tr16_b64_v4i16((LAS s16x4*)p); }
DI bf16x8 cat8(s16x4 a, s16x4 b) { return __builtin_shufflevector(a, b, 0, 1, 2, 3, 4, 5, 6, 7); }
template <int OFF> DI void glds(const char* base, unsigned voff, unsigned dst) {
    asm volatile("s_mov_b32 m0, %2\n\ts_nop 0\n\tglobal_load_lds_dwordx4 %0, %1 offset:%3" :: "v"(voff), "s"(base), "s"(dst), "i"(OFF) : "memory"); }
DI void glds_pair(const char* base, unsigned voff, unsigned dst) {
    asm volatile("s_mov_b32 m0, %2\n\ts_nop 0\n\tglobal_load_lds_dwordx4 %0, %1\n\tglobal_load_lds_dwordx4 %0, %1 offset:1024" :: "v"(voff), "s"(base), "s"(dst) : "memory"); }
#define ST_MODE 0
DI void st16(void* p, u32x4 v) {
    *(u32x4*)p = v;
}
#define WAIT_V(n) asm volatile("s_waitcnt vmcnt(" #n ")" ::: "memory")
#define WAIT_L(n) asm volatile("s_waitcnt lgkmcnt(" #n ")" ::: "memory")
#define BAR __builtin_amdgcn_s_barrier()
#define SCHED __builtin_amdgcn_sched_barrier(0)
#define MFMA16(a, b, c) __builtin_amdgcn_mfma_f32_16x16x32_bf16(a, b, c, 0, 0, 0)
#define MFMA32(a, b, c) __builtin_amdgcn_mfma_f32_32x32x16_bf16(a, b, c, 0, 0, 0)

__global__ __launch_bounds__(512) void k_ln(const u16* __restrict__ x, char* __restrict__ ws) {
    __shared__ float red[2][8][8];
    const int tid = threadIdx.x, w = tid >> 6, l = tid & 63, r = l & 7, g = (l >> 3) & 3, kbl = l >> 5;
    TS_DECL; TS(0);
    const int row = blockIdx.x * 8 + r;
    if (blockIdx.x == 0) { unsigned* fl = (unsigned*)(ws + WS_FLAG); for (int i = tid; i < N_FLAG_WORDS; i += 512) fl[i] = 0u; }
    u32x2 v[2][2];
#pragma unroll
    for (int c = 0; c < 2; ++c) { const u16* p = x + (size_t)row * DM + (4 * w + 2 * c + kbl) * 32 + 4 * g;
        v[c][0] = *(const u32x2*)p; v[c][1] = *(const u32x2*)(p + 16); }
    float f[16];
#pragma unroll
    for (int c = 0; c < 2; ++c)
#pragma unroll
        for (int h = 0; h < 2; ++h) { f[c * 8 + h * 4 + 0] = bf_lo(v[c][h][0]); f[c * 8 + h * 4 + 1] = bf_hi(v[c][h][0]); f[c * 8 + h * 4 + 2] = bf_lo(v[c][h][1]); f[c * 8 + h * 4 + 3] = bf_hi(v[c][h][1]); }
    float s = 0.f;
#pragma unroll
    for (int i = 0; i < 16; ++i) s += f[i];
    TS(1);
    s += __shfl_xor(s, 8); s += __shfl_xor(s, 16); s += __shfl_xor(s, 32);
    if (l < 8) red[0][w][l] = s;
    __syncthreads();
    float mean = 0.f;
#pragma unroll
    for (int i = 0; i < 8; ++i) mean += red[0][i][r];
    mean *= (1.0f / DM);
    float q = 0.f;
#pragma unroll
    for (int i = 0; i < 16; ++i) { const float d = f[i] - mean; q += d * d; }
    q += __shfl_xor(q, 8); q += __shfl_xor(q, 16); q += __shfl_xor(q, 32);
    if (l < 8) red[1][w][l] = q;
    __syncthreads();
    float var = 0.f;
#pragma unroll
    for (int i = 0; i < 8; ++i) var += red[1][i][r];
    var *= (1.0f / DM);
    const float mb = rbf(mean), sd = rbf(sqrtf(rbf(rbf(var) + 1e-5f))), inv = 1.0f / sd;
#pragma unroll
    for (int c = 0; c < 2; ++c) { u32x4 o;
#pragma unroll
        for (int i = 0; i < 4; ++i) o[i] = cvtpk(rbf(f[c * 8 + 2 * i] - mb) * inv, rbf(f[c * 8 + 2 * i + 1] - mb) * inv);
        const int kb = 4 * w + 2 * c + kbl;
        *(u32x4*)(ws + WS_XN + ((size_t)(row >> 4) * KB_X + kb) * 1024 + (g * 16 + (row & 15)) * 16) = o; }
    TS(2); TS_OUT(0);
}

DI float silu_mul(float g, float u) { return g * u * __builtin_amdgcn_rcpf(1.0f + __builtin_amdgcn_exp2f(g * -1.4426950408889634f)); }
DI u32x4 pack8(const f32x4& a, const f32x4& b) { u32x4 o; o[0] = cvtpk(a[0], a[1]); o[1] = cvtpk(a[2], a[3]); o[2] = cvtpk(b[0], b[1]); o[3] = cvtpk(b[2], b[3]); return o; }

struct G128 { const char* a; size_t a_rb; const char* b0; const char* b1; const char* c0; const char* c1; int nswitch; unsigned ldb2; int nt; };
DI void g128_issue(LAS char* lds, const G128& g, int t, int lw, unsigned voffA, unsigned voffB) {
    const unsigned slot = (unsigned)(size_t)lds + (t & 3) * 32768;
    const char* a = g.a + (size_t)t * 2048; const size_t ko = (size_t)t * 64 * g.ldb2;
    const char* b0 = (t < g.nswitch ? g.b0 : g.c0) + ko; const char* b1 = (t < g.nswitch ? g.b1 : g.c1) + ko;
#pragma unroll
    for (int i = 0; i < 2; ++i) { const char* ar = a + (size_t)i * g.a_rb;
        glds_pair(ar, voffA, slot + (lw * 4 + i * 2) * 1024); }
#pragma unroll
    for (int i = 0; i < 2; ++i) { const size_t ro = (size_t)i * 8 * g.ldb2;
        glds<0>(b0 + ro, voffB, slot + 16384 + (lw * 2 + i) * 1024); glds<0>(b1 + ro, voffB, slot + 24576 + (lw * 2 + i) * 1024); }
}
DI void g128_loader_offs(const G128& g, int lw, int lane, unsigned& voffA, unsigned& voffB) {
    voffA = (unsigned)(lw * 2 * g.a_rb) + lane * 16;
    voffB = (unsigned)(lw * 16 + (lane >> 3)) * g.ldb2 + (((lane & 7) ^ (2 * ((lane >> 4) & 3))) * 16);
}
DI void g128_loader_loop(LAS char* lds, const G128& g, int lw, unsigned voffA, unsigned voffB) {
    WAIT_V(24); BAR;
    WAIT_V(16);
    for (int t = 0; t < g.nt; ++t) {
        BAR;
        if (t + 4 < g.nt) { g128_issue(lds, g, t + 4, lw, voffA, voffB); WAIT_V(16); }
        else if (t + 3 < g.nt) { WAIT_V(8); } else { WAIT_V(0); }
    }
}
DI void g128_compute(LAS char* lds, int nt, int cw, int lane, f32x4 (&acc)[4][4]) {
    const int wm = cw >> 1, wn = cw & 1, g = lane >> 4, q = (lane & 15) >> 2, p = lane & 3, swz = (2 * g + (q >> 1)) & 3;
    const int aoff = wm * 8192 + lane * 16;
    const int boff0 = 16384 + (4 * g + q) * 128 + (((2 * wn) ^ swz) * 32) + p * 8, boff1 = boff0 ^ 32;
    bf16x8 AX[4], BX[4], AY[4], BY[4];
#define G_RD(A_, B_, slot, kk) do { const LAS char* s_ = lds + (slot); \
        _Pragma("unroll") for (int n = 0; n < 4; ++n) { const LAS char* b_ = s_ + ((n & 1) ? boff1 : boff0) + (n >> 1) * 8192 + (kk) * 4096; B_[n] = cat8(trd(b_), trd(b_ + 2048)); } \
        _Pragma("unroll") for (int m = 0; m < 4; ++m) A_[m] = *(const LAS bf16x8*)(s_ + aoff + (m * 2 + (kk)) * 1024); } while (0)
#define G_MM(A_, B_) do { _Pragma("unroll") for (int m = 0; m < 4; ++m) _Pragma("unroll") for (int n = 0; n < 4; ++n) acc[m][n] = MFMA16(B_[n], A_[m], acc[m][n]); } while (0)
#define G_IL() do { _Pragma("unroll") for (int i_ = 0; i_ < 12; ++i_) { __builtin_amdgcn_sched_group_barrier(0x100, 1, 0); __builtin_amdgcn_sched_group_barrier(0x8, 1, 0); } __builtin_amdgcn_sched_group_barrier(0x8, 4, 0); } while (0)
#pragma unroll
    for (int m = 0; m < 4; ++m)
#pragma unroll
        for (int n = 0; n < 4; ++n) acc[m][n] = (f32x4){0.f, 0.f, 0.f, 0.f};
    BAR;
    G_RD(AX, BX, 0, 0);
    for (int t = 0; t < nt - 1; ++t) {
        G_RD(AY, BY, (t & 3) * 32768, 1); G_MM(AX, BX); G_IL();
        SCHED; WAIT_L(0); BAR; SCHED;
        G_RD(AX, BX, ((t + 1) & 3) * 32768, 0); G_MM(AY, BY); G_IL();
        SCHED;
    }
    G_RD(AY, BY, ((nt - 1) & 3) * 32768, 1); G_MM(AX, BX);
    SCHED; WAIT_L(0); BAR; SCHED;
    G_MM(AY, BY);
#undef G_RD
#undef G_MM
#undef G_IL
}

constexpr float QSCALE = 0.17677669529663687f * 1.4426950408889634f;
constexpr int G1_LDS = 131072 + 16384;
__global__ __launch_bounds__(512, 2) void k_gemm1(const u16* __restrict__ wqkv, const u16* __restrict__ w1, const u16* __restrict__ w3, char* __restrict__ ws) {
    extern __shared__ __attribute__((aligned(16))) char smem[];
    LAS char* lds = (LAS char*)smem;
    const int tid = threadIdx.x, wid = __builtin_amdgcn_readfirstlane(tid >> 6), lane = tid & 63, wr = wid >> 2, wc = wid & 3;
    const int xcd = blockIdx.x & 7, idx = blockIdx.x >> 3;
    TS_DECL; TS(0);
    const int ct = xcd * 4 + (idx >> 3), rt = idx & 7;
    const bool qkv = ct < 12;
    const unsigned ldb2 = qkv ? 3 * DM * 2 : FF * 2;
    const char* cB0 = qkv ? (const char*)wqkv + ct * 512 : (const char*)w1 + (ct - 12) * 256;
    const char* cB1 = qkv ? cB0 + 256 : (const char*)w3 + (ct - 12) * 256;
    const char* cA = ws + WS_XN + (size_t)rt * 16 * KB_X * 1024;
    const unsigned voffA = wid * (KB_X * 1024) + lane * 16;
    const unsigned voffB = (unsigned)(wid * 8 + (lane >> 3)) * ldb2 + (((lane & 7) ^ (2 * ((lane >> 4) & 3))) * 16);
    const char* pE = ((lane & 1) ? (const char*)w3 : (const char*)w1) + (size_t)(wid * 8 + (lane >> 3)) * (FF * 2) + (2560 + ct * 32) * 2 + ((((lane >> 1) & 3) ^ ((lane >> 4) & 3)) * 16);
    const size_t kstepB = (size_t)64 * ldb2; const unsigned lds0 = (unsigned)(size_t)lds;
    f32x4 acc[2][2][4][2], ace[2][4];
    {
        const int g = lane >> 4, q = (lane & 15) >> 2, p = lane & 3, swz = (2 * g + (q >> 1)) & 3;
        const int aoff = wr * 8192 + lane * 16;
        const int boff0 = 65536 + (wc >> 1) * 8192 + (4 * g + q) * 128 + (((2 * (wc & 1)) ^ swz) * 32) + p * 8, boff1 = boff0 ^ 32;
        const int eoff = 131072 + (4 * g + q) * 128 + ((wc ^ swz) * 32) + p * 8;
#define SA(b, h) (((b) * 2 + (h)) * 16384)
#define SB(b, h) (((b) * 2 + (h)) * 16384)
#define STA(b, h, kt) do { const char* s_ = cA + (size_t)(h) * 8 * KB_X * 1024 + (size_t)(kt) * 2048; \
        glds_pair(s_, voffA, lds0 + SA(b, h) + wid * 2048); } while (0)
#define STB(b, h, kt) do { const char* s_ = ((h) ? cB1 : cB0) + (size_t)(kt) * kstepB; \
        glds<0>(s_, voffB, lds0 + 65536 + SB(b, h) + wid * 1024); glds<128>(s_, voffB, lds0 + 65536 + SB(b, h) + 8192 - 128 + wid * 1024); } while (0)
#define STE(b, kt) do { const char* s_ = pE + (size_t)(kt) * (64 * FF * 2); \
        asm volatile("s_mov_b32 m0, %1\n\ts_nop 0\n\tglobal_load_lds_dwordx4 %0, off" :: "v"(s_), "s"(lds0 + 131072 + (b) * 8192 + wid * 1024) : "memory"); } while (0)
#define LDA(dst, b, h) do { _Pragma("unroll") for (int m = 0; m < 4; ++m) _Pragma("unroll") for (int k = 0; k < 2; ++k) \
        dst[m][k] = *(const LAS bf16x8*)(lds + aoff + SA(b, h) + (m * 2 + k) * 1024); } while (0)
#define LDB(dst, b, h) do { _Pragma("unroll") for (int n = 0; n < 2; ++n) _Pragma("unroll") for (int k = 0; k < 2; ++k) { \
        const LAS char* b_ = lds + (n ? boff1 : boff0) + SB(b, h) + k * 4096; dst[n][k] = cat8(trd(b_), trd(b_ + 2048)); } } while (0)
#define LDE(dst, b) do { _Pragma("unroll") for (int k = 0; k < 2; ++k) { const LAS char* b_ = lds + eoff + (b) * 8192 + k * 4096; dst[k] = cat8(trd(b_), trd(b_ + 2048)); } } while (0)
#define MMA(ai) do { __builtin_amdgcn_s_setprio(1); \
        _Pragma("unroll") for (int k = 0; k < 2; ++k) _Pragma("unroll") for (int m = 0; m < 4; ++m) { \
            acc[ai][0][m][0] = MFMA16(B0[0][k], At[m][k], acc[ai][0][m][0]); acc[ai][0][m][1] = MFMA16(B0[1][k], At[m][k], acc[ai][0][m][1]); \
            acc[ai][1][m][0] = MFMA16(B1[0][k], At[m][k], acc[ai][1][m][0]); acc[ai][1][m][1] = MFMA16(B1[1][k], At[m][k], acc[ai][1][m][1]); \
            ace[ai][m] = MFMA16(BE[k], At[m][k], ace[ai][m]); } \
        __builtin_amdgcn_s_setprio(0); } while (0)
#pragma unroll
        for (int a = 0; a < 2; ++a)
#pragma unroll
            for (int m = 0; m < 4; ++m) { ace[a][m] = (f32x4){0.f, 0.f, 0.f, 0.f};
#pragma unroll
                for (int b = 0; b < 2; ++b)
#pragma unroll
                    for (int n = 0; n < 2; ++n) acc[a][b][m][n] = (f32x4){0.f, 0.f, 0.f, 0.f}; }
        bf16x8 At[4][2], B0[2][2], B1[2][2], BE[2];
        constexpr int nt = DM / 64;
        STB(0, 0, 0); STB(0, 1, 0); STE(0, 0); STA(0, 0, 0); STA(0, 1, 0);
        if (wr == 1) BAR;
        WAIT_V(2); BAR;
        STB(1, 0, 1); STB(1, 1, 1); STE(1, 1); STA(1, 0, 1);
        WAIT_V(7); BAR;
        TS(1);
        for (int t = 0; t < nt - 2; t += 2) {
            LDB(B0, 0, 0); LDB(B1, 0, 1); LDE(BE, 0); SCHED; LDA(At, 0, 0); STA(1, 1, t + 1);
            WAIT_V(9); WAIT_L(0); BAR; MMA(0); BAR; SCHED;
            LDA(At, 0, 1); STB(0, 0, t + 2); STB(0, 1, t + 2); STE(0, t + 2); STA(0, 0, t + 2);
            WAIT_V(9); WAIT_L(0); BAR; MMA(1); BAR; SCHED;
            LDB(B0, 1, 0); LDB(B1, 1, 1); LDE(BE, 1); SCHED; LDA(At, 1, 0); STA(0, 1, t + 2);
            WAIT_V(9); WAIT_L(0); BAR; MMA(0); BAR; SCHED;
            LDA(At, 1, 1); STB(1, 0, t + 3); STB(1, 1, t + 3); STE(1, t + 3); STA(1, 0, t + 3);
            WAIT_V(9); WAIT_L(0); BAR; MMA(1); BAR; SCHED;
        }
        LDB(B0, 0, 0); LDB(B1, 0, 1); LDE(BE, 0); SCHED; LDA(At, 0, 0); STA(1, 1, nt - 1);
        WAIT_V(9); WAIT_L(0); BAR; MMA(0); BAR; SCHED;
        LDA(At, 0, 1);
        WAIT_V(2); WAIT_L(0); BAR; MMA(1); BAR; SCHED;
        LDB(B0, 1, 0); LDB(B1, 1, 1); LDE(BE, 1); SCHED; LDA(At, 1, 0);
        WAIT_V(0); WAIT_L(0); BAR; MMA(0); BAR; SCHED;
        LDA(At, 1, 1);
        WAIT_L(0); BAR; MMA(1);
        if (wr == 0) BAR;
        TS(2);
#undef SA
#undef SB
#undef STA
#undef STB
#undef STE
#undef LDA
#undef LDB
#undef LDE
#undef MMA
    }
    int tz = threadIdx.x; asm volatile("" : "+v"(tz));
    const int ln = tz & 63, fr = ln & 15, fq = ln >> 4;
    if (qkv) {
        const int kind = ct >> 2;
        const float sc = kind == 0 ? QSCALE : 1.0f;
        char* base = ws + WS_Q + (size_t)kind * (SEQ * DM * 2) + (size_t)((ct & 3) * 8 + wc) * 131072 + (rt * 256 + wr * 64) * 64;
        const unsigned lo0 = kind == 2 ? fr * 64 + fq * 16 : fr * 16 + fq * 512, lo1 = lo0 + (kind == 2 ? 1024 : 256);
#pragma unroll
        for (int bj = 0; bj < 2; ++bj)
#pragma unroll
            for (int ai = 0; ai < 2; ++ai)
#pragma unroll
                for (int m = 0; m < 4; ++m)
                    st16(base + bj * 4 * 131072 + ai * 8192 + (m >> 1) * 2048 + ((m & 1) ? lo1 : lo0), pack8(acc[ai][bj][m][0] * sc, acc[ai][bj][m][1] * sc));
    } else {
        const int kb = KB_X + (ct - 12) * 4 + wc;
#pragma unroll
        for (int ai = 0; ai < 2; ++ai)
#pragma unroll
            for (int m = 0; m < 4; ++m) { const int rb = rt * 16 + ai * 8 + wr * 4 + m; f32x4 h0, h1;
#pragma unroll
                for (int j = 0; j < 4; ++j) { h0[j] = silu_mul(acc[ai][0][m][0][j], acc[ai][1][m][0][j]); h1[j] = silu_mul(acc[ai][0][m][1][j], acc[ai][1][m][1][j]); }
                st16(ws + WS_AH + ((size_t)rb * KB_AH + kb) * 1024 + ln * 16, pack8(h0, h1)); }
    }
    {
        const int cq = 2 * wc + (fq & 1);
        char* base = ws + WS_AH + ((size_t)(rt * 16 + wr * 4 + (ln >> 5)) * KB_AH + KB_X + 80 + ct) * 1024 + ((cq & 3) * 16 + fr) * 16 + (cq >> 2) * 8;
#pragma unroll
        for (int ai = 0; ai < 2; ++ai)
#pragma unroll
            for (int mp = 0; mp < 2; ++mp) { float h[4];
#pragma unroll
                for (int j = 0; j < 4; ++j) { auto r = __builtin_amdgcn_permlane32_swap(__float_as_uint(ace[ai][2 * mp][j]), __float_as_uint(ace[ai][2 * mp + 1][j]), false, false);
                    h[j] = silu_mul(__uint_as_float(r[0]), __uint_as_float(r[1])); }
                *(u32x2*)(base + (size_t)(ai * 8 + mp * 2) * KB_AH * 1024) = (u32x2){cvtpk(h[0], h[1]), cvtpk(h[2], h[3])}; }
    }
    TS(3); TS_OUT(1);
}

constexpr int AT_STAGE = 32768, AT_NS = 3, AT_LDS = AT_STAGE * AT_NS + 4096;
#define PIN(x) asm volatile("" : "+v"(x))
__global__ __launch_bounds__(512, 2) void k_attn(char* __restrict__ ws, const u16* __restrict__ wo, const u16* __restrict__ w2) {
    extern __shared__ __attribute__((aligned(16))) char smem[];
    LAS char* lds = (LAS char*)smem;
    const int tid = threadIdx.x, wid = __builtin_amdgcn_readfirstlane(tid >> 6), lane = tid & 63, hi = lane >> 5;
    const int xcd = blockIdx.x & 7, idx = blockIdx.x >> 3, head = xcd * 4 + (idx >> 3), qb = idx & 7;
    TS_DECL; TS(0);
    const char* Kg = ws + WS_K + (size_t)head * 131072; const char* Vg = ws + WS_V + (size_t)head * 131072;
    const unsigned soff = wid * 2048 + lane * 16;
    const unsigned sdst = (unsigned)(size_t)lds + wid * 2048;
#define AT_ISSUE(s, slot) do { glds_pair(Kg + (s) * 16384, soff, sdst + (slot)); glds_pair(Vg + (s) * 16384, soff, sdst + (slot) + 16384); } while (0)
    const char* Qg = ws + WS_Q + ((size_t)head * 64 + qb * 8 + wid) * 2048 + lane * 16;
    bf16x8 q0 = *(const bf16x8*)Qg, q1 = *(const bf16x8*)(Qg + 1024);
    AT_ISSUE(0, 0); AT_ISSUE(1, AT_STAGE);
    bf16x8 sel; { const short one = ((lane & 15) == ((lane >> 4) & 1)) ? (short)0x3f80 : (short)0;
#pragma unroll
        for (int i = 0; i < 8; ++i) sel[i] = one; }
    const LAS char* kb0 = lds + lane * 16;
    const LAS char* vb0 = lds + 16384 + (4 * hi + ((lane & 15) >> 2)) * 64 + ((lane >> 4) & 1) * 32 + (lane & 3) * 8;
    f32x16 O, negm, SA, SB; f32x4 L = {0.f, 0.f, 0.f, 0.f}; float mrow;
    bf16x8 k0, k1, v0, v1;
#pragma unroll
    for (int i = 0; i < 16; ++i) { O[i] = 0.f; negm[i] = 0.f; }
    WAIT_V(4); BAR;
    TS(1);
    {
        k0 = *(const LAS bf16x8*)(kb0); k1 = *(const LAS bf16x8*)(kb0 + 1024);
        SA = MFMA32(k0, q0, negm); SA = MFMA32(k1, q1, SA);
        k0 = *(const LAS bf16x8*)(kb0 + 2048); k1 = *(const LAS bf16x8*)(kb0 + 3072);
        v0 = cat8(trd(vb0), trd(vb0 + 512)); v1 = cat8(trd(vb0 + 1024), trd(vb0 + 1536));
        float m = SA[0];
#pragma unroll
        for (int i = 1; i < 16; ++i) m = fmaxf(m, SA[i]);
        m = fmaxf(m, __shfl_xor(m, 32)); mrow = m;
#pragma unroll
        for (int i = 0; i < 16; ++i) { negm[i] = -m; SA[i] -= m; }
        PIN(negm);
    }
    TS(2);
#define EX(S, i) S[i] = __builtin_amdgcn_exp2f(S[i])
#define BC8(x) __builtin_bit_cast(bf16x8, x)
#define AT_STEP(SC, SN, kp, v1p, v0p) do { \
        O = MFMA32(v1, BC8(p1), O); SCHED; v1 = cat8(trd(v1p), trd((v1p) + 512)); EX(SC, 0); EX(SC, 1); EX(SC, 2); PIN(SC); SCHED; \
        SN = MFMA32(k0, q0, negm); SCHED; k0 = *(const LAS bf16x8*)(kp); EX(SC, 3); EX(SC, 4); EX(SC, 5); PIN(SC); SCHED; \
        L = MFMA16(sel, BC8(p1), L); SCHED; EX(SC, 6); EX(SC, 7); \
        p0[0] = cvtpk(SC[0], SC[1]); p0[1] = cvtpk(SC[2], SC[3]); p0[2] = cvtpk(SC[4], SC[5]); p0[3] = cvtpk(SC[6], SC[7]); PIN(p0); SCHED; \
        O = MFMA32(v0, BC8(p0), O); SCHED; v0 = cat8(trd(v0p), trd((v0p) + 512)); EX(SC, 8); EX(SC, 9); EX(SC, 10); PIN(SC); SCHED; \
        SN = MFMA32(k1, q1, SN); SCHED; k1 = *(const LAS bf16x8*)((kp) + 1024); EX(SC, 11); EX(SC, 12); EX(SC, 13); PIN(SC); SCHED; \
        L = MFMA16(sel, BC8(p0), L); SCHED; EX(SC, 14); EX(SC, 15); \
        p1[0] = cvtpk(SC[8], SC[9]); p1[1] = cvtpk(SC[10], SC[11]); p1[2] = cvtpk(SC[12], SC[13]); p1[3] = cvtpk(SC[14], SC[15]); PIN(p1); SCHED; } while (0)
    u32x4 p0, p1 = {0u, 0u, 0u, 0u};
    int cur = 0, nxt = AT_STAGE, fre = 2 * AT_STAGE;
    for (int s = 0; s < 8; ++s) {
        if (s == 1) { WAIT_V(1); } else { WAIT_V(0); }
        WAIT_L(0); BAR;
        if (s + 2 < 8) AT_ISSUE(s + 2, fre);
        if (s == 0) {
            const int pi = blockIdx.x * 9 + wid;
            const char* src = pi < 512 ? (const char*)wo + (size_t)pi * 4096 : (const char*)w2 + (size_t)(pi - 512) * 4096;
            asm volatile("s_mov_b32 m0, %2\n\ts_nop 0\n\tglobal_load_lds_dword %0, %1" :: "v"(lane * 64), "s"(src), "s"((unsigned)(size_t)lds + AT_STAGE * AT_NS + wid * 256) : "memory");
        }
        if (s == 2 && wid == 0) {
            const int pi = blockIdx.x * 9 + 8;
            const char* src = pi < 512 ? (const char*)wo + (size_t)pi * 4096 : (const char*)w2 + (size_t)(pi - 512) * 4096;
            asm volatile("s_mov_b32 m0, %2\n\ts_nop 0\n\tglobal_load_lds_dword %0, %1" :: "v"(lane * 64), "s"(src), "s"((unsigned)(size_t)lds + AT_STAGE * AT_NS) : "memory");
        }
        const LAS char* kc = kb0 + cur; const LAS char* vc = vb0 + cur; const LAS char* kn = kb0 + nxt; const LAS char* vn = vb0 + nxt;
        AT_STEP(SA, SB, kc + 2 * 2048, vc + 1024, vc + 1 * 2048);            AT_STEP(SB, SA, kc + 3 * 2048, vc + 1 * 2048 + 1024, vc + 2 * 2048);
        AT_STEP(SA, SB, kc + 4 * 2048, vc + 2 * 2048 + 1024, vc + 3 * 2048); AT_STEP(SB, SA, kc + 5 * 2048, vc + 3 * 2048 + 1024, vc + 4 * 2048);
        AT_STEP(SA, SB, kc + 6 * 2048, vc + 4 * 2048 + 1024, vc + 5 * 2048); AT_STEP(SB, SA, kc + 7 * 2048, vc + 5 * 2048 + 1024, vc + 6 * 2048);
        AT_STEP(SA, SB, kn, vc + 6 * 2048 + 1024, vc + 7 * 2048);            AT_STEP(SB, SA, kn + 2048, vc + 7 * 2048 + 1024, vn);
        const int tmp = cur; cur = nxt; nxt = fre; fre = tmp;
    }
    O = MFMA32(v1, BC8(p1), O); L = MFMA16(sel, BC8(p1), L);
    TS(3);
    float lrow; { const float a = __shfl(L[0], lane & 15), b = __shfl(L[1], lane & 15); lrow = (lane & 16) ? b : a; }
    if (__any(!(lrow < 1.0e18f))) {
        float m = -INFINITY, l = 0.f;
#pragma unroll
        for (int i = 0; i < 16; ++i) { O[i] = 0.f; negm[i] = 0.f; }
        const u16* Vs = (const u16*)Vg;
        for (int t = 0; t < 64; ++t) { const int key0 = t * 32;
            k0 = *(const bf16x8*)(Kg + key0 * 64 + lane * 16); k1 = *(const bf16x8*)(Kg + key0 * 64 + 1024 + lane * 16);
#pragma unroll
            for (int j = 0; j < 8; ++j) { const int kk = key0 + 8 * (j >> 2) + 4 * hi + (j & 3); v0[j] = (short)Vs[kk * 32 + (lane & 31)]; v1[j] = (short)Vs[(kk + 16) * 32 + (lane & 31)]; }
            f32x16 S = MFMA32(k0, q0, negm); S = MFMA32(k1, q1, S);
            float mt = S[0];
#pragma unroll
            for (int i = 1; i < 16; ++i) mt = fmaxf(mt, S[i]);
            mt = fmaxf(mt, __shfl_xor(mt, 32));
            const float mn = fmaxf(m, mt), al = __builtin_amdgcn_exp2f(m - mn); m = mn;
            float ps = 0.f;
#pragma unroll
            for (int i = 0; i < 16; ++i) { S[i] = rbf(__builtin_amdgcn_exp2f(S[i] - mn)); ps += S[i]; O[i] *= al; }
            l = l * al + ps + __shfl_xor(ps, 32);
#pragma unroll
            for (int i = 0; i < 4; ++i) { p0[i] = cvtpk(S[2 * i], S[2 * i + 1]); p1[i] = cvtpk(S[8 + 2 * i], S[9 + 2 * i]); }
            O = MFMA32(v0, __builtin_bit_cast(bf16x8, p0), O); O = MFMA32(v1, __builtin_bit_cast(bf16x8, p1), O);
        }
        lrow = l;
    }
    {
        const float inv = 1.0f / lrow;
        unsigned pk[8];
#pragma unroll
        for (int i = 0; i < 8; ++i) pk[i] = cvtpk(O[2 * i] * inv, O[2 * i + 1] * inv);
        char* dst = ws + WS_AH + ((size_t)(qb * 16 + wid * 2 + ((lane >> 4) & 1)) * KB_AH + head) * 1024 + (lane & 15) * 16;
#pragma unroll
        for (int c = 0; c < 2; ++c) {
            auto r0 = __builtin_amdgcn_permlane32_swap(pk[4 * c + 0], pk[4 * c + 2], false, false);
            auto r1 = __builtin_amdgcn_permlane32_swap(pk[4 * c + 1], pk[4 * c + 3], false, false);
            *(u32x4*)(dst + (2 * c + hi) * 256) = (u32x4){r0[0], r1[0], r0[1], r1[1]};
        }
    }
    TS(4); TS_OUT(2);
#undef AT_ISSUE
#undef AT_STEP
#undef EX
#undef BC8
}

__global__ __launch_bounds__(512, 2) void k_gemm2(const u16* __restrict__ x, const u16* __restrict__ wo, const u16* __restrict__ w2, char* __restrict__ ws, u16* __restrict__ out) {
    extern __shared__ __attribute__((aligned(16))) char smem[];
    LAS char* lds = (LAS char*)smem;
    const int tid = threadIdx.x, wid = __builtin_amdgcn_readfirstlane(tid >> 6), lane = tid & 63;
    const int xcd = blockIdx.x & 7, idx = blockIdx.x >> 3;
    const int kh = xcd & 1, rt = ((xcd >> 1) & 1) * 8 + (idx & 7), ct = (xcd >> 2) * 4 + (idx >> 3), tile = rt * 8 + ct;
    TS_DECL; TS(0);
    constexpr int NT = 36;
    G128 g; g.a = ws + WS_AH + ((size_t)rt * 8 * KB_AH + kh * NT * 2) * 1024; g.a_rb = KB_AH * 1024; g.ldb2 = DM * 2; g.nt = NT;
    g.b0 = (const char*)wo + ct * 256; g.b1 = g.b0 + 128; g.nswitch = kh ? 0 : 16;
    g.c0 = (const char*)w2 + ct * 256 + ((ptrdiff_t)kh * NT - 16) * 64 * (DM * 2); g.c1 = g.c0 + 128;
    GAS unsigned* myflag = (GAS unsigned*)(ws + WS_FLAG) + (tile * 2 + kh) * 16; GAS unsigned* pflag = (GAS unsigned*)(ws + WS_FLAG) + (tile * 2 + (kh ^ 1)) * 16;
    const __amdgpu_buffer_rsrc_t rs = __builtin_amdgcn_make_buffer_rsrc((void*)(ws + WS_PART + (size_t)tile * 32768), 0, 32768, 0x00020000);
    if (wid >= 4) {
        unsigned vA, vB; g128_loader_offs(g, wid - 4, lane, vA, vB);
#pragma unroll
        for (int t = 0; t < 4; ++t) g128_issue(lds, g, t, wid - 4, vA, vB);
        g128_loader_loop(lds, g, wid - 4, vA, vB);
        TS(1);
        BAR;
        if (wid == 4) {
            if (lane == 0) __hip_atomic_store(myflag, 1u, __ATOMIC_RELAXED, __HIP_MEMORY_SCOPE_AGENT);
            for (unsigned spins = 0; __builtin_amdgcn_readfirstlane(__hip_atomic_load(pflag, __ATOMIC_RELAXED, __HIP_MEMORY_SCOPE_AGENT)) == 0u && spins < (1u << 22); ++spins) __builtin_amdgcn_s_sleep(1);
        }
        asm volatile("" ::: "memory"); BAR;
        TS(3);
    } else {
        f32x4 acc[4][4];
        g128_compute(lds, NT, wid, lane, acc);
        TS(1);
        const int fr = lane & 15, fq = lane >> 4, wm = wid >> 1, wn = wid & 1;
        const size_t rowo = (size_t)(rt * 128 + wm * 64 + kh * 32 + fr) * DM + ct * 128 + wn * 32 + (fq & 1) * 16 + (fq >> 1) * 8;
        u32x4 xv[2][2];
        f32x4 keep[2][4];
        if (kh == 0) {
#pragma unroll
            for (int m = 0; m < 2; ++m)
#pragma unroll
                for (int s = 0; s < 2; ++s) { __builtin_amdgcn_raw_buffer_store_b128(pack8(acc[2 + m][2 * s], acc[2 + m][2 * s + 1]), rs, ((m * 2 + s) * 256 + tid) * 16, 0, 16);
                    keep[m][2 * s] = acc[m][2 * s]; keep[m][2 * s + 1] = acc[m][2 * s + 1]; }
        } else {
#pragma unroll
            for (int m = 0; m < 2; ++m)
#pragma unroll
                for (int s = 0; s < 2; ++s) { __builtin_amdgcn_raw_buffer_store_b128(pack8(acc[m][2 * s], acc[m][2 * s + 1]), rs, 16384 + ((m * 2 + s) * 256 + tid) * 16, 0, 16);
                    keep[m][2 * s] = acc[2 + m][2 * s]; keep[m][2 * s + 1] = acc[2 + m][2 * s + 1]; }
        }
        WAIT_V(0); BAR;
        TS(2);
#pragma unroll
        for (int m = 0; m < 2; ++m)
#pragma unroll
            for (int s = 0; s < 2; ++s) xv[m][s] = *(const u32x4*)(x + rowo + (size_t)m * 16 * DM + s * 64);
        asm volatile("" ::: "memory"); BAR; asm volatile("" ::: "memory");
        TS(3);
        u32x4 pv[2][2];
#pragma unroll
        for (int m = 0; m < 2; ++m)
#pragma unroll
            for (int s = 0; s < 2; ++s) pv[m][s] = __builtin_amdgcn_raw_buffer_load_b128(rs, (kh ^ 1) * 16384 + ((m * 2 + s) * 256 + tid) * 16, 0, 16);
#pragma unroll
        for (int m = 0; m < 2; ++m)
#pragma unroll
            for (int s = 0; s < 2; ++s) { float lo[4], hi4[4]; const u32x4 p_ = pv[m][s], x_ = xv[m][s];
                const f32x4 a0 = keep[m][2 * s] + (f32x4){bf_lo(p_[0]), bf_hi(p_[0]), bf_lo(p_[1]), bf_hi(p_[1])}, a1 = keep[m][2 * s + 1] + (f32x4){bf_lo(p_[2]), bf_hi(p_[2]), bf_lo(p_[3]), bf_hi(p_[3])};
#pragma unroll
                for (int i = 0; i < 4; ++i) { auto r = __builtin_amdgcn_permlane16_swap(__float_as_uint(a0[i]), __float_as_uint(a1[i]), false, false);
                    lo[i] = __uint_as_float(r[0]); hi4[i] = __uint_as_float(r[1]); }
                u32x4 o; o[0] = cvtpk(lo[0] + bf_lo(x_[0]), lo[1] + bf_hi(x_[0])); o[1] = cvtpk(lo[2] + bf_lo(x_[1]), lo[3] + bf_hi(x_[1]));
                o[2] = cvtpk(hi4[0] + bf_lo(x_[2]), hi4[1] + bf_hi(x_[2])); o[3] = cvtpk(hi4[2] + bf_lo(x_[3]), hi4[3] + bf_hi(x_[3]));
                *(u32x4*)(out + rowo + (size_t)m * 16 * DM + s * 64) = o; }
    }
    TS(5); TS_OUT(3);
}

extern "C" void kernel_launch(void* const* d_in, const int* in_sizes, int n_in, void* d_out, int out_size, void* d_ws, size_t ws_size, hipStream_t stream) {
    const u16* x = (const u16*)d_in[0]; const u16* wqkv = (const u16*)d_in[1]; const u16* wo = (const u16*)d_in[2];
    const u16* w1 = (const u16*)d_in[4]; const u16* w3 = (const u16*)d_in[5]; const u16* w2 = (const u16*)d_in[6];
    char* ws = (char*)d_ws;
    (void)hipFuncSetAttribute((const void*)k_gemm1, hipFuncAttributeMaxDynamicSharedMemorySize, G1_LDS);
    (void)hipFuncSetAttribute((const void*)k_attn, hipFuncAttributeMaxDynamicSharedMemorySize, AT_LDS);
    (void)hipFuncSetAttribute((const void*)k_gemm2, hipFuncAttributeMaxDynamicSharedMemorySize, 131072);
    k_ln<<<256, 512, 0, stream>>>(x, ws);
    k_gemm1<<<256, 512, G1_LDS, stream>>>(wqkv, w1, w3, ws);
    k_attn<<<256, 512, AT_LDS, stream>>>(ws, wo, w2);
    k_gemm2<<<256, 512, 131072, stream>>>(x, wo, w2, ws, (u16*)d_out);
}
```

```cpp
#include <hip/hip_runtime.h>
#include <hip/hip_bf16.h>
#include <stdint.h>

#define LAS __attribute__((address_space(3)))
#define GAS __attribute__((address_space(1)))
#define DI __device__ __forceinline__
typedef unsigned short u16;
typedef unsigned long long u64;
typedef short bf16x8 __attribute__((ext_vector_type(8)));
typedef short s16x4 __attribute__((ext_vector_type(4)));
typedef float f32x2 __attribute__((ext_vector_type(2)));
typedef float f32x4 __attribute__((ext_vector_type(4)));
typedef float f32x16 __attribute__((ext_vector_type(16)));
typedef unsigned u32x2 __attribute__((ext_vector_type(2)));
typedef unsigned u32x4 __attribute__((ext_vector_type(4)));
typedef __bf16 bf16x2_t __attribute__((ext_vector_type(2)));

constexpr int SEQ = 2048, DM = 1024, FF = 3584;
constexpr int KB_X = DM / 32;
constexpr int KB_AH = (DM + FF) / 32;
constexpr unsigned WS_XN = 0;
constexpr unsigned WS_Q = WS_XN + SEQ * DM * 2;
constexpr unsigned WS_K = WS_Q + SEQ * DM * 2;
constexpr unsigned WS_V = WS_K + SEQ * DM * 2;
constexpr unsigned WS_AH = WS_V + SEQ * DM * 2;
constexpr unsigned WS_PART = WS_AH + SEQ * (DM + FF) * 2;
constexpr unsigned WS_FLAG = WS_PART + 128 * 32768;
constexpr unsigned WS_END = WS_FLAG + 16384;
constexpr int F_LN = 32, F_QKV = F_LN + 256, F_H = F_QKV + 256, F_AT = F_H + 256, F_G2 = F_AT + 256;
constexpr int LDS_BYTES = 163840;

DI unsigned cvtpk(float lo, float hi) { f32x2 v = {lo, hi}; bf16x2_t b = __builtin_convertvector(v, bf16x2_t); return __builtin_bit_cast(unsigned, b); }
DI float bf_lo(unsigned u) { return __uint_as_float(u << 16); }
DI float bf_hi(unsigned u) { return __uint_as_float(u & 0xffff0000u); }
DI float rbf(float f) { return __uint_as_float(cvtpk(f, f) << 16); }
DI s16x4 trd(const LAS char* p) { return __builtin_amdgcn_ds_read_tr16_b64_v4i16((LAS s16x4*)p); }
DI bf16x8 cat8(s16x4 a, s16x4 b) { return __builtin_shufflevector(a, b, 0, 1, 2, 3, 4, 5, 6, 7); }
template <int OFF, int D> DI void glds(const char* base, unsigned voff, unsigned dst) {
    asm volatile("s_add_u32 m0, %2, %4\n\ts_nop 0\n\tglobal_load_lds_dwordx4 %0, %1 offset:%3" :: "v"(voff), "s"(base), "s"(dst), "i"(OFF), "i"(D - OFF) : "memory", "scc"); }
template <int D> DI void glds_pair_sc1(const char* base, unsigned voff, unsigned dst) {
    asm volatile("s_add_u32 m0, %2, %3\n\ts_nop 0\n\tglobal_load_lds_dwordx4 %0, %1 sc1\n\tglobal_load_lds_dwordx4 %0, %1 offset:1024 sc1" :: "v"(voff), "s"(base), "s"(dst), "i"(D) : "memory", "scc"); }
template <int D> DI void touch(const char* base, unsigned voff, unsigned dst) {
    asm volatile("s_add_u32 m0, %2, %3\n\ts_nop 0\n\tglobal_load_lds_dword %0, %1" :: "v"(voff), "s"(base), "s"(dst), "i"(D) : "memory", "scc"); }
#define WAIT_V(n) asm volatile("s_waitcnt vmcnt(" #n ")" ::: "memory")
#define WAIT_L(n) asm volatile("s_waitcnt lgkmcnt(" #n ")" ::: "memory")
#define BAR __builtin_amdgcn_s_barrier()
#define CBAR asm volatile("" ::: "memory")
#define SCHED __builtin_amdgcn_sched_barrier(0)
#define PIN(x) asm volatile("" : "+v"(x))
#define MFMA16(a, b, c) __builtin_amdgcn_mfma_f32_16x16x32_bf16(a, b, c, 0, 0, 0)
#define MFMA32(a, b, c) __builtin_amdgcn_mfma_f32_32x32x16_bf16(a, b, c, 0, 0, 0)
#define ST16(v, off) __builtin_amdgcn_raw_buffer_store_b128(v, rs, (int)(off), 0, 16)
#define ST8(v, off) __builtin_amdgcn_raw_buffer_store_b64(v, rs, (int)(off), 0, 16)
#define LD16(off) __builtin_amdgcn_raw_buffer_load_b128(rs, (int)(off), 0, 16)
DI u64 flag_ld(const GAS u64* p) { return __hip_atomic_load(p, __ATOMIC_RELAXED, __HIP_MEMORY_SCOPE_AGENT); }
DI void flag_st(GAS u64* p, u64 v) { __hip_atomic_store(p, v, __ATOMIC_RELAXED, __HIP_MEMORY_SCOPE_AGENT); }
DI void flag_wait(const GAS u64* p, u64 tag) {
    for (unsigned spins = 0; spins < (1u << 18); ++spins) { if (__all(flag_ld(p) == tag)) break; __builtin_amdgcn_s_sleep(1); }
    CBAR;
}
DI float sum8lanes(float v) {
    v += __builtin_bit_cast(float, __builtin_amdgcn_update_dpp(0, __builtin_bit_cast(int, v), 0x128  , 0xf, 0xf, false));
    { auto r = __builtin_amdgcn_permlane16_swap(__float_as_uint(v), __float_as_uint(v), false, false); v = __uint_as_float(r[0]) + __uint_as_float(r[1]); }
    { auto r = __builtin_amdgcn_permlane32_swap(__float_as_uint(v), __float_as_uint(v), false, false); v = __uint_as_float(r[0]) + __uint_as_float(r[1]); }
    return v;
}
DI int opaque_tid() { int t = threadIdx.x; asm volatile("" : "+v"(t)); return t; }

#define TS(i)
#define TR(i)

DI float silu_mul(float g, float u) { return g * u * __builtin_amdgcn_rcpf(1.0f + __builtin_amdgcn_exp2f(g * -1.4426950408889634f)); }
DI u32x4 pack8(const f32x4& a, const f32x4& b) { u32x4 o; o[0] = cvtpk(a[0], a[1]); o[1] = cvtpk(a[2], a[3]); o[2] = cvtpk(b[0], b[1]); o[3] = cvtpk(b[2], b[3]); return o; }
constexpr float QSCALE = 0.17677669529663687f * 1.4426950408889634f;

struct G128 { const char* a0; const char* a1; const char* b0; const char* b1; int nsw; };
DI void g128_issue(unsigned lds0, const G128& g, int t, int lw, unsigned voffA, unsigned voffB) {
    const unsigned sa = lds0 + (t & 3) * 32768 + lw * 4096, sb = lds0 + (t & 3) * 32768 + lw * 2048;
    const char* a = (t < g.nsw ? g.a0 : g.a1) + (size_t)t * 2048; const char* b = (t < g.nsw ? g.b0 : g.b1) + (size_t)t * (64 * DM * 2);
    glds_pair_sc1<0>(a, voffA, sa); glds_pair_sc1<2048>(a + KB_AH * 1024, voffA, sa);
    glds<0, 16384>(b, voffB, sb); glds<128, 24576>(b, voffB, sb);
    glds<0, 16384 + 1024>(b + 8 * DM * 2, voffB, sb); glds<128, 24576 + 1024>(b + 8 * DM * 2, voffB, sb);
}

__global__ __launch_bounds__(512, 2) void k_block(const u16* __restrict__ x, const u16* __restrict__ wqkv, const u16* __restrict__ wo, const u16* __restrict__ w1,
                                                  const u16* __restrict__ w3, const u16* __restrict__ w2, char* __restrict__ ws, u16* __restrict__ out) {
    extern __shared__ __attribute__((aligned(16))) char smem[];
    LAS char* lds = (LAS char*)smem;
    const unsigned lds0 = (unsigned)(size_t)lds;
    const int wid = __builtin_amdgcn_readfirstlane(threadIdx.x >> 6);
    const int bid = blockIdx.x, xcd = bid & 7, idx = bid >> 3;
    GAS u64* flags = (GAS u64*)(ws + WS_FLAG);
    const __amdgpu_buffer_rsrc_t rs = __builtin_amdgcn_make_buffer_rsrc((void*)ws, 0, WS_END, 0x00020000);
    TR(30); TS(0);
    u64 tag;
    const int cj = idx >> 2, ct = cj < 3 ? (xcd >> 1) * 3 + cj : 12 + (xcd >> 1) * 5 + (cj - 3), rt = (xcd & 1) * 4 + (idx & 3);
    const bool qkv = ct < 12;
    u32x4 hmain[8]; u32x2 hext[4];

    {
        const int tid = opaque_tid(), lane = tid & 63, wr = wid >> 2, wc = wid & 3;
        const unsigned ldb2 = qkv ? 3 * DM * 2 : FF * 2;
        const char* cB0 = qkv ? (const char*)wqkv + ct * 512 : (const char*)w1 + (ct - 12) * 256;
        const char* cB1 = qkv ? cB0 + 256 : (const char*)w3 + (ct - 12) * 256;
        const char* cA = ws + WS_XN + rt * 16 * KB_X * 1024;
        const unsigned voffA = wid * (KB_X * 1024) + lane * 16;
        const unsigned voffB = (unsigned)(wid * 8 + (lane >> 3)) * ldb2 + (((lane & 7) ^ (2 * ((lane >> 4) & 3))) * 16);
        const char* pE = ((lane & 1) ? (const char*)w3 : (const char*)w1) + (size_t)(wid * 8 + (lane >> 3)) * (FF * 2) + (2560 + ct * 32) * 2 + ((((lane >> 1) & 3) ^ ((lane >> 4) & 3)) * 16);
        const size_t kstepB = (size_t)64 * ldb2;
        const unsigned ldsA = lds0 + wid * 2048, ldsB = lds0 + wid * 1024;
        const char* cT = ((idx & 2) ? cB1 : cB0) + (idx & 1) * 128;
#define SA(b, h) (((b) * 2 + (h)) * 16384)
#define SB(b, h) (((b) * 2 + (h)) * 16384)
#define STA(b, h, kt) glds_pair_sc1<SA(b, h)>(cA + (h) * 8 * KB_X * 1024 + (kt) * 2048, voffA, ldsA)
#define STB(b, h, kt) do { const char* s_ = ((h) ? cB1 : cB0) + (size_t)(kt) * kstepB; \
        glds<0, 65536 + SB(b, h)>(s_, voffB, ldsB); glds<128, 65536 + SB(b, h) + 8192>(s_, voffB, ldsB); } while (0)
#define TCH(kt) touch<148480>(cT + (size_t)(kt) * kstepB, voffB, ldsB)
#define STE(b, kt) do { const char* s_ = pE + (size_t)(kt) * (64 * FF * 2); \
        asm volatile("s_add_u32 m0, %1, %2\n\ts_nop 0\n\tglobal_load_lds_dwordx4 %0, off" :: "v"(s_), "s"(ldsB), "i"(131072 + (b) * 8192) : "memory", "scc"); } while (0)
        {
            LAS f32x2* red = (LAS f32x2*)(lds + 147456);
            const int r = lane & 7, g = (lane >> 3) & 3, kbl = lane >> 5, row = bid * 8 + r;
            float f[32];
            if (wid < 4) {
                const u64 epoch = flag_ld(flags);
                u32x2 v[4][2];
#pragma unroll
                for (int c = 0; c < 4; ++c) { const u16* p = x + (size_t)row * DM + (8 * wid + 2 * c + kbl) * 32 + 4 * g;
                    v[c][0] = *(const u32x2*)p; v[c][1] = *(const u32x2*)(p + 16); }
#pragma unroll
                for (int c = 0; c < 4; ++c)
#pragma unroll
                    for (int h = 0; h < 2; ++h) { f[c * 8 + h * 4 + 0] = bf_lo(v[c][h][0]); f[c * 8 + h * 4 + 1] = bf_hi(v[c][h][0]); f[c * 8 + h * 4 + 2] = bf_lo(v[c][h][1]); f[c * 8 + h * 4 + 3] = bf_hi(v[c][h][1]); }
                float s = 0.f;
#pragma unroll
                for (int i = 0; i < 32; ++i) s += f[i];
                const float mw = sum8lanes(s) * (1.0f / 256.0f);
                float q = 0.f;
#pragma unroll
                for (int i = 0; i < 32; ++i) { const float d = f[i] - mw; q += d * d; }
                q = sum8lanes(q);
                if (lane < 8) red[wid * 8 + lane] = (f32x2){mw, q};
                if (lane == 8) *(LAS u64*)(red + 32) = epoch + 1;
                WAIT_L(0);
            } else {
#define STB2(b, h, kt) do { STB(b, h, kt); const char* s2_ = ((h) ? cB1 : cB0) + (size_t)(kt) * kstepB - 32 * (size_t)ldb2; \
        glds<0, 65536 + SB(b, h) - 4096>(s2_, voffB, ldsB); glds<128, 65536 + SB(b, h) + 8192 - 4096>(s2_, voffB, ldsB); } while (0)
#define STE2(b, kt) do { STE(b, kt); const char* s2_ = pE + (size_t)(kt) * (64 * FF * 2) - 32 * FF * 2; \
        asm volatile("s_add_u32 m0, %1, %2\n\ts_nop 0\n\tglobal_load_lds_dwordx4 %0, off" :: "v"(s2_), "s"(ldsB), "i"(131072 + (b) * 8192 - 4096) : "memory", "scc"); } while (0)
                STB2(0, 0, 0); STB2(0, 1, 0); STE2(0, 0); STB2(1, 0, 1); STB2(1, 1, 1); STE2(1, 1);
                TCH(2); TCH(3); touch<148480>(cT + 2 * kstepB - 32 * (size_t)ldb2, voffB, ldsB); touch<148480>(cT + 3 * kstepB - 32 * (size_t)ldb2, voffB, ldsB);
#undef STB2
#undef STE2
            }
            BAR; CBAR;
            { const u32x2 e = *(LAS u32x2*)(red + 32); tag = ((u64)(unsigned)__builtin_amdgcn_readfirstlane(e[1]) << 32) | (unsigned)__builtin_amdgcn_readfirstlane(e[0]); }
            asm volatile("" : "+s"(tag));
            if (wid < 4) {
                const f32x2 a = red[r], b = red[8 + r], c = red[16 + r], d = red[24 + r];
                const float mean = (a.x + b.x + c.x + d.x) * 0.25f, da = a.x - mean, db = b.x - mean, dc = c.x - mean, dd = d.x - mean;
                const float var = ((a.y + b.y) + (c.y + d.y) + 256.0f * ((da * da + db * db) + (dc * dc + dd * dd))) * (1.0f / DM);
                const float mb = rbf(mean), sd = rbf(sqrtf(rbf(rbf(var) + 1e-5f))), inv = 1.0f / sd;
#pragma unroll
                for (int c4 = 0; c4 < 4; ++c4) { u32x4 o;
#pragma unroll
                    for (int i = 0; i < 4; ++i) o[i] = cvtpk(rbf(f[c4 * 8 + 2 * i] - mb) * inv, rbf(f[c4 * 8 + 2 * i + 1] - mb) * inv);
                    ST16(o, WS_XN + ((row >> 4) * KB_X + 8 * wid + 2 * c4 + kbl) * 1024 + (g * 16 + (row & 15)) * 16); }
                WAIT_V(0);
            }
            BAR;
            TS(1);
            if (wid == 0) { if (lane == 0) flag_st(flags + F_LN + bid, tag); flag_wait(flags + F_LN + rt * 32 + (lane & 31), tag); }
            BAR; CBAR;
            TS(2);
        }
        f32x4 acc[2][2][4][2], ace[2][4];
        {
            const int g = lane >> 4, q = (lane & 15) >> 2, p = lane & 3, swz = (2 * g + (q >> 1)) & 3;
            const int aoff = wr * 8192 + lane * 16;
            const int boff0 = 65536 + (wc >> 1) * 8192 + (4 * g + q) * 128 + (((2 * (wc & 1)) ^ swz) * 32) + p * 8, boff1 = boff0 ^ 32;
            int eoff = 131072 + (4 * g + q) * 128 + ((wc ^ swz) * 32) + p * 8; PIN(eoff);
#define LDA(dst, b, h) do { _Pragma("unroll") for (int m = 0; m < 4; ++m) _Pragma("unroll") for (int k = 0; k < 2; ++k) \
        dst[m][k] = *(const LAS bf16x8*)(lds + aoff + SA(b, h) + (m * 2 + k) * 1024); } while (0)
#define LDB(dst, b, h) do { _Pragma("unroll") for (int n = 0; n < 2; ++n) _Pragma("unroll") for (int k = 0; k < 2; ++k) { \
        const LAS char* b_ = lds + (n ? boff1 : boff0) + SB(b, h) + k * 4096; dst[n][k] = cat8(trd(b_), trd(b_ + 2048)); } } while (0)
#define LDE(dst, b) do { _Pragma("unroll") for (int k = 0; k < 2; ++k) { const LAS char* b_ = lds + eoff + (b) * 8192 + k * 4096; dst[k] = cat8(trd(b_), trd(b_ + 2048)); } } while (0)
#define MMA(ai) do { __builtin_amdgcn_s_setprio(1); \
        _Pragma("unroll") for (int k = 0; k < 2; ++k) _Pragma("unroll") for (int m = 0; m < 4; ++m) { \
            acc[ai][0][m][0] = MFMA16(B0[0][k], At[m][k], acc[ai][0][m][0]); acc[ai][0][m][1] = MFMA16(B0[1][k], At[m][k], acc[ai][0][m][1]); \
            acc[ai][1][m][0] = MFMA16(B1[0][k], At[m][k], acc[ai][1][m][0]); acc[ai][1][m][1] = MFMA16(B1[1][k], At[m][k], acc[ai][1][m][1]); \
            ace[ai][m] = MFMA16(BE[k], At[m][k], ace[ai][m]); } \
        __builtin_amdgcn_s_setprio(0); } while (0)
            STA(0, 0, 0); STA(0, 1, 0); STA(1, 0, 1);
#pragma unroll
            for (int a = 0; a < 2; ++a)
#pragma unroll
                for (int m = 0; m < 4; ++m) { ace[a][m] = (f32x4){0.f, 0.f, 0.f, 0.f};
#pragma unroll
                    for (int b = 0; b < 2; ++b)
#pragma unroll
                        for (int n = 0; n < 2; ++n) acc[a][b][m][n] = (f32x4){0.f, 0.f, 0.f, 0.f}; }
            bf16x8 At[4][2], B0[2][2], B1[2][2], BE[2];
            constexpr int nt = DM / 64;
            if (wr == 1) BAR;
            WAIT_V(4); BAR;
            WAIT_V(2); BAR;
            TS(3);
            for (int t = 0; t < nt; t += 2) {
                const int t2 = t + 2 < nt ? t + 2 : t, tt = t + 4 < nt ? t + 4 : t;
                LDB(B0, 0, 0); LDB(B1, 0, 1); LDE(BE, 0); SCHED; LDA(At, 0, 0); STA(1, 1, t + 1); TCH(tt);
                WAIT_V(10); WAIT_L(0); BAR; MMA(0); BAR; SCHED;
                LDA(At, 0, 1); STB(0, 0, t2); STB(0, 1, t2); STE(0, t2); STA(0, 0, t2);
                WAIT_V(10); WAIT_L(0); BAR; MMA(1); BAR; SCHED;
                LDB(B0, 1, 0); LDB(B1, 1, 1); LDE(BE, 1); SCHED; LDA(At, 1, 0); STA(0, 1, t2); TCH(tt + 1);
                WAIT_V(10); WAIT_L(0); BAR; MMA(0); BAR; SCHED;
                LDA(At, 1, 1); STB(1, 0, t2 + 1); STB(1, 1, t2 + 1); STE(1, t2 + 1); STA(1, 0, t2 + 1);
                WAIT_V(10); WAIT_L(0); BAR; MMA(1); if (t + 2 < nt || wr == 0) BAR; SCHED;
            }
            TS(4);
#undef SA
#undef SB
#undef STA
#undef STB
#undef STE
#undef TCH
#undef LDA
#undef LDB
#undef LDE
#undef MMA
        }
        const int ln = opaque_tid() & 63, fr = ln & 15, fq = ln >> 4;
        if (qkv) {
            const int kind = ct >> 2;
            const float sc = kind == 0 ? QSCALE : 1.0f;
            const unsigned base = WS_Q + kind * (SEQ * DM * 2) + ((ct & 3) * 8 + wc) * 131072 + (rt * 256 + wr * 64) * 64;
            const unsigned lo0 = base + (kind == 2 ? fr * 64 + fq * 16 : fr * 16 + fq * 512), lo1 = lo0 + (kind == 2 ? 1024 : 256);
#pragma unroll
            for (int bj = 0; bj < 2; ++bj)
#pragma unroll
                for (int ai = 0; ai < 2; ++ai)
#pragma unroll
                    for (int m = 0; m < 4; ++m)
                        ST16(pack8(acc[ai][bj][m][0] * sc, acc[ai][bj][m][1] * sc), bj * 4 * 131072 + ai * 8192 + (m >> 1) * 2048 + ((m & 1) ? lo1 : lo0));
            WAIT_V(0); BAR;
            if (wid == 0 && ln == 0) flag_st(flags + F_QKV + rt * 32 + ct, tag);
        } else {
#pragma unroll
            for (int ai = 0; ai < 2; ++ai)
#pragma unroll
                for (int m = 0; m < 4; ++m) { f32x4 h0, h1;
#pragma unroll
                    for (int j = 0; j < 4; ++j) { h0[j] = silu_mul(acc[ai][0][m][0][j], acc[ai][1][m][0][j]); h1[j] = silu_mul(acc[ai][0][m][1][j], acc[ai][1][m][1][j]); }
                    hmain[ai * 4 + m] = pack8(h0, h1); }
        }
#pragma unroll
        for (int ai = 0; ai < 2; ++ai)
#pragma unroll
            for (int mp = 0; mp < 2; ++mp) { float h[4];
#pragma unroll
                for (int j = 0; j < 4; ++j) { auto r = __builtin_amdgcn_permlane32_swap(__float_as_uint(ace[ai][2 * mp][j]), __float_as_uint(ace[ai][2 * mp + 1][j]), false, false);
                    h[j] = silu_mul(__uint_as_float(r[0]), __uint_as_float(r[1])); }
                hext[ai * 2 + mp] = (u32x2){cvtpk(h[0], h[1]), cvtpk(h[2], h[3])}; }
        TS(5);
        WAIT_V(0);
        TS(6);
    }

    const int kh = xcd & 1, rt2 = ((xcd >> 1) & 1) * 8 + (idx & 7), ct2 = (xcd >> 2) * 4 + (idx >> 3);
    constexpr int NT2 = 36;
    G128 g2;
    g2.nsw = kh ? NT2 : 20;
    g2.a0 = ws + WS_AH + (rt2 * 8 * KB_AH + (kh ? 72 : 32)) * 1024; g2.a1 = ws + WS_AH + rt2 * 8 * KB_AH * 1024 - 20 * 2048;
    g2.b0 = (const char*)w2 + ct2 * 256 + (kh ? 1280 * DM * 2 : 0); g2.b1 = (const char*)wo + ct2 * 256 - 20 * 64 * DM * 2;

    {
        constexpr int AT_BASE = 65536, AT_STAGE = 32768;
        const int lane = opaque_tid() & 63, hi = lane >> 5;
        const int head = xcd * 4 + (idx >> 3), qb = idx & 7;
        if (wid == 0) flag_wait(flags + F_QKV + (lane < 4 ? (lane & 1) * 32 + 4 + (lane >> 1) * 4 : qb * 32) + (head >> 3), tag);
        BAR; CBAR;
        TS(7);
        const char* Kg = ws + WS_K + head * 131072; const char* Vg = ws + WS_V + head * 131072;
        const unsigned soff = wid * 2048 + lane * 16;
        const unsigned sdst = lds0 + AT_BASE + wid * 2048;
#define AT_ISSUE(s, slot) do { glds_pair_sc1<0>(Kg + (s) * 16384, soff, sdst + (slot)); glds_pair_sc1<16384>(Vg + (s) * 16384, soff, sdst + (slot)); } while (0)
        const unsigned qoff = WS_Q + (head * 64 + qb * 8 + wid) * 2048 + lane * 16;
        const bf16x8 q0 = __builtin_bit_cast(bf16x8, LD16(qoff)), q1 = __builtin_bit_cast(bf16x8, LD16(qoff + 1024));
        AT_ISSUE(0, 0); AT_ISSUE(1, AT_STAGE);
        const GAS u64* kvflag = flags + F_QKV + (lane & 7) * 32 + 4 + ((lane >> 3) & 1) * 4 + (head >> 3);
        u64 seen = flag_ld(kvflag);
        bf16x8 sel; { const short one = ((lane & 15) == ((lane >> 4) & 1)) ? (short)0x3f80 : (short)0;
#pragma unroll
            for (int i = 0; i < 8; ++i) sel[i] = one; }
        const LAS char* kb0 = lds + AT_BASE + lane * 16;
        const LAS char* vb0 = lds + AT_BASE + 16384 + (4 * hi + ((lane & 15) >> 2)) * 64 + ((lane >> 4) & 1) * 32 + (lane & 3) * 8;
        f32x16 O, negm, SA, SB; f32x4 L = {0.f, 0.f, 0.f, 0.f};
        bf16x8 k0, k1, v0, v1;
#pragma unroll
        for (int i = 0; i < 16; ++i) { O[i] = 0.f; negm[i] = 0.f; }
        WAIT_V(5); BAR;
        TS(8);
        {
            k0 = *(const LAS bf16x8*)(kb0); k1 = *(const LAS bf16x8*)(kb0 + 1024);
            SA = MFMA32(k0, q0, negm); SA = MFMA32(k1, q1, SA);
            k0 = *(const LAS bf16x8*)(kb0 + 2048); k1 = *(const LAS bf16x8*)(kb0 + 3072);
            v0 = cat8(trd(vb0), trd(vb0 + 512)); v1 = cat8(trd(vb0 + 1024), trd(vb0 + 1536));
            float m = SA[0];
#pragma unroll
            for (int i = 1; i < 16; ++i) m = fmaxf(m, SA[i]);
            m = fmaxf(m, __shfl_xor(m, 32));
#pragma unroll
            for (int i = 0; i < 16; ++i) { negm[i] = -m; SA[i] -= m; }
            PIN(negm);
        }
#define EX(S, i) S[i] = __builtin_amdgcn_exp2f(S[i])
#define BC8(x) __builtin_bit_cast(bf16x8, x)
#define AT_STEP(SC, SN, kp, v1p, v0p) do { \
        O = MFMA32(v1, BC8(p1), O); SCHED; v1 = cat8(trd(v1p), trd((v1p) + 512)); EX(SC, 0); EX(SC, 1); EX(SC, 2); PIN(SC); SCHED; \
        SN = MFMA32(k0, q0, negm); SCHED; k0 = *(const LAS bf16x8*)(kp); EX(SC, 3); EX(SC, 4); EX(SC, 5); PIN(SC); SCHED; \
        L = MFMA16(sel, BC8(p1), L); SCHED; EX(SC, 6); EX(SC, 7); \
        p0[0] = cvtpk(SC[0], SC[1]); p0[1] = cvtpk(SC[2], SC[3]); p0[2] = cvtpk(SC[4], SC[5]); p0[3] = cvtpk(SC[6], SC[7]); PIN(p0); SCHED; \
        O = MFMA32(v0, BC8(p0), O); SCHED; v0 = cat8(trd(v0p), trd((v0p) + 512)); EX(SC, 8); EX(SC, 9); EX(SC, 10); PIN(SC); SCHED; \
        SN = MFMA32(k1, q1, SN); SCHED; k1 = *(const LAS bf16x8*)((kp) + 1024); EX(SC, 11); EX(SC, 12); EX(SC, 13); PIN(SC); SCHED; \
        L = MFMA16(sel, BC8(p0), L); SCHED; EX(SC, 14); EX(SC, 15); \
        p1[0] = cvtpk(SC[8], SC[9]); p1[1] = cvtpk(SC[10], SC[11]); p1[2] = cvtpk(SC[12], SC[13]); p1[3] = cvtpk(SC[14], SC[15]); PIN(p1); SCHED; } while (0)
        u32x4 p0, p1 = {0u, 0u, 0u, 0u};
        int cur = 0, nxt = AT_STAGE, fre = 2 * AT_STAGE;
        const GAS u64* hflag = flags + F_H + (rt2 >> 1) * 32 + (lane & 31);
        const unsigned hb = WS_AH + ((rt * 16 + (wid >> 2) * 4) * KB_AH + KB_X + (ct - 12) * 4 + (wid & 3)) * 1024 + lane * 16;
        const int cq = 2 * (wid & 3) + ((lane >> 4) & 1);
        const unsigned eb = WS_AH + ((rt * 16 + (wid >> 2) * 4 + hi) * KB_AH + KB_X + 80 + ct) * 1024 + ((cq & 3) * 16 + (lane & 15)) * 16 + (cq >> 2) * 8;
#define H_MAIN(j) do { if (!qkv && s < 4) { if (s == 0) ST16(hmain[j], hb + (j) * KB_AH * 1024); else if (s == 1) ST16(hmain[2 + (j)], hb + (2 + (j)) * KB_AH * 1024); \
            else if (s == 2) ST16(hmain[4 + (j)], hb + (8 + (j)) * KB_AH * 1024); else ST16(hmain[6 + (j)], hb + (10 + (j)) * KB_AH * 1024); } SCHED; } while (0)
#define H_EXT() do { if (s < 4) { if (s == 0) ST8(hext[0], eb); else if (s == 1) ST8(hext[1], eb + 2 * KB_AH * 1024); \
            else if (s == 2) ST8(hext[2], eb + 8 * KB_AH * 1024); else ST8(hext[3], eb + 10 * KB_AH * 1024); } SCHED; } while (0)
        for (int s = 0; s < 8; ++s) {
            if (s >= 1 && s <= 4) { if (qkv) { WAIT_V(2); } else { WAIT_V(4); } } else { WAIT_V(0); }
            WAIT_L(0); BAR;
            if (s == 0) { if (!__all(seen == tag)) flag_wait(kvflag, tag); }
            if (s + 2 < 8) AT_ISSUE(s + 2, fre);
            if (s < 4) {
                const int pi = (bid * 8 + wid) + (s & 1) * 2048, pj = pi < 2304 ? pi : wid;
                touch<-AT_BASE>(pj < 512 ? (const char*)wo + (size_t)pj * 4096 : (const char*)w2 + (size_t)(pj - 512) * 4096, lane * 64, sdst);
            }
            if (s == 5) { if (wid == 0 && lane == 0) flag_st(flags + F_H + rt * 32 + ct, tag); }
            if (s == 6) seen = flag_ld(hflag);
            if (s == 7 && wid >= 4) {
                if (!__all(seen == tag)) flag_wait(hflag, tag);
                const unsigned vA = (wid - 4) * 2 * KB_AH * 1024 + lane * 16, vB = ((wid - 4) * 16 + (lane >> 3)) * (DM * 2) + (((lane & 7) ^ (2 * ((lane >> 4) & 3))) * 16);
                g128_issue(lds0, g2, 0, wid - 4, vA, vB); g128_issue(lds0, g2, 1, wid - 4, vA, vB);
            }
            if (wid >= 4) __builtin_amdgcn_s_setprio(1);
            const LAS char* kc = kb0 + cur; const LAS char* vc = vb0 + cur; const LAS char* kn = kb0 + nxt; const LAS char* vn = vb0 + nxt;
            AT_STEP(SA, SB, kc + 2 * 2048, vc + 1024, vc + 1 * 2048);            AT_STEP(SB, SA, kc + 3 * 2048, vc + 1 * 2048 + 1024, vc + 2 * 2048);
            H_MAIN(0);
            AT_STEP(SA, SB, kc + 4 * 2048, vc + 2 * 2048 + 1024, vc + 3 * 2048); AT_STEP(SB, SA, kc + 5 * 2048, vc + 3 * 2048 + 1024, vc + 4 * 2048);
            H_EXT();
            AT_STEP(SA, SB, kc + 6 * 2048, vc + 4 * 2048 + 1024, vc + 5 * 2048); __builtin_amdgcn_s_setprio(0); AT_STEP(SB, SA, kc + 7 * 2048, vc + 5 * 2048 + 1024, vc + 6 * 2048);
            H_MAIN(1);
            AT_STEP(SA, SB, kn, vc + 6 * 2048 + 1024, vc + 7 * 2048);            AT_STEP(SB, SA, kn + 2048, vc + 7 * 2048 + 1024, vn);
            const int tmp = cur; cur = nxt; nxt = fre; fre = tmp;
        }
        O = MFMA32(v1, BC8(p1), O); L = MFMA16(sel, BC8(p1), L);
        TS(9);
        float lrow; { const float a = __shfl(L[0], lane & 15), b = __shfl(L[1], lane & 15); lrow = (lane & 16) ? b : a; }
        if (__any(!(lrow < 1.0e18f))) {
            __builtin_amdgcn_fence(__ATOMIC_ACQUIRE, "agent");
            float m = -INFINITY, l = 0.f;
#pragma unroll
            for (int i = 0; i < 16; ++i) { O[i] = 0.f; negm[i] = 0.f; }
            const u16* Vs = (const u16*)Vg;
            for (int t = 0; t < 64; ++t) { const int key0 = t * 32;
                k0 = *(const bf16x8*)(Kg + key0 * 64 + lane * 16); k1 = *(const bf16x8*)(Kg + key0 * 64 + 1024 + lane * 16);
#pragma unroll
                for (int j = 0; j < 8; ++j) { const int kk = key0 + 8 * (j >> 2) + 4 * hi + (j & 3); v0[j] = (short)Vs[kk * 32 + (lane & 31)]; v1[j] = (short)Vs[(kk + 16) * 32 + (lane & 31)]; }
                f32x16 S = MFMA32(k0, q0, negm); S = MFMA32(k1, q1, S);
                float mt = S[0];
#pragma unroll
                for (int i = 1; i < 16; ++i) mt = fmaxf(mt, S[i]);
                mt = fmaxf(mt, __shfl_xor(mt, 32));
                const float mn = fmaxf(m, mt), al = __builtin_amdgcn_exp2f(m - mn); m = mn;
                float ps = 0.f;
#pragma unroll
                for (int i = 0; i < 16; ++i) { S[i] = rbf(__builtin_amdgcn_exp2f(S[i] - mn)); ps += S[i]; O[i] *= al; }
                l = l * al + ps + __shfl_xor(ps, 32);
#pragma unroll
                for (int i = 0; i < 4; ++i) { p0[i] = cvtpk(S[2 * i], S[2 * i + 1]); p1[i] = cvtpk(S[8 + 2 * i], S[9 + 2 * i]); }
                O = MFMA32(v0, BC8(p0), O); O = MFMA32(v1, BC8(p1), O);
            }
            lrow = l;
        }
        {
            const float inv = 1.0f / lrow;
            unsigned pk[8];
#pragma unroll
            for (int i = 0; i < 8; ++i) pk[i] = cvtpk(O[2 * i] * inv, O[2 * i + 1] * inv);
            const unsigned dst = WS_AH + ((qb * 16 + wid * 2 + ((lane >> 4) & 1)) * KB_AH + head) * 1024 + (lane & 15) * 16 + hi * 256;
#pragma unroll
            for (int c = 0; c < 2; ++c) {
                auto r0 = __builtin_amdgcn_permlane32_swap(pk[4 * c + 0], pk[4 * c + 2], false, false);
                auto r1 = __builtin_amdgcn_permlane32_swap(pk[4 * c + 1], pk[4 * c + 3], false, false);
                ST16(((u32x4){r0[0], r1[0], r0[1], r1[1]}), dst + c * 512);
            }
        }
        if (wid >= 4) { WAIT_V(2); }
        WAIT_L(0); BAR;
        TS(10);
#undef AT_ISSUE
#undef AT_STEP
#undef H_MAIN
#undef H_EXT
#undef EX
#undef BC8
    }

    {
        const int tid = opaque_tid(), lane = tid & 63, tile = rt2 * 8 + ct2;
        GAS u64* myflag = flags + F_G2 + tile * 2 + kh; const GAS u64* pflag = flags + F_G2 + tile * 2 + (kh ^ 1);
        if (wid >= 4) {
            const int lw = wid - 4;
            const unsigned vA = lw * 2 * KB_AH * 1024 + lane * 16, vB = (lw * 16 + (lane >> 3)) * (DM * 2) + (((lane & 7) ^ (2 * ((lane >> 4) & 3))) * 16);
            g128_issue(lds0, g2, 2, lw, vA, vB); g128_issue(lds0, g2, 3, lw, vA, vB);
            WAIT_V(16);
            for (int t = 0; t < NT2; ++t) {
                BAR;
                if (t == 0 && wid == 4 && lane == 0) flag_st(flags + F_AT + (idx & 7) * 32 + xcd * 4 + (idx >> 3), tag);
                if (t + 4 < NT2) {
                    if (t + 4 == g2.nsw) flag_wait(flags + F_AT + (rt2 >> 1) * 32 + (lane & 31), tag);
                    g128_issue(lds0, g2, t + 4, lw, vA, vB); WAIT_V(16);
                } else if (t + 3 < NT2) { WAIT_V(8); } else { WAIT_V(0); }
            }
            BAR;
            if (wid == 4) { if (lane == 0) flag_st(myflag, tag); flag_wait(pflag, tag); }
            BAR;
            if (bid == 0 && wid == 4) {
#pragma unroll
                for (int i = 0; i < 4; ++i) flag_wait(flags + F_LN + i * 64 + lane, tag);
                if (lane == 0) flag_st(flags, tag);
            }
        } else {
            const int wm = wid >> 1, wn = wid & 1, g = lane >> 4, q = (lane & 15) >> 2, p = lane & 3, swz = (2 * g + (q >> 1)) & 3;
            const int aoff = wm * 8192 + lane * 16;
            const int boff0 = 16384 + (4 * g + q) * 128 + (((2 * wn) ^ swz) * 32) + p * 8, boff1 = boff0 ^ 32;
            f32x4 acc[4][4]; bf16x8 AX[4], BX[4], AY[4], BY[4];
#define G_RD(A_, B_, slot, kk) do { const LAS char* s_ = lds + (slot); \
        _Pragma("unroll") for (int n = 0; n < 4; ++n) { const LAS char* b_ = s_ + ((n & 1) ? boff1 : boff0) + (n >> 1) * 8192 + (kk) * 4096; B_[n] = cat8(trd(b_), trd(b_ + 2048)); } \
        _Pragma("unroll") for (int m = 0; m < 4; ++m) A_[m] = *(const LAS bf16x8*)(s_ + aoff + (m * 2 + (kk)) * 1024); } while (0)
#define G_MM(A_, B_) do { _Pragma("unroll") for (int m = 0; m < 4; ++m) _Pragma("unroll") for (int n = 0; n < 4; ++n) acc[m][n] = MFMA16(B_[n], A_[m], acc[m][n]); } while (0)
#define G_IL() do { _Pragma("unroll") for (int i_ = 0; i_ < 12; ++i_) { __builtin_amdgcn_sched_group_barrier(0x100, 1, 0); __builtin_amdgcn_sched_group_barrier(0x8, 1, 0); } __builtin_amdgcn_sched_group_barrier(0x8, 4, 0); } while (0)
#pragma unroll
            for (int m = 0; m < 4; ++m)
#pragma unroll
                for (int n = 0; n < 4; ++n) acc[m][n] = (f32x4){0.f, 0.f, 0.f, 0.f};
            G_RD(AX, BX, 0, 0);
            for (int t = 0; t < NT2 - 1; ++t) {
                G_RD(AY, BY, (t & 3) * 32768, 1); G_MM(AX, BX); G_IL();
                SCHED; asm volatile("s_waitcnt vmcnt(0) lgkmcnt(0)" ::: "memory"); BAR; SCHED;
                G_RD(AX, BX, ((t + 1) & 3) * 32768, 0); G_MM(AY, BY); G_IL();
                SCHED;
            }
            G_RD(AY, BY, ((NT2 - 1) & 3) * 32768, 1); G_MM(AX, BX);
            SCHED; WAIT_L(0); BAR; SCHED;
            G_MM(AY, BY);
#undef G_RD
#undef G_MM
#undef G_IL
            TS(11);
            const int fr = lane & 15, fq = lane >> 4;
            const size_t rowo = (size_t)(rt2 * 128 + wm * 64 + kh * 32 + fr) * DM + ct2 * 128 + wn * 32 + (fq & 1) * 16 + (fq >> 1) * 8;
            const unsigned pbase = WS_PART + tile * 32768 + tid * 16;
            u32x4 xv[2][2]; f32x4 keep[2][4];
            if (kh == 0) {
#pragma unroll
                for (int m = 0; m < 2; ++m)
#pragma unroll
                    for (int s = 0; s < 2; ++s) { ST16(pack8(acc[2 + m][2 * s], acc[2 + m][2 * s + 1]), pbase + (m * 2 + s) * 4096);
                        keep[m][2 * s] = acc[m][2 * s]; keep[m][2 * s + 1] = acc[m][2 * s + 1]; }
            } else {
#pragma unroll
                for (int m = 0; m < 2; ++m)
#pragma unroll
                    for (int s = 0; s < 2; ++s) { ST16(pack8(acc[m][2 * s], acc[m][2 * s + 1]), pbase + 16384 + (m * 2 + s) * 4096);
                        keep[m][2 * s] = acc[2 + m][2 * s]; keep[m][2 * s + 1] = acc[2 + m][2 * s + 1]; }
            }
            WAIT_V(0); BAR;
            TS(12);
#pragma unroll
            for (int m = 0; m < 2; ++m)
#pragma unroll
                for (int s = 0; s < 2; ++s) xv[m][s] = *(const u32x4*)(x + rowo + (size_t)m * 16 * DM + s * 64);
            CBAR; BAR; CBAR;
            TS(13);
            u32x4 pv[2][2];
#pragma unroll
            for (int m = 0; m < 2; ++m)
#pragma unroll
                for (int s = 0; s < 2; ++s) pv[m][s] = LD16(pbase + (kh ^ 1) * 16384 + (m * 2 + s) * 4096);
#pragma unroll
            for (int m = 0; m < 2; ++m)
#pragma unroll
                for (int s = 0; s < 2; ++s) { float lo[4], hi4[4]; const u32x4 p_ = pv[m][s], x_ = xv[m][s];
                    const f32x4 a0 = keep[m][2 * s] + (f32x4){bf_lo(p_[0]), bf_hi(p_[0]), bf_lo(p_[1]), bf_hi(p_[1])}, a1 = keep[m][2 * s + 1] + (f32x4){bf_lo(p_[2]), bf_hi(p_[2]), bf_lo(p_[3]), bf_hi(p_[3])};
#pragma unroll
                    for (int i = 0; i < 4; ++i) { auto r = __builtin_amdgcn_permlane16_swap(__float_as_uint(a0[i]), __float_as_uint(a1[i]), false, false);
                        lo[i] = __uint_as_float(r[0]); hi4[i] = __uint_as_float(r[1]); }
                    u32x4 o; o[0] = cvtpk(lo[0] + bf_lo(x_[0]), lo[1] + bf_hi(x_[0])); o[1] = cvtpk(lo[2] + bf_lo(x_[1]), lo[3] + bf_hi(x_[1]));
                    o[2] = cvtpk(hi4[0] + bf_lo(x_[2]), hi4[1] + bf_hi(x_[2])); o[3] = cvtpk(hi4[2] + bf_lo(x_[3]), hi4[3] + bf_hi(x_[3]));
                    *(u32x4*)(out + rowo + (size_t)m * 16 * DM + s * 64) = o; }
        }
        TS(14); TR(31);
    }
}

extern "C" void kernel_launch(void* const* d_in, const int* in_sizes, int n_in, void* d_out, int out_size, void* d_ws, size_t ws_size, hipStream_t stream) {
    (void)hipFuncSetAttribute((const void*)k_block, hipFuncAttributeMaxDynamicSharedMemorySize, LDS_BYTES);
    k_block<<<256, 512, LDS_BYTES, stream>>>((const u16*)d_in[0], (const u16*)d_in[1], (const u16*)d_in[2], (const u16*)d_in[4], (const u16*)d_in[5], (const u16*)d_in[6], (char*)d_ws, (u16*)d_out);
}
```

```cpp
#include <hip/hip_runtime.h>
#include <hip/hip_bf16.h>
#include <stdint.h>

#define LAS __attribute__((address_space(3)))
#define GAS __attribute__((address_space(1)))
#define DI __device__ __forceinline__
typedef unsigned short u16;
typedef unsigned long long u64;
typedef short bf16x8 __attribute__((ext_vector_type(8)));
typedef short s16x4 __attribute__((ext_vector_type(4)));
typedef float f32x2 __attribute__((ext_vector_type(2)));
typedef float f32x4 __attribute__((ext_vector_type(4)));
typedef float f32x16 __attribute__((ext_vector_type(16)));
typedef unsigned u32x2 __attribute__((ext_vector_type(2)));
typedef unsigned u32x4 __attribute__((ext_vector_type(4)));
typedef __bf16 bf16x2_t __attribute__((ext_vector_type(2)));

constexpr int SEQ = 2048, DM = 1024, FF = 3584;
constexpr int KB_X = DM / 32;
constexpr int KB_AH = (DM + FF) / 32;
constexpr unsigned WS_XN = 0;
constexpr unsigned WS_Q = WS_XN + SEQ * DM * 2;
constexpr unsigned WS_K = WS_Q + SEQ * DM * 2;
constexpr unsigned WS_V = WS_K + SEQ * DM * 2;
constexpr unsigned WS_AH = WS_V + SEQ * DM * 2;
constexpr unsigned WS_PART = WS_AH + SEQ * (DM + FF) * 2;
constexpr unsigned WS_FLAG = WS_PART + 128 * 32768;
constexpr unsigned WS_END = WS_FLAG + 16384;
constexpr int F_LN = 32, F_QKV = F_LN + 256, F_H = F_QKV + 256, F_AT = F_H + 256, F_G2 = F_AT + 256;
constexpr int LDS_BYTES = 163840;

DI unsigned cvtpk(float lo, float hi) { f32x2 v = {lo, hi}; bf16x2_t b = __builtin_convertvector(v, bf16x2_t); return __builtin_bit_cast(unsigned, b); }
DI float bf_lo(unsigned u) { return __uint_as_float(u << 16); }
DI float bf_hi(unsigned u) { return __uint_as_float(u & 0xffff0000u); }
DI float rbf(float f) { return __uint_as_float(cvtpk(f, f) << 16); }
DI s16x4 trd(const LAS char* p) { return __builtin_amdgcn_ds_read_tr16_b64_v4i16((LAS s16x4*)p); }
DI bf16x8 cat8(s16x4 a, s16x4 b) { return __builtin_shufflevector(a, b, 0, 1, 2, 3, 4, 5, 6, 7); }
template <int OFF, int D> DI void glds(const char* base, unsigned voff, unsigned dst) {
    asm volatile("s_add_u32 m0, %2, %4\n\ts_nop 0\n\tglobal_load_lds_dwordx4 %0, %1 offset:%3" :: "v"(voff), "s"(base), "s"(dst), "i"(OFF), "i"(D - OFF) : "memory", "scc"); }
template <int D> DI void glds_pair_sc1(const char* base, unsigned voff, unsigned dst) {
    asm volatile("s_add_u32 m0, %2, %3\n\ts_nop 0\n\tglobal_load_lds_dwordx4 %0, %1 sc1\n\tglobal_load_lds_dwordx4 %0, %1 offset:1024 sc1" :: "v"(voff), "s"(base), "s"(dst), "i"(D) : "memory", "scc"); }
template <int D> DI void touch(const char* base, unsigned voff, unsigned dst) {
    asm volatile("s_add_u32 m0, %2, %3\n\ts_nop 0\n\tglobal_load_lds_dword %0, %1" :: "v"(voff), "s"(base), "s"(dst), "i"(D) : "memory", "scc"); }
#define WAIT_V(n) asm volatile("s_waitcnt vmcnt(" #n ")" ::: "memory")
#define WAIT_L(n) asm volatile("s_waitcnt lgkmcnt(" #n ")" ::: "memory")
#define BAR __builtin_amdgcn_s_barrier()
#define CBAR asm volatile("" ::: "memory")
#define SCHED __builtin_amdgcn_sched_barrier(0)
#define PIN(x) asm volatile("" : "+v"(x))
#define MFMA16(a, b, c) __builtin_amdgcn_mfma_f32_16x16x32_bf16(a, b, c, 0, 0, 0)
#define MFMA16_ACC(c, a, b) asm volatile("v_mfma_f32_16x16x32_bf16 %0, %1, %2, %0" : "+v"(c) : "v"(a), "v"(b))
#define MFMA32(a, b, c) __builtin_amdgcn_mfma_f32_32x32x16_bf16(a, b, c, 0, 0, 0)
#define ST16(v, off) __builtin_amdgcn_raw_buffer_store_b128(v, rs, (int)(off), 0, 16)
#define ST8(v, off) __builtin_amdgcn_raw_buffer_store_b64(v, rs, (int)(off), 0, 16)
#define LD16(off) __builtin_amdgcn_raw_buffer_load_b128(rs, (int)(off), 0, 16)
DI u64 flag_ld(const GAS u64* p) { return __hip_atomic_load(p, __ATOMIC_RELAXED, __HIP_MEMORY_SCOPE_AGENT); }
DI void flag_st(GAS u64* p, u64 v) { __hip_atomic_store(p, v, __ATOMIC_RELAXED, __HIP_MEMORY_SCOPE_AGENT); }
DI void flag_wait(const GAS u64* p, u64 tag) {
    for (unsigned spins = 0; spins < (1u << 18); ++spins) { if (__all(flag_ld(p) == tag)) break; __builtin_amdgcn_s_sleep(1); }
    CBAR;
}
DI float sum8lanes(float v) {
    v += __builtin_bit_cast(float, __builtin_amdgcn_update_dpp(0, __builtin_bit_cast(int, v), 0x128  , 0xf, 0xf, false));
    { auto r = __builtin_amdgcn_permlane16_swap(__float_as_uint(v), __float_as_uint(v), false, false); v = __uint_as_float(r[0]) + __uint_as_float(r[1]); }
    { auto r = __builtin_amdgcn_permlane32_swap(__float_as_uint(v), __float_as_uint(v), false, false); v = __uint_as_float(r[0]) + __uint_as_float(r[1]); }
    return v;
}
DI int opaque_tid() { int t = threadIdx.x; asm volatile("" : "+v"(t)); return t; }

#define TS(i)
#define TR(i)

DI float silu_mul(float g, float u) { return g * u * __builtin_amdgcn_rcpf(1.0f + __builtin_amdgcn_exp2f(g * -1.4426950408889634f)); }
DI u32x4 pack8(const f32x4& a, const f32x4& b) { u32x4 o; o[0] = cvtpk(a[0], a[1]); o[1] = cvtpk(a[2], a[3]); o[2] = cvtpk(b[0], b[1]); o[3] = cvtpk(b[2], b[3]); return o; }
constexpr float QSCALE = 0.17677669529663687f * 1.4426950408889634f;

struct G128 { const char* a0; const char* a1; const char* b0; const char* b1; int nsw; };
DI void g128_issue(unsigned lds0, const G128& g, int t, int slot, int lw, unsigned voffA, unsigned voffB) {
    const unsigned sa = lds0 + slot * 32768 + lw * 4096, sb = lds0 + slot * 32768 + lw * 2048;
    const char* a = (t < g.nsw ? g.a0 : g.a1) + (size_t)t * 2048; const char* b = (t < g.nsw ? g.b0 : g.b1) + (size_t)t * (64 * DM * 2);
    glds_pair_sc1<0>(a, voffA, sa); glds_pair_sc1<2048>(a + KB_AH * 1024, voffA, sa);
    glds<0, 16384>(b, voffB, sb); glds<128, 24576>(b, voffB, sb);
    glds<0, 16384 + 1024>(b + 8 * DM * 2, voffB, sb); glds<128, 24576 + 1024>(b + 8 * DM * 2, voffB, sb);
}

__global__ __launch_bounds__(512, 2) void k_block(const u16* __restrict__ x, const u16* __restrict__ wqkv, const u16* __restrict__ wo, const u16* __restrict__ w1,
                                                  const u16* __restrict__ w3, const u16* __restrict__ w2, char* __restrict__ ws, u16* __restrict__ out) {
    extern __shared__ __attribute__((aligned(16))) char smem[];
    LAS char* lds = (LAS char*)smem;
    const unsigned lds0 = (unsigned)(size_t)lds;
    const int wid = __builtin_amdgcn_readfirstlane(threadIdx.x >> 6);
    const int bid = blockIdx.x, xcd = bid & 7, idx = bid >> 3;
    GAS u64* flags = (GAS u64*)(ws + WS_FLAG);
    const __amdgpu_buffer_rsrc_t rs = __builtin_amdgcn_make_buffer_rsrc((void*)ws, 0, WS_END, 0x00020000);
    TR(30); TS(0);
    u64 tag;
    const int cj = idx >> 2, ct = cj < 3 ? (xcd >> 1) * 3 + cj : 12 + (xcd >> 1) * 5 + (cj - 3), rt = (xcd & 1) * 4 + (idx & 3);
    const bool qkv = ct < 12;
    u32x4 hmain[8]; u32x2 hext[4];

    {
        const int tid = opaque_tid(), lane = tid & 63, wr = wid >> 2, wc = wid & 3;
        const unsigned ldb2 = qkv ? 3 * DM * 2 : FF * 2;
        const char* cB0 = qkv ? (const char*)wqkv + ct * 512 : (const char*)w1 + (ct - 12) * 256;
        const char* cB1 = qkv ? cB0 + 256 : (const char*)w3 + (ct - 12) * 256;
        const char* cA = ws + WS_XN + rt * 16 * KB_X * 1024;
        const unsigned voffA = wid * (KB_X * 1024) + lane * 16;
        const unsigned voffB = (unsigned)(wid * 8 + (lane >> 3)) * ldb2 + (((lane & 7) ^ (2 * ((lane >> 4) & 3))) * 16);
        const char* pE = ((lane & 1) ? (const char*)w3 : (const char*)w1) + (size_t)(wid * 8 + (lane >> 3)) * (FF * 2) + (2560 + ct * 32) * 2 + ((((lane >> 1) & 3) ^ ((lane >> 4) & 3)) * 16);
        const size_t kstepB = (size_t)64 * ldb2;
        const unsigned ldsA = lds0 + wid * 2048, ldsB = lds0 + wid * 1024;
        const char* cT = ((idx & 2) ? cB1 : cB0) + (idx & 1) * 128;
#define SA(b, h) (((b) * 2 + (h)) * 16384)
#define SB(b, h) (((b) * 2 + (h)) * 16384)
#define STA(b, h, kt) glds_pair_sc1<SA(b, h)>(cA + (h) * 8 * KB_X * 1024 + (kt) * 2048, voffA, ldsA)
#define STB(b, h, kt) do { const char* s_ = ((h) ? cB1 : cB0) + (size_t)(kt) * kstepB; \
        glds<0, 65536 + SB(b, h)>(s_, voffB, ldsB); glds<128, 65536 + SB(b, h) + 8192>(s_, voffB, ldsB); } while (0)
#define TCH(kt) touch<148480>(cT + (size_t)(kt) * kstepB, voffB, ldsB)
#define STE(b, kt) do { const char* s_ = pE + (size_t)(kt) * (64 * FF * 2); \
        asm volatile("s_add_u32 m0, %1, %2\n\ts_nop 0\n\tglobal_load_lds_dwordx4 %0, off" :: "v"(s_), "s"(ldsB), "i"(131072 + (b) * 8192) : "memory", "scc"); } while (0)
        {
            LAS f32x2* red = (LAS f32x2*)(lds + 147456);
            const int r = lane & 7, g = (lane >> 3) & 3, kbl = lane >> 5, row = bid * 8 + r;
            float f[32];
            if (wid < 4) {
                const u64 epoch = flag_ld(flags);
                u32x2 v[4][2];
#pragma unroll
                for (int c = 0; c < 4; ++c) { const u16* p = x + (size_t)row * DM + (8 * wid + 2 * c + kbl) * 32 + 4 * g;
                    v[c][0] = *(const u32x2*)p; v[c][1] = *(const u32x2*)(p + 16); }
#pragma unroll
                for (int c = 0; c < 4; ++c)
#pragma unroll
                    for (int h = 0; h < 2; ++h) { f[c * 8 + h * 4 + 0] = bf_lo(v[c][h][0]); f[c * 8 + h * 4 + 1] = bf_hi(v[c][h][0]); f[c * 8 + h * 4 + 2] = bf_lo(v[c][h][1]); f[c * 8 + h * 4 + 3] = bf_hi(v[c][h][1]); }
                float s = 0.f;
#pragma unroll
                for (int i = 0; i < 32; ++i) s += f[i];
                PIN(s); TS(16);
                const float mw = sum8lanes(s) * (1.0f / 256.0f);
                float q = 0.f;
#pragma unroll
                for (int i = 0; i < 32; ++i) { const float d = f[i] - mw; q += d * d; }
                q = sum8lanes(q);
                if (lane < 8) red[wid * 8 + lane] = (f32x2){mw, q};
                if (lane == 8) *(LAS u64*)(red + 32) = epoch + 1;
                WAIT_L(0);
            } else {
#define STB2(b, h, kt) do { STB(b, h, kt); const char* s2_ = ((h) ? cB1 : cB0) + (size_t)(kt) * kstepB - 32 * (size_t)ldb2; \
        glds<0, 65536 + SB(b, h) - 4096>(s2_, voffB, ldsB); glds<128, 65536 + SB(b, h) + 8192 - 4096>(s2_, voffB, ldsB); } while (0)
#define STE2(b, kt) do { STE(b, kt); const char* s2_ = pE + (size_t)(kt) * (64 * FF * 2) - 32 * FF * 2; \
        asm volatile("s_add_u32 m0, %1, %2\n\ts_nop 0\n\tglobal_load_lds_dwordx4 %0, off" :: "v"(s2_), "s"(ldsB), "i"(131072 + (b) * 8192 - 4096) : "memory", "scc"); } while (0)
                STB2(0, 0, 0); STB2(0, 1, 0); STE2(0, 0);
            }
            TS(17);
            BAR; CBAR;
            TS(18);
            { const u32x2 e = *(LAS u32x2*)(red + 32); tag = ((u64)(unsigned)__builtin_amdgcn_readfirstlane(e[1]) << 32) | (unsigned)__builtin_amdgcn_readfirstlane(e[0]); }
            asm volatile("" : "+s"(tag));
            if (wid < 4) {
                const f32x2 a = red[r], b = red[8 + r], c = red[16 + r], d = red[24 + r];
                const float mean = (a.x + b.x + c.x + d.x) * 0.25f, da = a.x - mean, db = b.x - mean, dc = c.x - mean, dd = d.x - mean;
                const float var = ((a.y + b.y) + (c.y + d.y) + 256.0f * ((da * da + db * db) + (dc * dc + dd * dd))) * (1.0f / DM);
                const float mb = rbf(mean), sd = rbf(__builtin_amdgcn_sqrtf(rbf(rbf(var) + 1e-5f))), inv = __builtin_amdgcn_rcpf(sd);
#pragma unroll
                for (int c4 = 0; c4 < 4; ++c4) { u32x4 o;
#pragma unroll
                    for (int i = 0; i < 4; ++i) o[i] = cvtpk(rbf(f[c4 * 8 + 2 * i] - mb) * inv, rbf(f[c4 * 8 + 2 * i + 1] - mb) * inv);
                    ST16(o, WS_XN + ((row >> 4) * KB_X + 8 * wid + 2 * c4 + kbl) * 1024 + (g * 16 + (row & 15)) * 16); }
                TS(19);
                WAIT_V(0);
            } else { STB2(1, 0, 1); STB2(1, 1, 1); }
            TS(20);
            BAR;
            TS(1);
            if (wid == 0) { if (lane == 0) flag_st(flags + F_LN + bid, tag); flag_wait(flags + F_LN + rt * 32 + (lane & 31), tag); }
            else if (wid >= 4) { STE2(1, 1); TCH(2); TCH(3); touch<148480>(cT + 2 * kstepB - 32 * (size_t)ldb2, voffB, ldsB); touch<148480>(cT + 3 * kstepB - 32 * (size_t)ldb2, voffB, ldsB); }
#undef STB2
#undef STE2
            BAR; CBAR;
            TS(2);
        }
        f32x4 acc[2][2][4][2], ace[2][4];
        {
            const int g = lane >> 4, q = (lane & 15) >> 2, p = lane & 3, swz = (2 * g + (q >> 1)) & 3;
            const int aoff = wr * 8192 + lane * 16;
            const int boff0 = 65536 + (wc >> 1) * 8192 + (4 * g + q) * 128 + (((2 * (wc & 1)) ^ swz) * 32) + p * 8, boff1 = boff0 ^ 32;
            int eoff = 131072 + (4 * g + q) * 128 + ((wc ^ swz) * 32) + p * 8; PIN(eoff);
#define LDA(dst, b, h) do { _Pragma("unroll") for (int m = 0; m < 4; ++m) _Pragma("unroll") for (int k = 0; k < 2; ++k) \
        dst[m][k] = *(const LAS bf16x8*)(lds + aoff + SA(b, h) + (m * 2 + k) * 1024); } while (0)
#define LDB(dst, b, h) do { _Pragma("unroll") for (int n = 0; n < 2; ++n) _Pragma("unroll") for (int k = 0; k < 2; ++k) { \
        const LAS char* b_ = lds + (n ? boff1 : boff0) + SB(b, h) + k * 4096; dst[n][k] = cat8(trd(b_), trd(b_ + 2048)); } } while (0)
#define LDE(dst, b) do { _Pragma("unroll") for (int k = 0; k < 2; ++k) { const LAS char* b_ = lds + eoff + (b) * 8192 + k * 4096; dst[k] = cat8(trd(b_), trd(b_ + 2048)); } } while (0)
#define MMA(ai) do { __builtin_amdgcn_s_setprio(1); \
        _Pragma("unroll") for (int k = 0; k < 2; ++k) _Pragma("unroll") for (int m = 0; m < 4; ++m) { \
            acc[ai][0][m][0] = MFMA16(B0[0][k], At[m][k], acc[ai][0][m][0]); acc[ai][0][m][1] = MFMA16(B0[1][k], At[m][k], acc[ai][0][m][1]); \
            acc[ai][1][m][0] = MFMA16(B1[0][k], At[m][k], acc[ai][1][m][0]); acc[ai][1][m][1] = MFMA16(B1[1][k], At[m][k], acc[ai][1][m][1]); \
            ace[ai][m] = MFMA16(BE[k], At[m][k], ace[ai][m]); } \
        __builtin_amdgcn_s_setprio(0); } while (0)
            STA(0, 0, 0); STA(0, 1, 0); STA(1, 0, 1);
#pragma unroll
            for (int a = 0; a < 2; ++a)
#pragma unroll
                for (int m = 0; m < 4; ++m) { ace[a][m] = (f32x4){0.f, 0.f, 0.f, 0.f};
#pragma unroll
                    for (int b = 0; b < 2; ++b)
#pragma unroll
                        for (int n = 0; n < 2; ++n) acc[a][b][m][n] = (f32x4){0.f, 0.f, 0.f, 0.f}; }
            bf16x8 At[4][2], B0[2][2], B1[2][2], BE[2];
            constexpr int nt = DM / 64;
            if (wr == 1) BAR;
            WAIT_V(4); BAR;
            WAIT_V(2); BAR;
            TS(3);
            for (int t = 0; t < nt; t += 2) {
                const int t2 = t + 2 < nt ? t + 2 : t, tt = t + 4 < nt ? t + 4 : t;
                LDB(B0, 0, 0); LDB(B1, 0, 1); LDE(BE, 0); SCHED; LDA(At, 0, 0); STA(1, 1, t + 1); TCH(tt);
                WAIT_V(10); WAIT_L(0); BAR; MMA(0); BAR; SCHED;
                LDA(At, 0, 1); STB(0, 0, t2); STB(0, 1, t2); STE(0, t2); STA(0, 0, t2);
                WAIT_V(10); WAIT_L(0); BAR; MMA(1); BAR; SCHED;
                LDB(B0, 1, 0); LDB(B1, 1, 1); LDE(BE, 1); SCHED; LDA(At, 1, 0); STA(0, 1, t2); TCH(tt + 1);
                WAIT_V(10); WAIT_L(0); BAR; MMA(0); BAR; SCHED;
                LDA(At, 1, 1); STB(1, 0, t2 + 1); STB(1, 1, t2 + 1); STE(1, t2 + 1); STA(1, 0, t2 + 1);
                WAIT_V(10); WAIT_L(0); BAR; MMA(1); if (t + 2 < nt || wr == 0) BAR; SCHED;
            }
            TS(4);
#undef SA
#undef SB
#undef STA
#undef STB
#undef STE
#undef TCH
#undef LDA
#undef LDB
#undef LDE
#undef MMA
        }
        const int ln = opaque_tid() & 63, fr = ln & 15, fq = ln >> 4;
        if (qkv) {
            const int kind = ct >> 2;
            const float sc = kind == 0 ? QSCALE : 1.0f;
            const unsigned base = WS_Q + kind * (SEQ * DM * 2) + ((ct & 3) * 8 + wc) * 131072 + (rt * 256 + wr * 64) * 64;
            const unsigned lo0 = base + (kind == 2 ? fr * 64 + fq * 16 : fr * 16 + fq * 512), lo1 = lo0 + (kind == 2 ? 1024 : 256);
#pragma unroll
            for (int bj = 0; bj < 2; ++bj)
#pragma unroll
                for (int ai = 0; ai < 2; ++ai)
#pragma unroll
                    for (int m = 0; m < 4; ++m)
                        ST16(pack8(acc[ai][bj][m][0] * sc, acc[ai][bj][m][1] * sc), bj * 4 * 131072 + ai * 8192 + (m >> 1) * 2048 + ((m & 1) ? lo1 : lo0));
            WAIT_V(0); BAR;
            if (wid == 0 && ln == 0) flag_st(flags + F_QKV + rt * 32 + ct, tag);
        } else {
#pragma unroll
            for (int ai = 0; ai < 2; ++ai)
#pragma unroll
                for (int m = 0; m < 4; ++m) { f32x4 h0, h1;
#pragma unroll
                    for (int j = 0; j < 4; ++j) { h0[j] = silu_mul(acc[ai][0][m][0][j], acc[ai][1][m][0][j]); h1[j] = silu_mul(acc[ai][0][m][1][j], acc[ai][1][m][1][j]); }
                    hmain[ai * 4 + m] = pack8(h0, h1);
                }
        }
#pragma unroll
        for (int ai = 0; ai < 2; ++ai)
#pragma unroll
            for (int mp = 0; mp < 2; ++mp) { float h[4];
#pragma unroll
                for (int j = 0; j < 4; ++j) { auto r = __builtin_amdgcn_permlane32_swap(__float_as_uint(ace[ai][2 * mp][j]), __float_as_uint(ace[ai][2 * mp + 1][j]), false, false);
                    h[j] = silu_mul(__uint_as_float(r[0]), __uint_as_float(r[1])); }
                hext[ai * 2 + mp] = (u32x2){cvtpk(h[0], h[1]), cvtpk(h[2], h[3])};
            }
        TS(5);
        WAIT_V(0);
        TS(6);
    }

    const int kh = xcd & 1, rt2 = ((xcd >> 1) & 1) * 8 + (idx & 7), ct2 = (xcd >> 2) * 4 + (idx >> 3);
    constexpr int NT2 = 36;
    G128 g2;
    g2.nsw = kh ? NT2 : 20;
    g2.a0 = ws + WS_AH + (rt2 * 8 * KB_AH + (kh ? 72 : 32)) * 1024; g2.a1 = ws + WS_AH + rt2 * 8 * KB_AH * 1024 - 20 * 2048;
    g2.b0 = (const char*)w2 + ct2 * 256 + (kh ? 1280 * DM * 2 : 0); g2.b1 = (const char*)wo + ct2 * 256 - 20 * 64 * DM * 2;

    {
        constexpr int AT_BASE = 65536, AT_STAGE = 32768;
        const int lane = opaque_tid() & 63, hi = lane >> 5;
        const int head = xcd * 4 + (idx >> 3), qb = idx & 7;
        if (wid == 0) flag_wait(flags + F_QKV + (lane < 4 ? (lane & 1) * 32 + 4 + (lane >> 1) * 4 : qb * 32) + (head >> 3), tag);
        BAR; CBAR;
        TS(7);
        const char* Kg = ws + WS_K + head * 131072; const char* Vg = ws + WS_V + head * 131072;
        const unsigned soff = wid * 2048 + lane * 16;
        const unsigned sdst = lds0 + AT_BASE + wid * 2048;
#define AT_ISSUE(s, slot) do { glds_pair_sc1<0>(Kg + (s) * 16384, soff, sdst + (slot)); glds_pair_sc1<16384>(Vg + (s) * 16384, soff, sdst + (slot)); } while (0)
        const unsigned qoff = WS_Q + (head * 64 + qb * 8 + wid) * 2048 + lane * 16;
        const bf16x8 q0 = __builtin_bit_cast(bf16x8, LD16(qoff)), q1 = __builtin_bit_cast(bf16x8, LD16(qoff + 1024));
        AT_ISSUE(0, 0); AT_ISSUE(1, AT_STAGE);
        const GAS u64* kvflag = flags + F_QKV + (lane & 7) * 32 + 4 + ((lane >> 3) & 1) * 4 + (head >> 3);
        u64 seen = flag_ld(kvflag);
        bf16x8 sel; { const short one = ((lane & 15) == ((lane >> 4) & 1)) ? (short)0x3f80 : (short)0;
#pragma unroll
            for (int i = 0; i < 8; ++i) sel[i] = one; }
        const LAS char* kb0 = lds + AT_BASE + lane * 16;
        const LAS char* vb0 = lds + AT_BASE + 16384 + (4 * hi + ((lane & 15) >> 2)) * 64 + ((lane >> 4) & 1) * 32 + (lane & 3) * 8;
        f32x16 O, negm, SA, SB; f32x4 L = {0.f, 0.f, 0.f, 0.f};
        bf16x8 k0, k1, v0, v1;
#pragma unroll
        for (int i = 0; i < 16; ++i) { O[i] = 0.f; negm[i] = 0.f; }
        WAIT_V(5); BAR;
        TS(8);
        {
            k0 = *(const LAS bf16x8*)(kb0); k1 = *(const LAS bf16x8*)(kb0 + 1024);
            SA = MFMA32(k0, q0, negm); SA = MFMA32(k1, q1, SA);
            k0 = *(const LAS bf16x8*)(kb0 + 2048); k1 = *(const LAS bf16x8*)(kb0 + 3072);
            v0 = cat8(trd(vb0), trd(vb0 + 512)); v1 = cat8(trd(vb0 + 1024), trd(vb0 + 1536));
            float m = SA[0];
#pragma unroll
            for (int i = 1; i < 16; ++i) m = fmaxf(m, SA[i]);
            m = fmaxf(m, __shfl_xor(m, 32));
#pragma unroll
            for (int i = 0; i < 16; ++i) { negm[i] = -m; SA[i] -= m; }
            PIN(negm);
        }
#define EX(S, i) S[i] = __builtin_amdgcn_exp2f(S[i])
#define BC8(x) __builtin_bit_cast(bf16x8, x)
#define AT_STEP(SC, SN, kp, v1p, v0p) do { \
        O = MFMA32(v1, BC8(p1), O); SCHED; v1 = cat8(trd(v1p), trd((v1p) + 512)); EX(SC, 0); EX(SC, 1); EX(SC, 2); PIN(SC); SCHED; \
        SN = MFMA32(k0, q0, negm); SCHED; k0 = *(const LAS bf16x8*)(kp); EX(SC, 3); EX(SC, 4); EX(SC, 5); PIN(SC); SCHED; \
        L = MFMA16(sel, BC8(p1), L); SCHED; EX(SC, 6); EX(SC, 7); \
        p0[0] = cvtpk(SC[0], SC[1]); p0[1] = cvtpk(SC[2], SC[3]); p0[2] = cvtpk(SC[4], SC[5]); p0[3] = cvtpk(SC[6], SC[7]); PIN(p0); SCHED; \
        O = MFMA32(v0, BC8(p0), O); SCHED; v0 = cat8(trd(v0p), trd((v0p) + 512)); EX(SC, 8); EX(SC, 9); EX(SC, 10); PIN(SC); SCHED; \
        SN = MFMA32(k1, q1, SN); SCHED; k1 = *(const LAS bf16x8*)((kp) + 1024); EX(SC, 11); EX(SC, 12); EX(SC, 13); PIN(SC); SCHED; \
        L = MFMA16(sel, BC8(p0), L); SCHED; EX(SC, 14); EX(SC, 15); \
        p1[0] = cvtpk(SC[8], SC[9]); p1[1] = cvtpk(SC[10], SC[11]); p1[2] = cvtpk(SC[12], SC[13]); p1[3] = cvtpk(SC[14], SC[15]); PIN(p1); SCHED; } while (0)
        u32x4 p0, p1 = {0u, 0u, 0u, 0u};
        int cur = 0, nxt = AT_STAGE, fre = 2 * AT_STAGE;
        const GAS u64* hflag = flags + F_H + (rt2 >> 1) * 32 + (lane & 31);
        const unsigned hb = WS_AH + ((rt * 16 + (wid >> 2) * 4) * KB_AH + KB_X + (ct - 12) * 4 + (wid & 3)) * 1024 + lane * 16;
        const int cq = 2 * (wid & 3) + ((lane >> 4) & 1);
        const unsigned eb = WS_AH + ((rt * 16 + (wid >> 2) * 4 + hi) * KB_AH + KB_X + 80 + ct) * 1024 + ((cq & 3) * 16 + (lane & 15)) * 16 + (cq >> 2) * 8;
#define H_MAIN(j) do { if (!qkv && s < 4) { if (s == 0) ST16(hmain[j], hb + (j) * KB_AH * 1024); else if (s == 1) ST16(hmain[2 + (j)], hb + (2 + (j)) * KB_AH * 1024); \
            else if (s == 2) ST16(hmain[4 + (j)], hb + (8 + (j)) * KB_AH * 1024); else ST16(hmain[6 + (j)], hb + (10 + (j)) * KB_AH * 1024); } SCHED; } while (0)
#define H_EXT() do { if (s < 4) { if (s == 0) ST8(hext[0], eb); else if (s == 1) ST8(hext[1], eb + 2 * KB_AH * 1024); \
            else if (s == 2) ST8(hext[2], eb + 8 * KB_AH * 1024); else ST8(hext[3], eb + 10 * KB_AH * 1024); } SCHED; } while (0)
        for (int s = 0; s < 8; ++s) {
            if (s >= 1 && s <= 4) { if (qkv) { WAIT_V(2); } else { WAIT_V(4); } } else { WAIT_V(0); }
            WAIT_L(0); BAR;
            if (s == 0) { if (!__all(seen == tag)) flag_wait(kvflag, tag); }
            if (s + 2 < 8) AT_ISSUE(s + 2, fre);
            if (s < 4) {
                const int r0 = (idx * 8 + wid) * 9 + (s < 3 ? s * 4 : 0), rr = (kh ? 1280 : 0) + r0;
                const char* src = (kh == 0 && r0 >= 1280 ? (const char*)wo - 1280 * (DM * 2) : (const char*)w2) + (size_t)rr * (DM * 2) + (xcd >> 2) * 1024;
                const int rl = s == 2 ? 0 : (lane >> 4);
                touch<-AT_BASE>(src, ((kh == 0 && r0 < 1280 && r0 + rl >= 1280) ? 0 : rl) * (DM * 2) + (lane & 15) * 64, sdst);
            }
            if (s == 5) { if (wid == 0 && lane == 0) flag_st(flags + F_H + rt * 32 + ct, tag); }
            if (s == 6) seen = flag_ld(hflag);
            if (s == 7 && wid >= 4) {
                if (!__all(seen == tag)) flag_wait(hflag, tag);
                const unsigned vA = (wid - 4) * 2 * KB_AH * 1024 + lane * 16, vB = ((wid - 4) * 16 + (lane >> 3)) * (DM * 2) + (((lane & 7) ^ (2 * ((lane >> 4) & 3))) * 16);
                g128_issue(lds0, g2, 0, 0, wid - 4, vA, vB); g128_issue(lds0, g2, 1, 1, wid - 4, vA, vB);
            }
            if (wid >= 4) __builtin_amdgcn_s_setprio(1);
            const LAS char* kc = kb0 + cur; const LAS char* vc = vb0 + cur; const LAS char* kn = kb0 + nxt; const LAS char* vn = vb0 + nxt;
            AT_STEP(SA, SB, kc + 2 * 2048, vc + 1024, vc + 1 * 2048);            AT_STEP(SB, SA, kc + 3 * 2048, vc + 1 * 2048 + 1024, vc + 2 * 2048);
            H_MAIN(0);
            AT_STEP(SA, SB, kc + 4 * 2048, vc + 2 * 2048 + 1024, vc + 3 * 2048); AT_STEP(SB, SA, kc + 5 * 2048, vc + 3 * 2048 + 1024, vc + 4 * 2048);
            H_EXT();
            AT_STEP(SA, SB, kc + 6 * 2048, vc + 4 * 2048 + 1024, vc + 5 * 2048); __builtin_amdgcn_s_setprio(0); AT_STEP(SB, SA, kc + 7 * 2048, vc + 5 * 2048 + 1024, vc + 6 * 2048);
            H_MAIN(1);
            AT_STEP(SA, SB, kn, vc + 6 * 2048 + 1024, vc + 7 * 2048);            AT_STEP(SB, SA, kn + 2048, vc + 7 * 2048 + 1024, vn);
            const int tmp = cur; cur = nxt; nxt = fre; fre = tmp;
        }
        O = MFMA32(v1, BC8(p1), O); L = MFMA16(sel, BC8(p1), L);
        TS(9);
        float lrow; { const float a = __shfl(L[0], lane & 15), b = __shfl(L[1], lane & 15); lrow = (lane & 16) ? b : a; }
        if (__any(!(lrow < 1.0e18f))) {
            __builtin_amdgcn_fence(__ATOMIC_ACQUIRE, "agent");
            float m = -INFINITY, l = 0.f;
#pragma unroll
            for (int i = 0; i < 16; ++i) { O[i] = 0.f; negm[i] = 0.f; }
            const u16* Vs = (const u16*)Vg;
            for (int t = 0; t < 64; ++t) { const int key0 = t * 32;
                k0 = *(const bf16x8*)(Kg + key0 * 64 + lane * 16); k1 = *(const bf16x8*)(Kg + key0 * 64 + 1024 + lane * 16);
#pragma unroll
                for (int j = 0; j < 8; ++j) { const int kk = key0 + 8 * (j >> 2) + 4 * hi + (j & 3); v0[j] = (short)Vs[kk * 32 + (lane & 31)]; v1[j] = (short)Vs[(kk + 16) * 32 + (lane & 31)]; }
                f32x16 S = MFMA32(k0, q0, negm); S = MFMA32(k1, q1, S);
                float mt = S[0];
#pragma unroll
                for (int i = 1; i < 16; ++i) mt = fmaxf(mt, S[i]);
                mt = fmaxf(mt, __shfl_xor(mt, 32));
                const float mn = fmaxf(m, mt), al = __builtin_amdgcn_exp2f(m - mn); m = mn;
                float ps = 0.f;
#pragma unroll
                for (int i = 0; i < 16; ++i) { S[i] = rbf(__builtin_amdgcn_exp2f(S[i] - mn)); ps += S[i]; O[i] *= al; }
                l = l * al + ps + __shfl_xor(ps, 32);
#pragma unroll
                for (int i = 0; i < 4; ++i) { p0[i] = cvtpk(S[2 * i], S[2 * i + 1]); p1[i] = cvtpk(S[8 + 2 * i], S[9 + 2 * i]); }
                O = MFMA32(v0, BC8(p0), O); O = MFMA32(v1, BC8(p1), O);
            }
            lrow = l;
        }
        {
            const float inv = 1.0f / lrow;
            unsigned pk[8];
#pragma unroll
            for (int i = 0; i < 8; ++i) pk[i] = cvtpk(O[2 * i] * inv, O[2 * i + 1] * inv);
            const unsigned dst = WS_AH + ((qb * 16 + wid * 2 + ((lane >> 4) & 1)) * KB_AH + head) * 1024 + (lane & 15) * 16 + hi * 256;
#pragma unroll
            for (int c = 0; c < 2; ++c) {
                auto r0 = __builtin_amdgcn_permlane32_swap(pk[4 * c + 0], pk[4 * c + 2], false, false);
                auto r1 = __builtin_amdgcn_permlane32_swap(pk[4 * c + 1], pk[4 * c + 3], false, false);
                ST16(((u32x4){r0[0], r1[0], r0[1], r1[1]}), dst + c * 512);
            }
        }
        if (wid >= 4) { WAIT_V(2); }
        WAIT_L(0); BAR;
        TS(10);
#undef AT_ISSUE
#undef AT_STEP
#undef H_MAIN
#undef H_EXT
#undef EX
#undef BC8
    }

    {
        const int tid = opaque_tid(), lane = tid & 63, tile = rt2 * 8 + ct2;
        GAS u64* myflag = flags + F_G2 + tile * 2 + kh; const GAS u64* pflag = flags + F_G2 + tile * 2 + (kh ^ 1);
        if (wid >= 4) {
            const int lw = wid - 4;
            const unsigned vA = lw * 2 * KB_AH * 1024 + lane * 16, vB = (lw * 16 + (lane >> 3)) * (DM * 2) + (((lane & 7) ^ (2 * ((lane >> 4) & 3))) * 16);
            g128_issue(lds0, g2, 2, 2, lw, vA, vB); g128_issue(lds0, g2, 3, 3, lw, vA, vB);
            WAIT_V(16);
            int slot = 0;
            for (int t = 0; t < NT2; ++t) {
                BAR;
                if (t == 0 && wid == 4 && lane == 0) flag_st(flags + F_AT + (idx & 7) * 32 + xcd * 4 + (idx >> 3), tag);
                if (t + 4 < NT2) {
                    if (t + 4 == g2.nsw) flag_wait(flags + F_AT + (rt2 >> 1) * 32 + (lane & 31), tag);
                    g128_issue(lds0, g2, t + 4, slot, lw, vA, vB); slot = slot == 3 ? 0 : slot + 1;
                    WAIT_V(16);
                } else if (t + 3 < NT2) { WAIT_V(8); } else { WAIT_V(0); }
            }
            BAR;
            if (wid == 4) { if (lane == 0) flag_st(myflag, tag); flag_wait(pflag, tag); }
            BAR;
            if (bid == 0 && wid == 4) {
#pragma unroll
                for (int i = 0; i < 4; ++i) flag_wait(flags + F_LN + i * 64 + lane, tag);
                if (lane == 0) flag_st(flags, tag);
            }
        } else {
            const int wm = wid >> 1, wn = wid & 1, g = lane >> 4, q = (lane & 15) >> 2, p = lane & 3, swz = (2 * g + (q >> 1)) & 3;
            const int aoff = wm * 8192 + lane * 16;
            const int boff0 = 16384 + (4 * g + q) * 128 + (((2 * wn) ^ swz) * 32) + p * 8, boff1 = boff0 ^ 32;
            f32x4 acc[4][4]; bf16x8 AX[4], AY[4];
            s16x4 XL[4], XH[4], YL[4], YH[4];
            int pa[2] = {aoff, aoff + 65536}, pb0[2] = {boff0, boff0 + 65536}, pb1[2] = {boff1, boff1 + 65536};
            PIN(pa[1]); PIN(pb0[1]); PIN(pb1[1]);
#define G_BP(n, sl, kk) (lds + (((n) & 1) ? pb1[(sl) >> 1] : pb0[(sl) >> 1]) + ((sl) & 1) * 32768 + ((n) >> 1) * 8192 + (kk) * 4096)
#define G_BL(L_, n, sl, kk) do { L_[n] = trd(G_BP(n, sl, kk)); SCHED; } while (0)
#define G_BH(H_, n, sl, kk) do { H_[n] = trd(G_BP(n, sl, kk) + 2048); SCHED; } while (0)
#define G_A(A_, m, sl, kk) do { A_[m] = *(const LAS bf16x8*)(lds + pa[(sl) >> 1] + ((sl) & 1) * 32768 + ((m) * 2 + (kk)) * 1024); SCHED; } while (0)
#define G_M(A_, L_, H_, m, n) do { MFMA16_ACC(acc[m][n], cat8(L_[n], H_[n]), A_[m]); SCHED; } while (0)
#define G_REST(AC, CL, CH, AN, NL, NH, sl, kk) do { \
        G_BL(NL, 0, sl, kk); G_M(AC, CL, CH, 0, 2); G_BH(NH, 0, sl, kk); G_M(AC, CL, CH, 0, 3); G_A(AN, 0, sl, kk); G_M(AC, CL, CH, 1, 0); G_BL(NL, 1, sl, kk); G_M(AC, CL, CH, 1, 1); \
        G_BH(NH, 1, sl, kk); G_M(AC, CL, CH, 1, 2); G_BL(NL, 2, sl, kk); G_M(AC, CL, CH, 1, 3); G_BH(NH, 2, sl, kk); G_M(AC, CL, CH, 2, 0); G_BL(NL, 3, sl, kk); G_M(AC, CL, CH, 2, 1); \
        G_BH(NH, 3, sl, kk); G_M(AC, CL, CH, 2, 2); G_A(AN, 1, sl, kk); G_M(AC, CL, CH, 2, 3); G_A(AN, 2, sl, kk); G_M(AC, CL, CH, 3, 0); G_A(AN, 3, sl, kk); G_M(AC, CL, CH, 3, 1); \
        G_M(AC, CL, CH, 3, 2); G_M(AC, CL, CH, 3, 3); } while (0)
#define G_TILE(sl) do { G_M(AX, XL, XH, 0, 0); G_M(AX, XL, XH, 0, 1); G_REST(AX, XL, XH, AY, YL, YH, sl, 1); \
        G_M(AY, YL, YH, 0, 0); G_M(AY, YL, YH, 0, 1); \
        asm volatile("s_waitcnt vmcnt(0) lgkmcnt(0)" ::: "memory"); BAR; SCHED;        \
        G_REST(AY, YL, YH, AX, XL, XH, ((sl) + 1) & 3, 0); } while (0)
#pragma unroll
            for (int m = 0; m < 4; ++m)
#pragma unroll
                for (int n = 0; n < 4; ++n) { acc[m][n] = (f32x4){0.f, 0.f, 0.f, 0.f}; PIN(acc[m][n]); }
            SCHED;
#pragma unroll
            for (int i = 0; i < 4; ++i) { G_BL(XL, i, 0, 0); G_BH(XH, i, 0, 0); G_A(AX, i, 0, 0); }
            for (int t = 0; t < NT2; t += 4) { G_TILE(0); G_TILE(1); G_TILE(2); G_TILE(3); }
#undef G_BP
#undef G_BL
#undef G_BH
#undef G_A
#undef G_M
#undef G_REST
#undef G_TILE
            asm volatile("s_nop 7"); SCHED;
            TS(11);
            const int fr = lane & 15, fq = lane >> 4;
            const size_t rowo = (size_t)(rt2 * 128 + wm * 64 + kh * 32 + fr) * DM + ct2 * 128 + wn * 32 + (fq & 1) * 16 + (fq >> 1) * 8;
            const unsigned pbase = WS_PART + tile * 32768 + tid * 16;
            u32x4 xv[2][2]; f32x4 keep[2][4];
            if (kh == 0) {
#pragma unroll
                for (int m = 0; m < 2; ++m)
#pragma unroll
                    for (int s = 0; s < 2; ++s) { ST16(pack8(acc[2 + m][2 * s], acc[2 + m][2 * s + 1]), pbase + (m * 2 + s) * 4096);
                        keep[m][2 * s] = acc[m][2 * s]; keep[m][2 * s + 1] = acc[m][2 * s + 1]; }
            } else {
#pragma unroll
                for (int m = 0; m < 2; ++m)
#pragma unroll
                    for (int s = 0; s < 2; ++s) { ST16(pack8(acc[m][2 * s], acc[m][2 * s + 1]), pbase + 16384 + (m * 2 + s) * 4096);
                        keep[m][2 * s] = acc[2 + m][2 * s]; keep[m][2 * s + 1] = acc[2 + m][2 * s + 1]; }
            }
            WAIT_V(0); BAR;
            TS(12);
#pragma unroll
            for (int m = 0; m < 2; ++m)
#pragma unroll
                for (int s = 0; s < 2; ++s) xv[m][s] = *(const u32x4*)(x + rowo + (size_t)m * 16 * DM + s * 64);
            CBAR; BAR; CBAR;
            TS(13);
            u32x4 pv[2][2];
#pragma unroll
            for (int m = 0; m < 2; ++m)
#pragma unroll
                for (int s = 0; s < 2; ++s) pv[m][s] = LD16(pbase + (kh ^ 1) * 16384 + (m * 2 + s) * 4096);
#pragma unroll
            for (int m = 0; m < 2; ++m)
#pragma unroll
                for (int s = 0; s < 2; ++s) { float lo[4], hi4[4]; const u32x4 p_ = pv[m][s], x_ = xv[m][s];
                    const f32x4 a0 = keep[m][2 * s] + (f32x4){bf_lo(p_[0]), bf_hi(p_[0]), bf_lo(p_[1]), bf_hi(p_[1])}, a1 = keep[m][2 * s + 1] + (f32x4){bf_lo(p_[2]), bf_hi(p_[2]), bf_lo(p_[3]), bf_hi(p_[3])};
#pragma unroll
                    for (int i = 0; i < 4; ++i) { auto r = __builtin_amdgcn_permlane16_swap(__float_as_uint(a0[i]), __float_as_uint(a1[i]), false, false);
                        lo[i] = __uint_as_float(r[0]); hi4[i] = __uint_as_float(r[1]); }
                    u32x4 o; o[0] = cvtpk(lo[0] + bf_lo(x_[0]), lo[1] + bf_hi(x_[0])); o[1] = cvtpk(lo[2] + bf_lo(x_[1]), lo[3] + bf_hi(x_[1]));
                    o[2] = cvtpk(hi4[0] + bf_lo(x_[2]), hi4[1] + bf_hi(x_[2])); o[3] = cvtpk(hi4[2] + bf_lo(x_[3]), hi4[3] + bf_hi(x_[3]));
                    __builtin_nontemporal_store(o, (u32x4*)(out + rowo + (size_t)m * 16 * DM + s * 64));
                }
        }
        TS(14); TR(31);
    }
}

extern "C" void kernel_launch(void* const* d_in, const int* in_sizes, int n_in, void* d_out, int out_size, void* d_ws, size_t ws_size, hipStream_t stream) {
    (void)hipFuncSetAttribute((const void*)k_block, hipFuncAttributeMaxDynamicSharedMemorySize, LDS_BYTES);
    k_block<<<256, 512, LDS_BYTES, stream>>>((const u16*)d_in[0], (const u16*)d_in[1], (const u16*)d_in[2], (const u16*)d_in[4], (const u16*)d_in[5], (const u16*)d_in[6], (char*)d_ws, (u16*)d_out);
}
```

```cpp
#include <hip/hip_runtime.h>
#include <hip/hip_bf16.h>
#include <stdint.h>

#define LAS __attribute__((address_space(3)))
#define GAS __attribute__((address_space(1)))
#define DI __device__ __forceinline__
typedef unsigned short u16;
typedef unsigned long long u64;
typedef short bf16x8 __attribute__((ext_vector_type(8)));
typedef short s16x4 __attribute__((ext_vector_type(4)));
typedef float f32x2 __attribute__((ext_vector_type(2)));
typedef float f32x4 __attribute__((ext_vector_type(4)));
typedef float f32x16 __attribute__((ext_vector_type(16)));
typedef unsigned u32x2 __attribute__((ext_vector_type(2)));
typedef unsigned u32x4 __attribute__((ext_vector_type(4)));
typedef __bf16 bf16x2_t __attribute__((ext_vector_type(2)));

constexpr int SEQ = 2048, DM = 1024, FF = 3584;
constexpr int KB_X = DM / 32;
constexpr int KB_AH = (DM + FF) / 32;
constexpr unsigned WS_XN = 0;
constexpr unsigned WS_Q = WS_XN + SEQ * DM * 2;
constexpr unsigned WS_K = WS_Q + SEQ * DM * 2;
constexpr unsigned WS_V = WS_K + SEQ * DM * 2;
constexpr unsigned WS_AH = WS_V + SEQ * DM * 2;
constexpr unsigned WS_PART = WS_AH + SEQ * (DM + FF) * 2;
constexpr unsigned WS_FLAG = WS_PART + 128 * 32768;
constexpr unsigned WS_END = WS_FLAG + 16384;
constexpr int F_LN = 32, F_QKV = F_LN + 256, F_H = F_QKV + 256, F_AT = F_H + 256, F_G2 = F_AT + 256;
constexpr int LDS_BYTES = 163840;

DI unsigned cvtpk(float lo, float hi) { f32x2 v = {lo, hi}; bf16x2_t b = __builtin_convertvector(v, bf16x2_t); return __builtin_bit_cast(unsigned, b); }
DI float bf_lo(unsigned u) { return __uint_as_float(u << 16); }
DI float bf_hi(unsigned u) { return __uint_as_float(u & 0xffff0000u); }
DI float rbf(float f) { return __uint_as_float(cvtpk(f, f) << 16); }
DI s16x4 trd(const LAS char* p) { return __builtin_amdgcn_ds_read_tr16_b64_v4i16((LAS s16x4*)p); }
DI bf16x8 cat8(s16x4 a, s16x4 b) { return __builtin_shufflevector(a, b, 0, 1, 2, 3, 4, 5, 6, 7); }
template <int OFF, int D> DI void glds(const char* base, unsigned voff, unsigned dst) {
    asm volatile("s_add_u32 m0, %2, %4\n\ts_nop 0\n\tglobal_load_lds_dwordx4 %0, %1 offset:%3" :: "v"(voff), "s"(base), "s"(dst), "i"(OFF), "i"(D - OFF) : "memory", "scc"); }
template <int D> DI void glds_pair_sc1(const char* base, unsigned voff, unsigned dst) {
    asm volatile("s_add_u32 m0, %2, %3\n\ts_nop 0\n\tglobal_load_lds_dwordx4 %0, %1 sc1\n\tglobal_load_lds_dwordx4 %0, %1 offset:1024 sc1" :: "v"(voff), "s"(base), "s"(dst), "i"(D) : "memory", "scc"); }
template <int D> DI void touch(const char* base, unsigned voff, unsigned dst) {
    asm volatile("s_add_u32 m0, %2, %3\n\ts_nop 0\n\tglobal_load_lds_dword %0, %1" :: "v"(voff), "s"(base), "s"(dst), "i"(D) : "memory", "scc"); }
#define WAIT_V(n) asm volatile("s_waitcnt vmcnt(" #n ")" ::: "memory")
#define WAIT_L(n) asm volatile("s_waitcnt lgkmcnt(" #n ")" ::: "memory")
#define BAR __builtin_amdgcn_s_barrier()
#define CBAR asm volatile("" ::: "memory")
#define SCHED __builtin_amdgcn_sched_barrier(0)
#define PIN(x) asm volatile("" : "+v"(x))
#define MFMA16(a, b, c) __builtin_amdgcn_mfma_f32_16x16x32_bf16(a, b, c, 0, 0, 0)
#define MFMA16_ACC(c, a, b) asm volatile("v_mfma_f32_16x16x32_bf16 %0, %1, %2, %0" : "+v"(c) : "v"(a), "v"(b))
#define MFMA32(a, b, c) __builtin_amdgcn_mfma_f32_32x32x16_bf16(a, b, c, 0, 0, 0)
#define ST16(v, off) __builtin_amdgcn_raw_buffer_store_b128(v, rs, (int)(off), 0, 16)
#define ST8(v, off) __builtin_amdgcn_raw_buffer_store_b64(v, rs, (int)(off), 0, 16)
#define LD16(off) __builtin_amdgcn_raw_buffer_load_b128(rs, (int)(off), 0, 16)
DI u64 flag_ld(const GAS u64* p) { return __hip_atomic_load(p, __ATOMIC_RELAXED, __HIP_MEMORY_SCOPE_AGENT); }
DI void flag_st(GAS u64* p, u64 v) { __hip_atomic_store(p, v, __ATOMIC_RELAXED, __HIP_MEMORY_SCOPE_AGENT); }
DI void flag_wait(const GAS u64* p, u64 tag) {
    for (unsigned spins = 0; spins < (1u << 18); ++spins) { if (__all(flag_ld(p) == tag)) break; __builtin_amdgcn_s_sleep(1); }
    CBAR;
}
template <int N> DI float ror_add(float v) { return v + __builtin_bit_cast(float, __builtin_amdgcn_update_dpp(0, __builtin_bit_cast(int, v), 0x120 + N  , 0xf, 0xf, false)); }
DI float sum64(float v) {
    v = ror_add<1>(ror_add<2>(ror_add<4>(ror_add<8>(v))));
    { auto r = __builtin_amdgcn_permlane16_swap(__float_as_uint(v), __float_as_uint(v), false, false); v = __uint_as_float(r[0]) + __uint_as_float(r[1]); }
    { auto r = __builtin_amdgcn_permlane32_swap(__float_as_uint(v), __float_as_uint(v), false, false); v = __uint_as_float(r[0]) + __uint_as_float(r[1]); }
    return v;
}
DI int opaque_tid() { int t = threadIdx.x; asm volatile("" : "+v"(t)); return t; }

#define TS(i)
#define TR(i)

DI float silu_mul(float g, float u) { return g * u * __builtin_amdgcn_rcpf(1.0f + __builtin_amdgcn_exp2f(g * -1.4426950408889634f)); }
DI u32x4 pack8(const f32x4& a, const f32x4& b) { u32x4 o; o[0] = cvtpk(a[0], a[1]); o[1] = cvtpk(a[2], a[3]); o[2] = cvtpk(b[0], b[1]); o[3] = cvtpk(b[2], b[3]); return o; }
constexpr float QSCALE = 0.17677669529663687f * 1.4426950408889634f;

struct G128 { const char* a0; const char* a1; const char* b0; const char* b1; int nsw; };
DI void g128_issue(unsigned lds0, const G128& g, int t, int slot, int lw, unsigned voffA, unsigned voffB) {
    const unsigned sa = lds0 + slot * 32768 + lw * 4096, sb = lds0 + slot * 32768 + lw * 2048;
    const char* a = (t < g.nsw ? g.a0 : g.a1) + (size_t)t * 2048; const char* b = (t < g.nsw ? g.b0 : g.b1) + (size_t)t * (64 * DM * 2);
    glds_pair_sc1<0>(a, voffA, sa); glds_pair_sc1<2048>(a + KB_AH * 1024, voffA, sa);
    glds<0, 16384>(b, voffB, sb); glds<128, 24576>(b, voffB, sb);
    glds<0, 16384 + 1024>(b + 8 * DM * 2, voffB, sb); glds<128, 24576 + 1024>(b + 8 * DM * 2, voffB, sb);
}

__global__ __launch_bounds__(512, 2) void k_block(const u16* __restrict__ x, const u16* __restrict__ wqkv, const u16* __restrict__ wo, const u16* __restrict__ w1,
                                                  const u16* __restrict__ w3, const u16* __restrict__ w2, char* __restrict__ ws, u16* __restrict__ out) {
    extern __shared__ __attribute__((aligned(16))) char smem[];
    LAS char* lds = (LAS char*)smem;
    const unsigned lds0 = (unsigned)(size_t)lds;
    const int wid = __builtin_amdgcn_readfirstlane(threadIdx.x >> 6);
    const int bid = blockIdx.x, xcd = bid & 7, idx = bid >> 3;
    GAS u64* flags = (GAS u64*)(ws + WS_FLAG);
    const __amdgpu_buffer_rsrc_t rs = __builtin_amdgcn_make_buffer_rsrc((void*)ws, 0, WS_END, 0x00020000);
    TR(30); TS(0);
    u64 tag;
    asm volatile("s_load_dwordx2 %0, %1, 0x0" : "=s"(tag) : "s"(ws + WS_FLAG) : "memory");
    const int cj = idx >> 2, ct = cj < 3 ? (xcd >> 1) * 3 + cj : 12 + (xcd >> 1) * 5 + (cj - 3), rt = (xcd & 1) * 4 + (idx & 3);
    const bool qkv = ct < 12;
    u32x4 hmain[8]; u32x2 hext[4];

    {
        const int tid = opaque_tid(), lane = tid & 63, wr = wid >> 2, wc = wid & 3;
        const unsigned ldb2 = qkv ? 3 * DM * 2 : FF * 2;
        const char* cB0 = qkv ? (const char*)wqkv + ct * 512 : (const char*)w1 + (ct - 12) * 256;
        const char* cB1 = qkv ? cB0 + 256 : (const char*)w3 + (ct - 12) * 256;
        const char* cA = ws + WS_XN + rt * 16 * KB_X * 1024;
        const unsigned voffA = wid * (KB_X * 1024) + lane * 16;
        const unsigned voffB = (unsigned)(wid * 8 + (lane >> 3)) * ldb2 + (((lane & 7) ^ (2 * ((lane >> 4) & 3))) * 16);
        const char* pE = ((lane & 1) ? (const char*)w3 : (const char*)w1) + (size_t)(wid * 8 + (lane >> 3)) * (FF * 2) + (2560 + ct * 32) * 2 + ((((lane >> 1) & 3) ^ ((lane >> 4) & 3)) * 16);
        const size_t kstepB = (size_t)64 * ldb2;
        const unsigned ldsA = lds0 + wid * 2048, ldsB = lds0 + wid * 1024;
        const char* cT = ((idx & 2) ? cB1 : cB0) + (idx & 1) * 128;
#define SA(b, h) (((b) * 2 + (h)) * 16384)
#define SB(b, h) (((b) * 2 + (h)) * 16384)
#define STA(b, h, kt) glds_pair_sc1<SA(b, h)>(cA + (h) * 8 * KB_X * 1024 + (kt) * 2048, voffA, ldsA)
#define STB(b, h, kt) do { const char* s_ = ((h) ? cB1 : cB0) + (size_t)(kt) * kstepB; \
        glds<0, 65536 + SB(b, h)>(s_, voffB, ldsB); glds<128, 65536 + SB(b, h) + 8192>(s_, voffB, ldsB); } while (0)
#define TCH(kt) touch<148480>(cT + (size_t)(kt) * kstepB, voffB, ldsB)
#define STE(b, kt) do { const char* s_ = pE + (size_t)(kt) * (64 * FF * 2); \
        asm volatile("s_add_u32 m0, %1, %2\n\ts_nop 0\n\tglobal_load_lds_dwordx4 %0, off" :: "v"(s_), "s"(ldsB), "i"(131072 + (b) * 8192) : "memory", "scc"); } while (0)
        {
            u32x4 xa, xb;
            { const char* xp = (const char*)x + (size_t)(bid * 8 + wid) * (DM * 2);
              asm volatile("global_load_dwordx4 %0, %2, %3\n\tglobal_load_dwordx4 %1, %2, %3 offset:1024" : "=&v"(xa), "=&v"(xb) : "v"(lane * 16), "s"(xp) : "memory"); }
            if (wid == 7 && idx < 6) { const char* pc; asm volatile("s_getpc_b64 %0" : "=s"(pc));
              touch<148480>(pc + idx * 4096, lane * 64, ldsB); }
            STB(0, 0, 0); STB(0, 1, 0); STE(0, 0);
            asm volatile("s_waitcnt vmcnt(5) lgkmcnt(0)" : "+v"(xa), "+v"(xb), "+s"(tag) :: "memory");
            tag += 1;
            float f[16];
#pragma unroll
            for (int i = 0; i < 4; ++i) { f[2 * i] = bf_lo(xa[i]); f[2 * i + 1] = bf_hi(xa[i]); f[8 + 2 * i] = bf_lo(xb[i]); f[9 + 2 * i] = bf_hi(xb[i]); }
            float s = 0.f;
#pragma unroll
            for (int i = 0; i < 16; ++i) s += f[i];
            const float mean = sum64(s) * (1.0f / DM);
            float q = 0.f;
#pragma unroll
            for (int i = 0; i < 16; ++i) { const float d = f[i] - mean; q += d * d; }
            const float var = sum64(q) * (1.0f / DM);
            const float mb = rbf(mean), sd = rbf(__builtin_amdgcn_sqrtf(rbf(rbf(var) + 1e-5f))), inv = __builtin_amdgcn_rcpf(sd);
#pragma unroll
            for (int c = 0; c < 2; ++c) { u32x4 o;
#pragma unroll
                for (int i = 0; i < 4; ++i) o[i] = cvtpk(rbf(f[c * 8 + 2 * i] - mb) * inv, rbf(f[c * 8 + 2 * i + 1] - mb) * inv);
                *(LAS u32x4*)(lds + wid * 2080 + c * 1024 + lane * 16) = o; }
            WAIT_L(0); BAR; CBAR;
            const int r = lane & 7, g = (lane >> 3) & 3, row = bid * 8 + r;
#pragma unroll
            for (int c = 0; c < 2; ++c) { const int kb = 4 * wid + 2 * c + (lane >> 5); const LAS char* p = lds + r * 2080 + kb * 64 + g * 8;
                const u32x2 lo = *(const LAS u32x2*)p, hi = *(const LAS u32x2*)(p + 32);
                ST16(((u32x4){lo[0], lo[1], hi[0], hi[1]}), WS_XN + ((row >> 4) * KB_X + kb) * 1024 + (g * 16 + (row & 15)) * 16); }
            WAIT_V(0); BAR;
            TS(1);
            if (wid == 0) { if (lane == 0) flag_st(flags + F_LN + bid, tag); flag_wait(flags + F_LN + rt * 32 + (lane & 31), tag); }
            STB(1, 0, 1); STB(1, 1, 1); STE(1, 1);
            if (wid != 0) { TCH(2); TCH(3); }
            BAR; CBAR;
            TS(2);
        }
        f32x4 acc[2][2][4][2], ace[2][4];
        {
            const int g = lane >> 4, q = (lane & 15) >> 2, p = lane & 3, swz = (2 * g + (q >> 1)) & 3;
            const int aoff = wr * 8192 + lane * 16;
            const int boff0 = 65536 + (wc >> 1) * 8192 + (4 * g + q) * 128 + (((2 * (wc & 1)) ^ swz) * 32) + p * 8, boff1 = boff0 ^ 32;
            int eoff = 131072 + (4 * g + q) * 128 + ((wc ^ swz) * 32) + p * 8; PIN(eoff);
#define LDA(dst, b, h) do { _Pragma("unroll") for (int m = 0; m < 4; ++m) _Pragma("unroll") for (int k = 0; k < 2; ++k) \
        dst[m][k] = *(const LAS bf16x8*)(lds + aoff + SA(b, h) + (m * 2 + k) * 1024); } while (0)
#define LDB(dst, b, h) do { _Pragma("unroll") for (int n = 0; n < 2; ++n) _Pragma("unroll") for (int k = 0; k < 2; ++k) { \
        const LAS char* b_ = lds + (n ? boff1 : boff0) + SB(b, h) + k * 4096; dst[n][k] = cat8(trd(b_), trd(b_ + 2048)); } } while (0)
#define LDE(dst, b) do { _Pragma("unroll") for (int k = 0; k < 2; ++k) { const LAS char* b_ = lds + eoff + (b) * 8192 + k * 4096; dst[k] = cat8(trd(b_), trd(b_ + 2048)); } } while (0)
#define MMA(ai) do { __builtin_amdgcn_s_setprio(1); \
        _Pragma("unroll") for (int k = 0; k < 2; ++k) _Pragma("unroll") for (int m = 0; m < 4; ++m) { \
            acc[ai][0][m][0] = MFMA16(B0[0][k], At[m][k], acc[ai][0][m][0]); acc[ai][0][m][1] = MFMA16(B0[1][k], At[m][k], acc[ai][0][m][1]); \
            acc[ai][1][m][0] = MFMA16(B1[0][k], At[m][k], acc[ai][1][m][0]); acc[ai][1][m][1] = MFMA16(B1[1][k], At[m][k], acc[ai][1][m][1]); \
            ace[ai][m] = MFMA16(BE[k], At[m][k], ace[ai][m]); } \
        __builtin_amdgcn_s_setprio(0); } while (0)
            STA(0, 0, 0); STA(0, 1, 0); STA(1, 0, 1);
#pragma unroll
            for (int a = 0; a < 2; ++a)
#pragma unroll
                for (int m = 0; m < 4; ++m) { ace[a][m] = (f32x4){0.f, 0.f, 0.f, 0.f};
#pragma unroll
                    for (int b = 0; b < 2; ++b)
#pragma unroll
                        for (int n = 0; n < 2; ++n) acc[a][b][m][n] = (f32x4){0.f, 0.f, 0.f, 0.f}; }
            bf16x8 At[4][2], B0[2][2], B1[2][2], BE[2];
            constexpr int nt = DM / 64;
            if (wr == 1) BAR;
            WAIT_V(4); BAR;
            WAIT_V(2); BAR;
            TS(3);
            for (int t = 0; t < nt; t += 2) {
                const int t2 = t + 2 < nt ? t + 2 : t, tt = t + 4 < nt ? t + 4 : t;
                LDB(B0, 0, 0); LDB(B1, 0, 1); LDE(BE, 0); SCHED; LDA(At, 0, 0); STA(1, 1, t + 1); TCH(tt);
                WAIT_V(10); WAIT_L(0); BAR; MMA(0); BAR; SCHED;
                LDA(At, 0, 1); STB(0, 0, t2); STB(0, 1, t2); STE(0, t2); STA(0, 0, t2);
                WAIT_V(10); WAIT_L(0); BAR; MMA(1); BAR; SCHED;
                LDB(B0, 1, 0); LDB(B1, 1, 1); LDE(BE, 1); SCHED; LDA(At, 1, 0); STA(0, 1, t2); TCH(tt + 1);
                WAIT_V(10); WAIT_L(0); BAR; MMA(0); BAR; SCHED;
                LDA(At, 1, 1); STB(1, 0, t2 + 1); STB(1, 1, t2 + 1); STE(1, t2 + 1); STA(1, 0, t2 + 1);
                WAIT_V(10); WAIT_L(0); BAR; MMA(1); if (t + 2 < nt || wr == 0) BAR; SCHED;
            }
            TS(4);
#undef SA
#undef SB
#undef STA
#undef STB
#undef STE
#undef TCH
#undef LDA
#undef LDB
#undef LDE
#undef MMA
        }
        const int ln = opaque_tid() & 63, fr = ln & 15, fq = ln >> 4;
        if (qkv) {
            const int kind = ct >> 2;
            const float sc = kind == 0 ? QSCALE : 1.0f;
            const unsigned base = WS_Q + kind * (SEQ * DM * 2) + ((ct & 3) * 8 + wc) * 131072 + (rt * 256 + wr * 64) * 64;
            const unsigned lo0 = base + (kind == 2 ? fr * 64 + fq * 16 : fr * 16 + fq * 512), lo1 = lo0 + (kind == 2 ? 1024 : 256);
#pragma unroll
            for (int bj = 0; bj < 2; ++bj)
#pragma unroll
                for (int ai = 0; ai < 2; ++ai)
#pragma unroll
                    for (int m = 0; m < 4; ++m)
                        ST16(pack8(acc[ai][bj][m][0] * sc, acc[ai][bj][m][1] * sc), bj * 4 * 131072 + ai * 8192 + (m >> 1) * 2048 + ((m & 1) ? lo1 : lo0));
            WAIT_V(0); BAR;
            if (wid == 0 && ln == 0) flag_st(flags + F_QKV + rt * 32 + ct, tag);
        } else {
#pragma unroll
            for (int ai = 0; ai < 2; ++ai)
#pragma unroll
                for (int m = 0; m < 4; ++m) { f32x4 h0, h1;
#pragma unroll
                    for (int j = 0; j < 4; ++j) { h0[j] = silu_mul(acc[ai][0][m][0][j], acc[ai][1][m][0][j]); h1[j] = silu_mul(acc[ai][0][m][1][j], acc[ai][1][m][1][j]); }
                    hmain[ai * 4 + m] = pack8(h0, h1);
                }
        }
#pragma unroll
        for (int ai = 0; ai < 2; ++ai)
#pragma unroll
            for (int mp = 0; mp < 2; ++mp) { float h[4];
#pragma unroll
                for (int j = 0; j < 4; ++j) { auto r = __builtin_amdgcn_permlane32_swap(__float_as_uint(ace[ai][2 * mp][j]), __float_as_uint(ace[ai][2 * mp + 1][j]), false, false);
                    h[j] = silu_mul(__uint_as_float(r[0]), __uint_as_float(r[1])); }
                hext[ai * 2 + mp] = (u32x2){cvtpk(h[0], h[1]), cvtpk(h[2], h[3])};
            }
        TS(5);
        WAIT_V(0);
        TS(6);
    }

    const int kh = xcd & 1, rt2 = ((xcd >> 1) & 1) * 8 + (idx & 7), ct2 = (xcd >> 2) * 4 + (idx >> 3);
    constexpr int NT2 = 36;
    G128 g2;
    g2.nsw = kh ? NT2 : 20;
    g2.a0 = ws + WS_AH + (rt2 * 8 * KB_AH + (kh ? 72 : 32)) * 1024; g2.a1 = ws + WS_AH + rt2 * 8 * KB_AH * 1024 - 20 * 2048;
    g2.b0 = (const char*)w2 + ct2 * 256 + (kh ? 1280 * DM * 2 : 0); g2.b1 = (const char*)wo + ct2 * 256 - 20 * 64 * DM * 2;

    {
        constexpr int AT_BASE = 65536, AT_STAGE = 32768;
        const int lane = opaque_tid() & 63, hi = lane >> 5;
        const int head = xcd * 4 + (idx >> 3), qb = idx & 7;
        if (wid == 0) flag_wait(flags + F_QKV + (lane < 4 ? (lane & 1) * 32 + 4 + (lane >> 1) * 4 : qb * 32) + (head >> 3), tag);
        BAR; CBAR;
        TS(7);
        const char* Kg = ws + WS_K + head * 131072; const char* Vg = ws + WS_V + head * 131072;
        const unsigned soff = wid * 2048 + lane * 16;
        const unsigned sdst = lds0 + AT_BASE + wid * 2048;
#define AT_ISSUE(s, slot) do { glds_pair_sc1<0>(Kg + (s) * 16384, soff, sdst + (slot)); glds_pair_sc1<16384>(Vg + (s) * 16384, soff, sdst + (slot)); } while (0)
        const unsigned qoff = WS_Q + (head * 64 + qb * 8 + wid) * 2048 + lane * 16;
        const bf16x8 q0 = __builtin_bit_cast(bf16x8, LD16(qoff)), q1 = __builtin_bit_cast(bf16x8, LD16(qoff + 1024));
        AT_ISSUE(0, 0); AT_ISSUE(1, AT_STAGE);
        const GAS u64* kvflag = flags + F_QKV + (lane & 7) * 32 + 4 + ((lane >> 3) & 1) * 4 + (head >> 3);
        u64 seen = flag_ld(kvflag);
        bf16x8 sel; { const short one = ((lane & 15) == ((lane >> 4) & 1)) ? (short)0x3f80 : (short)0;
#pragma unroll
            for (int i = 0; i < 8; ++i) sel[i] = one; }
        const LAS char* kb0 = lds + AT_BASE + lane * 16;
        const LAS char* vb0 = lds + AT_BASE + 16384 + (4 * hi + ((lane & 15) >> 2)) * 64 + ((lane >> 4) & 1) * 32 + (lane & 3) * 8;
        f32x16 O, negm, SA, SB; f32x4 L = {0.f, 0.f, 0.f, 0.f};
        bf16x8 k0, k1, v0, v1;
#pragma unroll
        for (int i = 0; i < 16; ++i) { O[i] = 0.f; negm[i] = 0.f; }
        WAIT_V(5); BAR;
        TS(8);
        {
            k0 = *(const LAS bf16x8*)(kb0); k1 = *(const LAS bf16x8*)(kb0 + 1024);
            SA = MFMA32(k0, q0, negm); SA = MFMA32(k1, q1, SA);
            k0 = *(const LAS bf16x8*)(kb0 + 2048); k1 = *(const LAS bf16x8*)(kb0 + 3072);
            v0 = cat8(trd(vb0), trd(vb0 + 512)); v1 = cat8(trd(vb0 + 1024), trd(vb0 + 1536));
            float m = SA[0];
#pragma unroll
            for (int i = 1; i < 16; ++i) m = fmaxf(m, SA[i]);
            m = fmaxf(m, __shfl_xor(m, 32));
#pragma unroll
            for (int i = 0; i < 16; ++i) { negm[i] = -m; SA[i] -= m; }
            PIN(negm);
        }
#define EX(S, i) S[i] = __builtin_amdgcn_exp2f(S[i])
#define BC8(x) __builtin_bit_cast(bf16x8, x)
#define AT_STEP(SC, SN, kp, v1p, v0p) do { \
        O = MFMA32(v1, BC8(p1), O); SCHED; v1 = cat8(trd(v1p), trd((v1p) + 512)); EX(SC, 0); EX(SC, 1); EX(SC, 2); PIN(SC); SCHED; \
        SN = MFMA32(k0, q0, negm); SCHED; k0 = *(const LAS bf16x8*)(kp); EX(SC, 3); EX(SC, 4); EX(SC, 5); PIN(SC); SCHED; \
        L = MFMA16(sel, BC8(p1), L); SCHED; EX(SC, 6); EX(SC, 7); \
        p0[0] = cvtpk(SC[0], SC[1]); p0[1] = cvtpk(SC[2], SC[3]); p0[2] = cvtpk(SC[4], SC[5]); p0[3] = cvtpk(SC[6], SC[7]); PIN(p0); SCHED; \
        O = MFMA32(v0, BC8(p0), O); SCHED; v0 = cat8(trd(v0p), trd((v0p) + 512)); EX(SC, 8); EX(SC, 9); EX(SC, 10); PIN(SC); SCHED; \
        SN = MFMA32(k1, q1, SN); SCHED; k1 = *(const LAS bf16x8*)((kp) + 1024); EX(SC, 11); EX(SC, 12); EX(SC, 13); PIN(SC); SCHED; \
        L = MFMA16(sel, BC8(p0), L); SCHED; EX(SC, 14); EX(SC, 15); \
        p1[0] = cvtpk(SC[8], SC[9]); p1[1] = cvtpk(SC[10], SC[11]); p1[2] = cvtpk(SC[12], SC[13]); p1[3] = cvtpk(SC[14], SC[15]); PIN(p1); SCHED; } while (0)
        u32x4 p0, p1 = {0u, 0u, 0u, 0u};
        int cur = 0, nxt = AT_STAGE, fre = 2 * AT_STAGE;
        const GAS u64* hflag = flags + F_H + (rt2 >> 1) * 32 + (lane & 31);
        const unsigned hb = WS_AH + ((rt * 16 + (wid >> 2) * 4) * KB_AH + KB_X + (ct - 12) * 4 + (wid & 3)) * 1024 + lane * 16;
        const int cq = 2 * (wid & 3) + ((lane >> 4) & 1);
        const unsigned eb = WS_AH + ((rt * 16 + (wid >> 2) * 4 + hi) * KB_AH + KB_X + 80 + ct) * 1024 + ((cq & 3) * 16 + (lane & 15)) * 16 + (cq >> 2) * 8;
#define H_MAIN(j) do { if (!qkv && s < 4) { if (s == 0) ST16(hmain[j], hb + (j) * KB_AH * 1024); else if (s == 1) ST16(hmain[2 + (j)], hb + (2 + (j)) * KB_AH * 1024); \
            else if (s == 2) ST16(hmain[4 + (j)], hb + (8 + (j)) * KB_AH * 1024); else ST16(hmain[6 + (j)], hb + (10 + (j)) * KB_AH * 1024); } SCHED; } while (0)
#define H_EXT() do { if (s < 4) { if (s == 0) ST8(hext[0], eb); else if (s == 1) ST8(hext[1], eb + 2 * KB_AH * 1024); \
            else if (s == 2) ST8(hext[2], eb + 8 * KB_AH * 1024); else ST8(hext[3], eb + 10 * KB_AH * 1024); } SCHED; } while (0)
        for (int s = 0; s < 8; ++s) {
            if (s >= 1 && s <= 4) { if (qkv) { WAIT_V(2); } else { WAIT_V(4); } } else { WAIT_V(0); }
            WAIT_L(0); BAR;
            if (s == 0) { if (!__all(seen == tag)) flag_wait(kvflag, tag); }
            if (s + 2 < 8) AT_ISSUE(s + 2, fre);
            if (s < 4) {
                const int r0 = (idx * 8 + wid) * 9 + (s < 3 ? s * 4 : 0), rr = (kh ? 1280 : 0) + r0;
                const char* src = (kh == 0 && r0 >= 1280 ? (const char*)wo - 1280 * (DM * 2) : (const char*)w2) + (size_t)rr * (DM * 2) + (xcd >> 2) * 1024;
                const int rl = s == 2 ? 0 : (lane >> 4);
                touch<-AT_BASE>(src, ((kh == 0 && r0 < 1280 && r0 + rl >= 1280) ? 0 : rl) * (DM * 2) + (lane & 15) * 64, sdst);
            }
            if (s == 5) { if (wid == 0 && lane == 0) flag_st(flags + F_H + rt * 32 + ct, tag); }
            if (s == 6) seen = flag_ld(hflag);
            if (s == 7 && wid >= 4) {
                if (!__all(seen == tag)) flag_wait(hflag, tag);
                const unsigned vA = (wid - 4) * 2 * KB_AH * 1024 + lane * 16, vB = ((wid - 4) * 16 + (lane >> 3)) * (DM * 2) + (((lane & 7) ^ (2 * ((lane >> 4) & 3))) * 16);
                g128_issue(lds0, g2, 0, 0, wid - 4, vA, vB); g128_issue(lds0, g2, 1, 1, wid - 4, vA, vB);
            }
            if (wid >= 4) __builtin_amdgcn_s_setprio(1);
            const LAS char* kc = kb0 + cur; const LAS char* vc = vb0 + cur; const LAS char* kn = kb0 + nxt; const LAS char* vn = vb0 + nxt;
            AT_STEP(SA, SB, kc + 2 * 2048, vc + 1024, vc + 1 * 2048);            AT_STEP(SB, SA, kc + 3 * 2048, vc + 1 * 2048 + 1024, vc + 2 * 2048);
            H_MAIN(0);
            AT_STEP(SA, SB, kc + 4 * 2048, vc + 2 * 2048 + 1024, vc + 3 * 2048); AT_STEP(SB, SA, kc + 5 * 2048, vc + 3 * 2048 + 1024, vc + 4 * 2048);
            H_EXT();
            AT_STEP(SA, SB, kc + 6 * 2048, vc + 4 * 2048 + 1024, vc + 5 * 2048); __builtin_amdgcn_s_setprio(0); AT_STEP(SB, SA, kc + 7 * 2048, vc + 5 * 2048 + 1024, vc + 6 * 2048);
            H_MAIN(1);
            AT_STEP(SA, SB, kn, vc + 6 * 2048 + 1024, vc + 7 * 2048);            AT_STEP(SB, SA, kn + 2048, vc + 7 * 2048 + 1024, vn);
            const int tmp = cur; cur = nxt; nxt = fre; fre = tmp;
        }
        O = MFMA32(v1, BC8(p1), O); L = MFMA16(sel, BC8(p1), L);
        TS(9);
        float lrow; { const float a = __shfl(L[0], lane & 15), b = __shfl(L[1], lane & 15); lrow = (lane & 16) ? b : a; }
        if (__any(!(lrow < 1.0e18f))) {
            __builtin_amdgcn_fence(__ATOMIC_ACQUIRE, "agent");
            float m = -INFINITY, l = 0.f;
#pragma unroll
            for (int i = 0; i < 16; ++i) { O[i] = 0.f; negm[i] = 0.f; }
            const u16* Vs = (const u16*)Vg;
            for (int t = 0; t < 64; ++t) { const int key0 = t * 32;
                k0 = *(const bf16x8*)(Kg + key0 * 64 + lane * 16); k1 = *(const bf16x8*)(Kg + key0 * 64 + 1024 + lane * 16);
#pragma unroll
                for (int j = 0; j < 8; ++j) { const int kk = key0 + 8 * (j >> 2) + 4 * hi + (j & 3); v0[j] = (short)Vs[kk * 32 + (lane & 31)]; v1[j] = (short)Vs[(kk + 16) * 32 + (lane & 31)]; }
                f32x16 S = MFMA32(k0, q0, negm); S = MFMA32(k1, q1, S);
                float mt = S[0];
#pragma unroll
                for (int i = 1; i < 16; ++i) mt = fmaxf(mt, S[i]);
                mt = fmaxf(mt, __shfl_xor(mt, 32));
                const float mn = fmaxf(m, mt), al = __builtin_amdgcn_exp2f(m - mn); m = mn;
                float ps = 0.f;
#pragma unroll
                for (int i = 0; i < 16; ++i) { S[i] = rbf(__builtin_amdgcn_exp2f(S[i] - mn)); ps += S[i]; O[i] *= al; }
                l = l * al + ps + __shfl_xor(ps, 32);
#pragma unroll
                for (int i = 0; i < 4; ++i) { p0[i] = cvtpk(S[2 * i], S[2 * i + 1]); p1[i] = cvtpk(S[8 + 2 * i], S[9 + 2 * i]); }
                O = MFMA32(v0, BC8(p0), O); O = MFMA32(v1, BC8(p1), O);
            }
            lrow = l;
        }
        {
            const float inv = 1.0f / lrow;
            unsigned pk[8];
#pragma unroll
            for (int i = 0; i < 8; ++i) pk[i] = cvtpk(O[2 * i] * inv, O[2 * i + 1] * inv);
            const unsigned dst = WS_AH + ((qb * 16 + wid * 2 + ((lane >> 4) & 1)) * KB_AH + head) * 1024 + (lane & 15) * 16 + hi * 256;
#pragma unroll
            for (int c = 0; c < 2; ++c) {
                auto r0 = __builtin_amdgcn_permlane32_swap(pk[4 * c + 0], pk[4 * c + 2], false, false);
                auto r1 = __builtin_amdgcn_permlane32_swap(pk[4 * c + 1], pk[4 * c + 3], false, false);
                ST16(((u32x4){r0[0], r1[0], r0[1], r1[1]}), dst + c * 512);
            }
        }
        if (wid >= 4) { WAIT_V(2); }
        WAIT_L(0); BAR;
        TS(10);
#undef AT_ISSUE
#undef AT_STEP
#undef H_MAIN
#undef H_EXT
#undef EX
#undef BC8
    }

    {
        const int tid = opaque_tid(), lane = tid & 63, tile = rt2 * 8 + ct2;
        GAS u64* myflag = flags + F_G2 + tile * 2 + kh; const GAS u64* pflag = flags + F_G2 + tile * 2 + (kh ^ 1);
        if (wid >= 4) {
            const int lw = wid - 4;
            const unsigned vA = lw * 2 * KB_AH * 1024 + lane * 16, vB = (lw * 16 + (lane >> 3)) * (DM * 2) + (((lane & 7) ^ (2 * ((lane >> 4) & 3))) * 16);
            g128_issue(lds0, g2, 2, 2, lw, vA, vB); g128_issue(lds0, g2, 3, 3, lw, vA, vB);
            WAIT_V(16);
            int slot = 0;
            for (int t = 0; t < NT2; ++t) {
                BAR;
                if (t == 0 && wid == 4 && lane == 0) flag_st(flags + F_AT + (idx & 7) * 32 + xcd * 4 + (idx >> 3), tag);
                if (t + 4 < NT2) {
                    if (t + 4 == g2.nsw) flag_wait(flags + F_AT + (rt2 >> 1) * 32 + (lane & 31), tag);
                    g128_issue(lds0, g2, t + 4, slot, lw, vA, vB); slot = slot == 3 ? 0 : slot + 1;
                    WAIT_V(16);
                } else if (t + 3 < NT2) { WAIT_V(8); } else { WAIT_V(0); }
            }
            BAR;
            if (wid == 4) { if (lane == 0) flag_st(myflag, tag); flag_wait(pflag, tag); }
            BAR;
            if (bid == 0 && wid == 4) {
#pragma unroll
                for (int i = 0; i < 4; ++i) flag_wait(flags + F_LN + i * 64 + lane, tag);
                if (lane == 0) flag_st(flags, tag);
            }
        } else {
            const int wm = wid >> 1, wn = wid & 1, g = lane >> 4, q = (lane & 15) >> 2, p = lane & 3, swz = (2 * g + (q >> 1)) & 3;
            const int aoff = wm * 8192 + lane * 16;
            const int boff0 = 16384 + (4 * g + q) * 128 + (((2 * wn) ^ swz) * 32) + p * 8, boff1 = boff0 ^ 32;
            f32x4 acc[4][4]; bf16x8 AX[4], AY[4];
            s16x4 XL[4], XH[4], YL[4], YH[4];
            int pa[2] = {aoff, aoff + 65536}, pb0[2] = {boff0, boff0 + 65536}, pb1[2] = {boff1, boff1 + 65536};
            PIN(pa[1]); PIN(pb0[1]); PIN(pb1[1]);
#define G_BP(n, sl, kk) (lds + (((n) & 1) ? pb1[(sl) >> 1] : pb0[(sl) >> 1]) + ((sl) & 1) * 32768 + ((n) >> 1) * 8192 + (kk) * 4096)
#define G_BL(L_, n, sl, kk) do { L_[n] = trd(G_BP(n, sl, kk)); SCHED; } while (0)
#define G_BH(H_, n, sl, kk) do { H_[n] = trd(G_BP(n, sl, kk) + 2048); SCHED; } while (0)
#define G_A(A_, m, sl, kk) do { A_[m] = *(const LAS bf16x8*)(lds + pa[(sl) >> 1] + ((sl) & 1) * 32768 + ((m) * 2 + (kk)) * 1024); SCHED; } while (0)
#define G_M(A_, L_, H_, m, n) do { MFMA16_ACC(acc[m][n], cat8(L_[n], H_[n]), A_[m]); SCHED; } while (0)
#define G_REST(AC, CL, CH, AN, NL, NH, sl, kk) do { \
        G_BL(NL, 0, sl, kk); G_M(AC, CL, CH, 0, 2); G_BH(NH, 0, sl, kk); G_M(AC, CL, CH, 0, 3); G_A(AN, 0, sl, kk); G_M(AC, CL, CH, 1, 0); G_BL(NL, 1, sl, kk); G_M(AC, CL, CH, 1, 1); \
        G_BH(NH, 1, sl, kk); G_M(AC, CL, CH, 1, 2); G_BL(NL, 2, sl, kk); G_M(AC, CL, CH, 1, 3); G_BH(NH, 2, sl, kk); G_M(AC, CL, CH, 2, 0); G_BL(NL, 3, sl, kk); G_M(AC, CL, CH, 2, 1); \
        G_BH(NH, 3, sl, kk); G_M(AC, CL, CH, 2, 2); G_A(AN, 1, sl, kk); G_M(AC, CL, CH, 2, 3); G_A(AN, 2, sl, kk); G_M(AC, CL, CH, 3, 0); G_A(AN, 3, sl, kk); G_M(AC, CL, CH, 3, 1); \
        G_M(AC, CL, CH, 3, 2); G_M(AC, CL, CH, 3, 3); } while (0)
#define G_TILE(sl) do { G_M(AX, XL, XH, 0, 0); G_M(AX, XL, XH, 0, 1); G_REST(AX, XL, XH, AY, YL, YH, sl, 1); \
        G_M(AY, YL, YH, 0, 0); G_M(AY, YL, YH, 0, 1); \
        asm volatile("s_waitcnt vmcnt(0) lgkmcnt(0)" ::: "memory"); BAR; SCHED;        \
        G_REST(AY, YL, YH, AX, XL, XH, ((sl) + 1) & 3, 0); } while (0)
#pragma unroll
            for (int m = 0; m < 4; ++m)
#pragma unroll
                for (int n = 0; n < 4; ++n) { acc[m][n] = (f32x4){0.f, 0.f, 0.f, 0.f}; PIN(acc[m][n]); }
            SCHED;
#pragma unroll
            for (int i = 0; i < 4; ++i) { G_BL(XL, i, 0, 0); G_BH(XH, i, 0, 0); G_A(AX, i, 0, 0); }
            for (int t = 0; t < NT2; t += 4) { G_TILE(0); G_TILE(1); G_TILE(2); G_TILE(3); }
#undef G_BP
#undef G_BL
#undef G_BH
#undef G_A
#undef G_M
#undef G_REST
#undef G_TILE
            asm volatile("s_nop 7"); SCHED;
            TS(11);
            const int fr = lane & 15, fq = lane >> 4;
            const size_t rowo = (size_t)(rt2 * 128 + wm * 64 + kh * 32 + fr) * DM + ct2 * 128 + wn * 32 + (fq & 1) * 16 + (fq >> 1) * 8;
            const unsigned pbase = WS_PART + tile * 32768 + tid * 16;
            u32x4 xv[2][2]; f32x4 keep[2][4];
            if (kh == 0) {
#pragma unroll
                for (int m = 0; m < 2; ++m)
#pragma unroll
                    for (int s = 0; s < 2; ++s) { ST16(pack8(acc[2 + m][2 * s], acc[2 + m][2 * s + 1]), pbase + (m * 2 + s) * 4096);
                        keep[m][2 * s] = acc[m][2 * s]; keep[m][2 * s + 1] = acc[m][2 * s + 1]; }
            } else {
#pragma unroll
                for (int m = 0; m < 2; ++m)
#pragma unroll
                    for (int s = 0; s < 2; ++s) { ST16(pack8(acc[m][2 * s], acc[m][2 * s + 1]), pbase + 16384 + (m * 2 + s) * 4096);
                        keep[m][2 * s] = acc[2 + m][2 * s]; keep[m][2 * s + 1] = acc[2 + m][2 * s + 1]; }
            }
            WAIT_V(0); BAR;
            TS(12);
#pragma unroll
            for (int m = 0; m < 2; ++m)
#pragma unroll
                for (int s = 0; s < 2; ++s) xv[m][s] = *(const u32x4*)(x + rowo + (size_t)m * 16 * DM + s * 64);
            CBAR; BAR; CBAR;
            TS(13);
            u32x4 pv[2][2];
#pragma unroll
            for (int m = 0; m < 2; ++m)
#pragma unroll
                for (int s = 0; s < 2; ++s) pv[m][s] = LD16(pbase + (kh ^ 1) * 16384 + (m * 2 + s) * 4096);
#pragma unroll
            for (int m = 0; m < 2; ++m)
#pragma unroll
                for (int s = 0; s < 2; ++s) { float lo[4], hi4[4]; const u32x4 p_ = pv[m][s], x_ = xv[m][s];
                    const f32x4 a0 = keep[m][2 * s] + (f32x4){bf_lo(p_[0]), bf_hi(p_[0]), bf_lo(p_[1]), bf_hi(p_[1])}, a1 = keep[m][2 * s + 1] + (f32x4){bf_lo(p_[2]), bf_hi(p_[2]), bf_lo(p_[3]), bf_hi(p_[3])};
#pragma unroll
                    for (int i = 0; i < 4; ++i) { auto r = __builtin_amdgcn_permlane16_swap(__float_as_uint(a0[i]), __float_as_uint(a1[i]), false, false);
                        lo[i] = __uint_as_float(r[0]); hi4[i] = __uint_as_float(r[1]); }
                    u32x4 o; o[0] = cvtpk(lo[0] + bf_lo(x_[0]), lo[1] + bf_hi(x_[0])); o[1] = cvtpk(lo[2] + bf_lo(x_[1]), lo[3] + bf_hi(x_[1]));
                    o[2] = cvtpk(hi4[0] + bf_lo(x_[2]), hi4[1] + bf_hi(x_[2])); o[3] = cvtpk(hi4[2] + bf_lo(x_[3]), hi4[3] + bf_hi(x_[3]));
                    __builtin_nontemporal_store(o, (u32x4*)(out + rowo + (size_t)m * 16 * DM + s * 64));
                }
        }
        TS(14); TR(31);
    }
}

extern "C" void kernel_launch(void* const* d_in, const int* in_sizes, int n_in, void* d_out, int out_size, void* d_ws, size_t ws_size, hipStream_t stream) {
    (void)hipFuncSetAttribute((const void*)k_block, hipFuncAttributeMaxDynamicSharedMemorySize, LDS_BYTES);
    k_block<<<256, 512, LDS_BYTES, stream>>>((const u16*)d_in[0], (const u16*)d_in[1], (const u16*)d_in[2], (const u16*)d_in[4], (const u16*)d_in[5], (const u16*)d_in[6], (char*)d_ws, (u16*)d_out);
}
```

```cpp
#include <hip/hip_runtime.h>
#include <hip/hip_bf16.h>
#include <stdint.h>

#define LAS __attribute__((address_space(3)))
#define GAS __attribute__((address_space(1)))
#define DI __device__ __forceinline__
typedef unsigned short u16;
typedef unsigned long long u64;
typedef short bf16x8 __attribute__((ext_vector_type(8)));
typedef short s16x4 __attribute__((ext_vector_type(4)));
typedef float f32x2 __attribute__((ext_vector_type(2)));
typedef float f32x4 __attribute__((ext_vector_type(4)));
typedef float f32x16 __attribute__((ext_vector_type(16)));
typedef unsigned u32x2 __attribute__((ext_vector_type(2)));
typedef unsigned u32x4 __attribute__((ext_vector_type(4)));
typedef __bf16 bf16x2_t __attribute__((ext_vector_type(2)));

constexpr int SEQ = 2048, DM = 1024, FF = 3584;
constexpr int KB_X = DM / 32;
constexpr int KB_AH = (DM + FF) / 32;
constexpr unsigned WS_XN = 0;
constexpr unsigned WS_Q = WS_XN + SEQ * DM * 2;
constexpr unsigned WS_K = WS_Q + SEQ * DM * 2;
constexpr unsigned WS_V = WS_K + SEQ * DM * 2;
constexpr unsigned WS_AH = WS_V + SEQ * DM * 2;
constexpr unsigned WS_PART = WS_AH + SEQ * (DM + FF) * 2;
constexpr unsigned WS_FLAG = WS_PART + 128 * 32768;
constexpr unsigned WS_END = WS_FLAG + 16384;
constexpr int F_LN = 32, F_QKV = F_LN + 256, F_H = F_QKV + 256, F_AT = F_H + 256, F_G2 = F_AT + 256;
constexpr int LDS_BYTES = 163840;
constexpr unsigned CODE_BYTES = 27648;
#define TLBW 262144

DI unsigned cvtpk(float lo, float hi) { f32x2 v = {lo, hi}; bf16x2_t b = __builtin_convertvector(v, bf16x2_t); return __builtin_bit_cast(unsigned, b); }
DI float bf_lo(unsigned u) { return __uint_as_float(u << 16); }
DI float bf_hi(unsigned u) { return __uint_as_float(u & 0xffff0000u); }
DI float rbf(float f) { return __uint_as_float(cvtpk(f, f) << 16); }
DI s16x4 trd(const LAS char* p) { return __builtin_amdgcn_ds_read_tr16_b64_v4i16((LAS s16x4*)p); }
DI bf16x8 cat8(s16x4 a, s16x4 b) { return __builtin_shufflevector(a, b, 0, 1, 2, 3, 4, 5, 6, 7); }
template <int OFF, int D> DI void glds(const char* base, unsigned voff, unsigned dst) {
    asm volatile("s_add_u32 m0, %2, %4\n\ts_nop 0\n\tglobal_load_lds_dwordx4 %0, %1 offset:%3" :: "v"(voff), "s"(base), "s"(dst), "i"(OFF), "i"(D - OFF) : "memory", "scc"); }
template <int D> DI void glds_pair_sc1(const char* base, unsigned voff, unsigned dst) {
    asm volatile("s_add_u32 m0, %2, %3\n\ts_nop 0\n\tglobal_load_lds_dwordx4 %0, %1 sc1\n\tglobal_load_lds_dwordx4 %0, %1 offset:1024 sc1" :: "v"(voff), "s"(base), "s"(dst), "i"(D) : "memory", "scc"); }
template <int D> DI void touch(const char* base, unsigned voff, unsigned dst) {
    asm volatile("s_add_u32 m0, %2, %3\n\ts_nop 0\n\tglobal_load_lds_dword %0, %1" :: "v"(voff), "s"(base), "s"(dst), "i"(D) : "memory", "scc"); }
#define WAIT_V(n) asm volatile("s_waitcnt vmcnt(" #n ")" ::: "memory")
#define WAIT_L(n) asm volatile("s_waitcnt lgkmcnt(" #n ")" ::: "memory")
#define BAR __builtin_amdgcn_s_barrier()
#define CBAR asm volatile("" ::: "memory")
#define SCHED __builtin_amdgcn_sched_barrier(0)
#define PIN(x) asm volatile("" : "+v"(x))
#define MFMA16(a, b, c) __builtin_amdgcn_mfma_f32_16x16x32_bf16(a, b, c, 0, 0, 0)
#define MFMA16_ACC(c, a, b) asm volatile("v_mfma_f32_16x16x32_bf16 %0, %1, %2, %0" : "+v"(c) : "v"(a), "v"(b))
#define MFMA32(a, b, c) __builtin_amdgcn_mfma_f32_32x32x16_bf16(a, b, c, 0, 0, 0)
#define ST16(v, off) __builtin_amdgcn_raw_buffer_store_b128(v, rs, (int)(off), 0, 16)
#define ST8(v, off) __builtin_amdgcn_raw_buffer_store_b64(v, rs, (int)(off), 0, 16)
#define LD16(off) __builtin_amdgcn_raw_buffer_load_b128(rs, (int)(off), 0, 16)
DI u64 flag_ld(const GAS u64* p) { return __hip_atomic_load(p, __ATOMIC_RELAXED, __HIP_MEMORY_SCOPE_AGENT); }
DI void flag_st(GAS u64* p, u64 v) { __hip_atomic_store(p, v, __ATOMIC_RELAXED, __HIP_MEMORY_SCOPE_AGENT); }
DI void flag_wait(const GAS u64* p, u64 tag) {
    for (unsigned spins = 0; spins < (1u << 18); ++spins) { if (__all(flag_ld(p) == tag)) break; __builtin_amdgcn_s_sleep(1); }
    CBAR;
}
template <int N> DI float ror_add(float v) { return v + __builtin_bit_cast(float, __builtin_amdgcn_update_dpp(0, __builtin_bit_cast(int, v), 0x120 + N  , 0xf, 0xf, false)); }
DI float sum64(float v) {
    v = ror_add<1>(ror_add<2>(ror_add<4>(ror_add<8>(v))));
    { auto r = __builtin_amdgcn_permlane16_swap(__float_as_uint(v), __float_as_uint(v), false, false); v = __uint_as_float(r[0]) + __uint_as_float(r[1]); }
    { auto r = __builtin_amdgcn_permlane32_swap(__float_as_uint(v), __float_as_uint(v), false, false); v = __uint_as_float(r[0]) + __uint_as_float(r[1]); }
    return v;
}
DI int opaque_tid() { int t = threadIdx.x; asm volatile("" : "+v"(t)); return t; }

#define TS(i)
#define TR(i)

DI float silu_mul(float g, float u) { return g * u * __builtin_amdgcn_rcpf(1.0f + __builtin_amdgcn_exp2f(g * -1.4426950408889634f)); }
DI u32x4 pack8(const f32x4& a, const f32x4& b) { u32x4 o; o[0] = cvtpk(a[0], a[1]); o[1] = cvtpk(a[2], a[3]); o[2] = cvtpk(b[0], b[1]); o[3] = cvtpk(b[2], b[3]); return o; }
constexpr float QSCALE = 0.17677669529663687f * 1.4426950408889634f;

struct G128 { const char* a0; const char* a1; const char* b0; const char* b1; int nsw; };
DI void g128_issue(unsigned lds0, const G128& g, int t, int slot, int lw, unsigned voffA, unsigned voffB) {
    const unsigned sa = lds0 + slot * 32768 + lw * 4096, sb = lds0 + slot * 32768 + lw * 2048;
    const char* a = (t < g.nsw ? g.a0 : g.a1) + (size_t)t * 2048; const char* b = (t < g.nsw ? g.b0 : g.b1) + (size_t)t * (64 * DM * 2);
    glds_pair_sc1<0>(a, voffA, sa); glds_pair_sc1<2048>(a + KB_AH * 1024, voffA, sa);
    glds<0, 16384>(b, voffB, sb); glds<128, 24576>(b, voffB, sb);
    glds<0, 16384 + 1024>(b + 8 * DM * 2, voffB, sb); glds<128, 24576 + 1024>(b + 8 * DM * 2, voffB, sb);
}

__global__ __launch_bounds__(512, 2) void k_block(const u16* __restrict__ x, const u16* __restrict__ wqkv, const u16* __restrict__ wo, const u16* __restrict__ w1,
                                                  const u16* __restrict__ w3, const u16* __restrict__ w2, char* __restrict__ ws, u16* __restrict__ out) {
    extern __shared__ __attribute__((aligned(16))) char smem[];
    LAS char* lds = (LAS char*)smem;
    const unsigned lds0 = (unsigned)(size_t)lds;
    const int wid = __builtin_amdgcn_readfirstlane(threadIdx.x >> 6);
    const int bid = blockIdx.x, xcd = bid & 7, idx = bid >> 3;
    GAS u64* flags = (GAS u64*)(ws + WS_FLAG);
    const __amdgpu_buffer_rsrc_t rs = __builtin_amdgcn_make_buffer_rsrc((void*)ws, 0, WS_END, 0x00020000);
    {
        const char* pc; asm volatile("s_getpc_b64 %0" : "=s"(pc));
        const int l = threadIdx.x & 63, piece = (idx * 8 + wid) & 7;
        const unsigned o = wid == 0 ? l * (CODE_BYTES / 64) : piece * 4096 + l * 64;
        touch<148480>(pc, o < CODE_BYTES ? o : 0, lds0 + wid * 1024);
        if (wid == 1) {
            constexpr int NW = WS_END / TLBW + 1, NO = SEQ * DM * 2 / TLBW, N2 = FF * DM * 2 / TLBW;
#pragma unroll
            for (int j = 0; j < (NW + NO + N2 + 64) / 64; ++j) { const int pg = j * 64 + l;
                const char* pa = pg < NW ? ws + (size_t)pg * TLBW : pg < NW + NO ? (const char*)out + (size_t)(pg - NW) * TLBW : pg < NW + NO + N2 ? (const char*)w2 + (size_t)(pg - NW - NO) * TLBW : (const char*)wo;
                asm volatile("s_add_u32 m0, %1, 148480\n\ts_nop 0\n\tglobal_load_lds_dword %0, off sc1" :: "v"(pa), "s"(lds0 + 1024) : "memory", "scc"); }
        }
    }
    TR(30); TS(0);
    u64 tag;
    asm volatile("s_load_dwordx2 %0, %1, 0x0" : "=s"(tag) : "s"(ws + WS_FLAG) : "memory");
    const int cj = idx >> 2, ct = cj < 3 ? (xcd >> 1) * 3 + cj : 12 + (xcd >> 1) * 5 + (cj - 3), rt = (xcd & 1) * 4 + (idx & 3);
    const bool qkv = ct < 12;
    u32x4 hmain[8]; u32x2 hext[4];

    {
        const int tid = opaque_tid(), lane = tid & 63, wr = wid >> 2, wc = wid & 3;
        const unsigned ldb2 = qkv ? 3 * DM * 2 : FF * 2;
        const char* cB0 = qkv ? (const char*)wqkv + ct * 512 : (const char*)w1 + (ct - 12) * 256;
        const char* cB1 = qkv ? cB0 + 256 : (const char*)w3 + (ct - 12) * 256;
        const char* cA = ws + WS_XN + rt * 16 * KB_X * 1024;
        const unsigned voffA = wid * (KB_X * 1024) + lane * 16;
        const unsigned voffB = (unsigned)(wid * 8 + (lane >> 3)) * ldb2 + (((lane & 7) ^ (2 * ((lane >> 4) & 3))) * 16);
        const char* pE = ((lane & 1) ? (const char*)w3 : (const char*)w1) + (size_t)(wid * 8 + (lane >> 3)) * (FF * 2) + (2560 + ct * 32) * 2 + ((((lane >> 1) & 3) ^ ((lane >> 4) & 3)) * 16);
        const size_t kstepB = (size_t)64 * ldb2;
        const unsigned ldsA = lds0 + wid * 2048, ldsB = lds0 + wid * 1024;
        const char* cT = ((idx & 2) ? cB1 : cB0) + (idx & 1) * 128;
#define SA(b, h) (((b) * 2 + (h)) * 16384)
#define SB(b, h) (((b) * 2 + (h)) * 16384)
#define STA(b, h, kt) glds_pair_sc1<SA(b, h)>(cA + (h) * 8 * KB_X * 1024 + (kt) * 2048, voffA, ldsA)
#define STB(b, h, kt) do { const char* s_ = ((h) ? cB1 : cB0) + (size_t)(kt) * kstepB; \
        glds<0, 65536 + SB(b, h)>(s_, voffB, ldsB); glds<128, 65536 + SB(b, h) + 8192>(s_, voffB, ldsB); } while (0)
#define TCH(kt) touch<148480>(cT + (size_t)(kt) * kstepB, voffB, ldsB)
#define STE(b, kt) do { const char* s_ = pE + (size_t)(kt) * (64 * FF * 2); \
        asm volatile("s_add_u32 m0, %1, %2\n\ts_nop 0\n\tglobal_load_lds_dwordx4 %0, off" :: "v"(s_), "s"(ldsB), "i"(131072 + (b) * 8192) : "memory", "scc"); } while (0)
        {
            u32x4 xa, xb;
            { const char* xp = (const char*)x + (size_t)(bid * 8 + wid) * (DM * 2);
              asm volatile("global_load_dwordx4 %0, %2, %3\n\tglobal_load_dwordx4 %1, %2, %3 offset:1024" : "=&v"(xa), "=&v"(xb) : "v"(lane * 16), "s"(xp) : "memory"); }
            STB(0, 0, 0); STB(0, 1, 0); STE(0, 0);
            asm volatile("s_waitcnt vmcnt(5) lgkmcnt(0)" : "+v"(xa), "+v"(xb), "+s"(tag) :: "memory");
            tag += 1;
            float f[16];
#pragma unroll
            for (int i = 0; i < 4; ++i) { f[2 * i] = bf_lo(xa[i]); f[2 * i + 1] = bf_hi(xa[i]); f[8 + 2 * i] = bf_lo(xb[i]); f[9 + 2 * i] = bf_hi(xb[i]); }
            float s = 0.f;
#pragma unroll
            for (int i = 0; i < 16; ++i) s += f[i];
            const float mean = sum64(s) * (1.0f / DM);
            float q = 0.f;
#pragma unroll
            for (int i = 0; i < 16; ++i) { const float d = f[i] - mean; q += d * d; }
            const float var = sum64(q) * (1.0f / DM);
            const float mb = rbf(mean), sd = rbf(__builtin_amdgcn_sqrtf(rbf(rbf(var) + 1e-5f))), inv = __builtin_amdgcn_rcpf(sd);
#pragma unroll
            for (int c = 0; c < 2; ++c) { u32x4 o;
#pragma unroll
                for (int i = 0; i < 4; ++i) o[i] = cvtpk(rbf(f[c * 8 + 2 * i] - mb) * inv, rbf(f[c * 8 + 2 * i + 1] - mb) * inv);
                *(LAS u32x4*)(lds + wid * 2080 + c * 1024 + lane * 16) = o; }
            WAIT_L(0); BAR; CBAR;
            const int r = lane & 7, g = (lane >> 3) & 3, row = bid * 8 + r;
#pragma unroll
            for (int c = 0; c < 2; ++c) { const int kb = 4 * wid + 2 * c + (lane >> 5); const LAS char* p = lds + r * 2080 + kb * 64 + g * 8;
                const u32x2 lo = *(const LAS u32x2*)p, hi = *(const LAS u32x2*)(p + 32);
                ST16(((u32x4){lo[0], lo[1], hi[0], hi[1]}), WS_XN + ((row >> 4) * KB_X + kb) * 1024 + (g * 16 + (row & 15)) * 16); }
            WAIT_V(0); BAR;
            TS(1);
            if (wid == 0) { if (lane == 0) flag_st(flags + F_LN + bid, tag); flag_wait(flags + F_LN + rt * 32 + (lane & 31), tag); }
            STB(1, 0, 1); STB(1, 1, 1); STE(1, 1);
            if (wid != 0) { TCH(2); TCH(3); }
            BAR; CBAR;
            TS(2);
        }
        f32x4 acc[2][2][4][2], ace[2][4];
        {
            const int g = lane >> 4, q = (lane & 15) >> 2, p = lane & 3, swz = (2 * g + (q >> 1)) & 3;
            const int aoff = wr * 8192 + lane * 16;
            const int boff0 = 65536 + (wc >> 1) * 8192 + (4 * g + q) * 128 + (((2 * (wc & 1)) ^ swz) * 32) + p * 8, boff1 = boff0 ^ 32;
            int eoff = 131072 + (4 * g + q) * 128 + ((wc ^ swz) * 32) + p * 8; PIN(eoff);
#define LDA(dst, b, h) do { _Pragma("unroll") for (int m = 0; m < 4; ++m) _Pragma("unroll") for (int k = 0; k < 2; ++k) \
        dst[m][k] = *(const LAS bf16x8*)(lds + aoff + SA(b, h) + (m * 2 + k) * 1024); } while (0)
#define LDB(dst, b, h) do { _Pragma("unroll") for (int n = 0; n < 2; ++n) _Pragma("unroll") for (int k = 0; k < 2; ++k) { \
        const LAS char* b_ = lds + (n ? boff1 : boff0) + SB(b, h) + k * 4096; dst[n][k] = cat8(trd(b_), trd(b_ + 2048)); } } while (0)
#define LDE(dst, b) do { _Pragma("unroll") for (int k = 0; k < 2; ++k) { const LAS char* b_ = lds + eoff + (b) * 8192 + k * 4096; dst[k] = cat8(trd(b_), trd(b_ + 2048)); } } while (0)
#define MMA(ai) do { __builtin_amdgcn_s_setprio(1); \
        _Pragma("unroll") for (int k = 0; k < 2; ++k) _Pragma("unroll") for (int m = 0; m < 4; ++m) { \
            acc[ai][0][m][0] = MFMA16(B0[0][k], At[m][k], acc[ai][0][m][0]); acc[ai][0][m][1] = MFMA16(B0[1][k], At[m][k], acc[ai][0][m][1]); \
            acc[ai][1][m][0] = MFMA16(B1[0][k], At[m][k], acc[ai][1][m][0]); acc[ai][1][m][1] = MFMA16(B1[1][k], At[m][k], acc[ai][1][m][1]); \
            ace[ai][m] = MFMA16(BE[k], At[m][k], ace[ai][m]); } \
        __builtin_amdgcn_s_setprio(0); } while (0)
            STA(0, 0, 0); STA(0, 1, 0); STA(1, 0, 1);
#pragma unroll
            for (int a = 0; a < 2; ++a)
#pragma unroll
                for (int m = 0; m < 4; ++m) { ace[a][m] = (f32x4){0.f, 0.f, 0.f, 0.f};
#pragma unroll
                    for (int b = 0; b < 2; ++b)
#pragma unroll
                        for (int n = 0; n < 2; ++n) acc[a][b][m][n] = (f32x4){0.f, 0.f, 0.f, 0.f}; }
            bf16x8 At[4][2], B0[2][2], B1[2][2], BE[2];
            constexpr int nt = DM / 64;
            if (wr == 1) BAR;
            WAIT_V(4); BAR;
            WAIT_V(2); BAR;
            TS(3);
            for (int t = 0; t < nt; t += 2) {
                const int t2 = t + 2 < nt ? t + 2 : t, tt = t + 4 < nt ? t + 4 : t;
                LDB(B0, 0, 0); LDB(B1, 0, 1); LDE(BE, 0); SCHED; LDA(At, 0, 0); STA(1, 1, t + 1); TCH(tt);
                WAIT_V(10); WAIT_L(0); BAR; MMA(0); BAR; SCHED;
                LDA(At, 0, 1); STB(0, 0, t2); STB(0, 1, t2); STE(0, t2); STA(0, 0, t2);
                WAIT_V(10); WAIT_L(0); BAR; MMA(1); BAR; SCHED;
                LDB(B0, 1, 0); LDB(B1, 1, 1); LDE(BE, 1); SCHED; LDA(At, 1, 0); STA(0, 1, t2); TCH(tt + 1);
                WAIT_V(10); WAIT_L(0); BAR; MMA(0); BAR; SCHED;
                LDA(At, 1, 1); STB(1, 0, t2 + 1); STB(1, 1, t2 + 1); STE(1, t2 + 1); STA(1, 0, t2 + 1);
                WAIT_V(10); WAIT_L(0); BAR; MMA(1); if (t + 2 < nt || wr == 0) BAR; SCHED;
            }
            TS(4);
#undef SA
#undef SB
#undef STA
#undef STB
#undef STE
#undef TCH
#undef LDA
#undef LDB
#undef LDE
#undef MMA
        }
        const int ln = opaque_tid() & 63, fr = ln & 15, fq = ln >> 4;
        if (qkv) {
            const int kind = ct >> 2;
            const float sc = kind == 0 ? QSCALE : 1.0f;
            const unsigned base = WS_Q + kind * (SEQ * DM * 2) + ((ct & 3) * 8 + wc) * 131072 + (rt * 256 + wr * 64) * 64;
            const unsigned lo0 = base + (kind == 2 ? fr * 64 + fq * 16 : fr * 16 + fq * 512), lo1 = lo0 + (kind == 2 ? 1024 : 256);
#pragma unroll
            for (int bj = 0; bj < 2; ++bj)
#pragma unroll
                for (int ai = 0; ai < 2; ++ai)
#pragma unroll
                    for (int m = 0; m < 4; ++m)
                        ST16(pack8(acc[ai][bj][m][0] * sc, acc[ai][bj][m][1] * sc), bj * 4 * 131072 + ai * 8192 + (m >> 1) * 2048 + ((m & 1) ? lo1 : lo0));
            WAIT_V(0); BAR;
            if (wid == 0 && ln == 0) flag_st(flags + F_QKV + rt * 32 + ct, tag);
        } else {
#pragma unroll
            for (int ai = 0; ai < 2; ++ai)
#pragma unroll
                for (int m = 0; m < 4; ++m) { f32x4 h0, h1;
#pragma unroll
                    for (int j = 0; j < 4; ++j) { h0[j] = silu_mul(acc[ai][0][m][0][j], acc[ai][1][m][0][j]); h1[j] = silu_mul(acc[ai][0][m][1][j], acc[ai][1][m][1][j]); }
                    hmain[ai * 4 + m] = pack8(h0, h1);
                }
        }
#pragma unroll
        for (int ai = 0; ai < 2; ++ai)
#pragma unroll
            for (int mp = 0; mp < 2; ++mp) { float h[4];
#pragma unroll
                for (int j = 0; j < 4; ++j) { auto r = __builtin_amdgcn_permlane32_swap(__float_as_uint(ace[ai][2 * mp][j]), __float_as_uint(ace[ai][2 * mp + 1][j]), false, false);
                    h[j] = silu_mul(__uint_as_float(r[0]), __uint_as_float(r[1])); }
                hext[ai * 2 + mp] = (u32x2){cvtpk(h[0], h[1]), cvtpk(h[2], h[3])};
            }
        TS(5);
        WAIT_V(0);
        TS(6);
    }

    const int kh = xcd & 1, rt2 = ((xcd >> 1) & 1) * 8 + (idx & 7), ct2 = (xcd >> 2) * 4 + (idx >> 3);
    constexpr int NT2 = 36;
    G128 g2;
    g2.nsw = kh ? NT2 : 20;
    g2.a0 = ws + WS_AH + (rt2 * 8 * KB_AH + (kh ? 72 : 32)) * 1024; g2.a1 = ws + WS_AH + rt2 * 8 * KB_AH * 1024 - 20 * 2048;
    g2.b0 = (const char*)w2 + ct2 * 256 + (kh ? 1280 * DM * 2 : 0); g2.b1 = (const char*)wo + ct2 * 256 - 20 * 64 * DM * 2;

    {
        constexpr int AT_BASE = 65536, AT_STAGE = 32768;
        const int lane = opaque_tid() & 63, hi = lane >> 5;
        const int head = xcd * 4 + (idx >> 3), qb = idx & 7;
        if (wid == 0) flag_wait(flags + F_QKV + (lane < 4 ? (lane & 1) * 32 + 4 + (lane >> 1) * 4 : qb * 32) + (head >> 3), tag);
        BAR; CBAR;
        TS(7);
        const char* Kg = ws + WS_K + head * 131072; const char* Vg = ws + WS_V + head * 131072;
        const unsigned soff = wid * 2048 + lane * 16;
        const unsigned sdst = lds0 + AT_BASE + wid * 2048;
#define AT_ISSUE(s, slot) do { glds_pair_sc1<0>(Kg + (s) * 16384, soff, sdst + (slot)); glds_pair_sc1<16384>(Vg + (s) * 16384, soff, sdst + (slot)); } while (0)
        const unsigned qoff = WS_Q + (head * 64 + qb * 8 + wid) * 2048 + lane * 16;
        const bf16x8 q0 = __builtin_bit_cast(bf16x8, LD16(qoff)), q1 = __builtin_bit_cast(bf16x8, LD16(qoff + 1024));
        AT_ISSUE(0, 0); AT_ISSUE(1, AT_STAGE);
        const GAS u64* kvflag = flags + F_QKV + (lane & 7) * 32 + 4 + ((lane >> 3) & 1) * 4 + (head >> 3);
        u64 seen = flag_ld(kvflag);
        bf16x8 sel; { const short one = ((lane & 15) == ((lane >> 4) & 1)) ? (short)0x3f80 : (short)0;
#pragma unroll
            for (int i = 0; i < 8; ++i) sel[i] = one; }
        const LAS char* kb0 = lds + AT_BASE + lane * 16;
        const LAS char* vb0 = lds + AT_BASE + 16384 + (4 * hi + ((lane & 15) >> 2)) * 64 + ((lane >> 4) & 1) * 32 + (lane & 3) * 8;
        f32x16 O, negm, SA, SB; f32x4 L = {0.f, 0.f, 0.f, 0.f};
        bf16x8 k0, k1, v0, v1;
#pragma unroll
        for (int i = 0; i < 16; ++i) { O[i] = 0.f; negm[i] = 0.f; }
        WAIT_V(5); BAR;
        TS(8);
        {
            k0 = *(const LAS bf16x8*)(kb0); k1 = *(const LAS bf16x8*)(kb0 + 1024);
            SA = MFMA32(k0, q0, negm); SA = MFMA32(k1, q1, SA);
            k0 = *(const LAS bf16x8*)(kb0 + 2048); k1 = *(const LAS bf16x8*)(kb0 + 3072);
            v0 = cat8(trd(vb0), trd(vb0 + 512)); v1 = cat8(trd(vb0 + 1024), trd(vb0 + 1536));
            float m = SA[0];
#pragma unroll
            for (int i = 1; i < 16; ++i) m = fmaxf(m, SA[i]);
            m = fmaxf(m, __shfl_xor(m, 32));
#pragma unroll
            for (int i = 0; i < 16; ++i) { negm[i] = -m; SA[i] -= m; }
            PIN(negm);
        }
#define EX(S, i) S[i] = __builtin_amdgcn_exp2f(S[i])
#define BC8(x) __builtin_bit_cast(bf16x8, x)
#define AT_STEP(SC, SN, kp, v1p, v0p) do { \
        O = MFMA32(v1, BC8(p1), O); SCHED; v1 = cat8(trd(v1p), trd((v1p) + 512)); EX(SC, 0); EX(SC, 1); EX(SC, 2); PIN(SC); SCHED; \
        SN = MFMA32(k0, q0, negm); SCHED; k0 = *(const LAS bf16x8*)(kp); EX(SC, 3); EX(SC, 4); EX(SC, 5); PIN(SC); SCHED; \
        L = MFMA16(sel, BC8(p1), L); SCHED; EX(SC, 6); EX(SC, 7); \
        p0[0] = cvtpk(SC[0], SC[1]); p0[1] = cvtpk(SC[2], SC[3]); p0[2] = cvtpk(SC[4], SC[5]); p0[3] = cvtpk(SC[6], SC[7]); PIN(p0); SCHED; \
        O = MFMA32(v0, BC8(p0), O); SCHED; v0 = cat8(trd(v0p), trd((v0p) + 512)); EX(SC, 8); EX(SC, 9); EX(SC, 10); PIN(SC); SCHED; \
        SN = MFMA32(k1, q1, SN); SCHED; k1 = *(const LAS bf16x8*)((kp) + 1024); EX(SC, 11); EX(SC, 12); EX(SC, 13); PIN(SC); SCHED; \
        L = MFMA16(sel, BC8(p0), L); SCHED; EX(SC, 14); EX(SC, 15); \
        p1[0] = cvtpk(SC[8], SC[9]); p1[1] = cvtpk(SC[10], SC[11]); p1[2] = cvtpk(SC[12], SC[13]); p1[3] = cvtpk(SC[14], SC[15]); PIN(p1); SCHED; } while (0)
        u32x4 p0, p1 = {0u, 0u, 0u, 0u};
        int cur = 0, nxt = AT_STAGE, fre = 2 * AT_STAGE;
        const GAS u64* hflag = flags + F_H + (rt2 >> 1) * 32 + (lane & 31);
        const unsigned hb = WS_AH + ((rt * 16 + (wid >> 2) * 4) * KB_AH + KB_X + (ct - 12) * 4 + (wid & 3)) * 1024 + lane * 16;
        const int cq = 2 * (wid & 3) + ((lane >> 4) & 1);
        const unsigned eb = WS_AH + ((rt * 16 + (wid >> 2) * 4 + hi) * KB_AH + KB_X + 80 + ct) * 1024 + ((cq & 3) * 16 + (lane & 15)) * 16 + (cq >> 2) * 8;
#define H_MAIN(j) do { if (!qkv && s < 4) { if (s == 0) ST16(hmain[j], hb + (j) * KB_AH * 1024); else if (s == 1) ST16(hmain[2 + (j)], hb + (2 + (j)) * KB_AH * 1024); \
            else if (s == 2) ST16(hmain[4 + (j)], hb + (8 + (j)) * KB_AH * 1024); else ST16(hmain[6 + (j)], hb + (10 + (j)) * KB_AH * 1024); } SCHED; } while (0)
#define H_EXT() do { if (s < 4) { if (s == 0) ST8(hext[0], eb); else if (s == 1) ST8(hext[1], eb + 2 * KB_AH * 1024); \
            else if (s == 2) ST8(hext[2], eb + 8 * KB_AH * 1024); else ST8(hext[3], eb + 10 * KB_AH * 1024); } SCHED; } while (0)
        for (int s = 0; s < 8; ++s) {
            if (s >= 1 && s <= 4) { if (qkv) { WAIT_V(2); } else { WAIT_V(4); } } else { WAIT_V(0); }
            WAIT_L(0); BAR;
            if (s == 0) { if (!__all(seen == tag)) flag_wait(kvflag, tag); }
            if (s + 2 < 8) AT_ISSUE(s + 2, fre);
            if (s < 4) {
                const int r0 = (idx * 8 + wid) * 9 + (s < 3 ? s * 4 : 0), rr = (kh ? 1280 : 0) + r0;
                const char* src = (kh == 0 && r0 >= 1280 ? (const char*)wo - 1280 * (DM * 2) : (const char*)w2) + (size_t)rr * (DM * 2) + (xcd >> 2) * 1024;
                const int rl = s == 2 ? 0 : (lane >> 4);
                touch<-AT_BASE>(src, ((kh == 0 && r0 < 1280 && r0 + rl >= 1280) ? 0 : rl) * (DM * 2) + (lane & 15) * 64, sdst);
            }
            if (s == 5) { if (wid == 0 && lane == 0) flag_st(flags + F_H + rt * 32 + ct, tag); }
            if (s == 6) seen = flag_ld(hflag);
            if (s == 7 && wid >= 4) {
                if (!__all(seen == tag)) flag_wait(hflag, tag);
                const unsigned vA = (wid - 4) * 2 * KB_AH * 1024 + lane * 16, vB = ((wid - 4) * 16 + (lane >> 3)) * (DM * 2) + (((lane & 7) ^ (2 * ((lane >> 4) & 3))) * 16);
                g128_issue(lds0, g2, 0, 0, wid - 4, vA, vB); g128_issue(lds0, g2, 1, 1, wid - 4, vA, vB);
            }
            if (wid >= 4) __builtin_amdgcn_s_setprio(1);
            const LAS char* kc = kb0 + cur; const LAS char* vc = vb0 + cur; const LAS char* kn = kb0 + nxt; const LAS char* vn = vb0 + nxt;
            AT_STEP(SA, SB, kc + 2 * 2048, vc + 1024, vc + 1 * 2048);            AT_STEP(SB, SA, kc + 3 * 2048, vc + 1 * 2048 + 1024, vc + 2 * 2048);
            H_MAIN(0);
            AT_STEP(SA, SB, kc + 4 * 2048, vc + 2 * 2048 + 1024, vc + 3 * 2048); AT_STEP(SB, SA, kc + 5 * 2048, vc + 3 * 2048 + 1024, vc + 4 * 2048);
            H_EXT();
            AT_STEP(SA, SB, kc + 6 * 2048, vc + 4 * 2048 + 1024, vc + 5 * 2048); __builtin_amdgcn_s_setprio(0); AT_STEP(SB, SA, kc + 7 * 2048, vc + 5 * 2048 + 1024, vc + 6 * 2048);
            H_MAIN(1);
            AT_STEP(SA, SB, kn, vc + 6 * 2048 + 1024, vc + 7 * 2048);            AT_STEP(SB, SA, kn + 2048, vc + 7 * 2048 + 1024, vn);
            const int tmp = cur; cur = nxt; nxt = fre; fre = tmp;
        }
        O = MFMA32(v1, BC8(p1), O); L = MFMA16(sel, BC8(p1), L);
        TS(9);
        float lrow; { const float a = __shfl(L[0], lane & 15), b = __shfl(L[1], lane & 15); lrow = (lane & 16) ? b : a; }
        if (__any(!(lrow < 1.0e18f))) {
            __builtin_amdgcn_fence(__ATOMIC_ACQUIRE, "agent");
            float m = -INFINITY, l = 0.f;
#pragma unroll
            for (int i = 0; i < 16; ++i) { O[i] = 0.f; negm[i] = 0.f; }
            const u16* Vs = (const u16*)Vg;
            for (int t = 0; t < 64; ++t) { const int key0 = t * 32;
                k0 = *(const bf16x8*)(Kg + key0 * 64 + lane * 16); k1 = *(const bf16x8*)(Kg + key0 * 64 + 1024 + lane * 16);
#pragma unroll
                for (int j = 0; j < 8; ++j) { const int kk = key0 + 8 * (j >> 2) + 4 * hi + (j & 3); v0[j] = (short)Vs[kk * 32 + (lane & 31)]; v1[j] = (short)Vs[(kk + 16) * 32 + (lane & 31)]; }
                f32x16 S = MFMA32(k0, q0, negm); S = MFMA32(k1, q1, S);
                float mt = S[0];
#pragma unroll
                for (int i = 1; i < 16; ++i) mt = fmaxf(mt, S[i]);
                mt = fmaxf(mt, __shfl_xor(mt, 32));
                const float mn = fmaxf(m, mt), al = __builtin_amdgcn_exp2f(m - mn); m = mn;
                float ps = 0.f;
#pragma unroll
                for (int i = 0; i < 16; ++i) { S[i] = rbf(__builtin_amdgcn_exp2f(S[i] - mn)); ps += S[i]; O[i] *= al; }
                l = l * al + ps + __shfl_xor(ps, 32);
#pragma unroll
                for (int i = 0; i < 4; ++i) { p0[i] = cvtpk(S[2 * i], S[2 * i + 1]); p1[i] = cvtpk(S[8 + 2 * i], S[9 + 2 * i]); }
                O = MFMA32(v0, BC8(p0), O); O = MFMA32(v1, BC8(p1), O);
            }
            lrow = l;
        }
        {
            const float inv = 1.0f / lrow;
            unsigned pk[8];
#pragma unroll
            for (int i = 0; i < 8; ++i) pk[i] = cvtpk(O[2 * i] * inv, O[2 * i + 1] * inv);
            const unsigned dst = WS_AH + ((qb * 16 + wid * 2 + ((lane >> 4) & 1)) * KB_AH + head) * 1024 + (lane & 15) * 16 + hi * 256;
#pragma unroll
            for (int c = 0; c < 2; ++c) {
                auto r0 = __builtin_amdgcn_permlane32_swap(pk[4 * c + 0], pk[4 * c + 2], false, false);
                auto r1 = __builtin_amdgcn_permlane32_swap(pk[4 * c + 1], pk[4 * c + 3], false, false);
                ST16(((u32x4){r0[0], r1[0], r0[1], r1[1]}), dst + c * 512);
            }
        }
        if (wid >= 4) { WAIT_V(2); }
        WAIT_L(0); BAR;
        TS(10);
#undef AT_ISSUE
#undef AT_STEP
#undef H_MAIN
#undef H_EXT
#undef EX
#undef BC8
    }

    {
        const int tid = opaque_tid(), lane = tid & 63, tile = rt2 * 8 + ct2;
        GAS u64* myflag = flags + F_G2 + tile * 2 + kh; const GAS u64* pflag = flags + F_G2 + tile * 2 + (kh ^ 1);
        if (wid >= 4) {
            const int lw = wid - 4;
            const unsigned vA = lw * 2 * KB_AH * 1024 + lane * 16, vB = (lw * 16 + (lane >> 3)) * (DM * 2) + (((lane & 7) ^ (2 * ((lane >> 4) & 3))) * 16);
            g128_issue(lds0, g2, 2, 2, lw, vA, vB); g128_issue(lds0, g2, 3, 3, lw, vA, vB);
            WAIT_V(16);
            int slot = 0;
            for (int t = 0; t < NT2; ++t) {
                BAR;
                if (t == 0 && wid == 4 && lane == 0) flag_st(flags + F_AT + (idx & 7) * 32 + xcd * 4 + (idx >> 3), tag);
                if (t + 4 < NT2) {
                    if (t + 4 == g2.nsw) flag_wait(flags + F_AT + (rt2 >> 1) * 32 + (lane & 31), tag);
                    g128_issue(lds0, g2, t + 4, slot, lw, vA, vB); slot = slot == 3 ? 0 : slot + 1;
                    WAIT_V(16);
                } else if (t + 3 < NT2) { WAIT_V(8); } else { WAIT_V(0); }
            }
            BAR;
            if (wid == 4) { if (lane == 0) flag_st(myflag, tag); flag_wait(pflag, tag); }
            BAR;
            if (bid == 0 && wid == 4) {
#pragma unroll
                for (int i = 0; i < 4; ++i) flag_wait(flags + F_LN + i * 64 + lane, tag);
                if (lane == 0) flag_st(flags, tag);
            }
        } else {
            const int wm = wid >> 1, wn = wid & 1, g = lane >> 4, q = (lane & 15) >> 2, p = lane & 3, swz = (2 * g + (q >> 1)) & 3;
            const int aoff = wm * 8192 + lane * 16;
            const int boff0 = 16384 + (4 * g + q) * 128 + (((2 * wn) ^ swz) * 32) + p * 8, boff1 = boff0 ^ 32;
            f32x4 acc[4][4]; bf16x8 AX[4], AY[4];
            s16x4 XL[4], XH[4], YL[4], YH[4];
            int pa[2] = {aoff, aoff + 65536}, pb0[2] = {boff0, boff0 + 65536}, pb1[2] = {boff1, boff1 + 65536};
            PIN(pa[1]); PIN(pb0[1]); PIN(pb1[1]);
#define G_BP(n, sl, kk) (lds + (((n) & 1) ? pb1[(sl) >> 1] : pb0[(sl) >> 1]) + ((sl) & 1) * 32768 + ((n) >> 1) * 8192 + (kk) * 4096)
#define G_BL(L_, n, sl, kk) do { L_[n] = trd(G_BP(n, sl, kk)); SCHED; } while (0)
#define G_BH(H_, n, sl, kk) do { H_[n] = trd(G_BP(n, sl, kk) + 2048); SCHED; } while (0)
#define G_A(A_, m, sl, kk) do { A_[m] = *(const LAS bf16x8*)(lds + pa[(sl) >> 1] + ((sl) & 1) * 32768 + ((m) * 2 + (kk)) * 1024); SCHED; } while (0)
#define G_M(A_, L_, H_, m, n) do { MFMA16_ACC(acc[m][n], cat8(L_[n], H_[n]), A_[m]); SCHED; } while (0)
#define G_REST(AC, CL, CH, AN, NL, NH, sl, kk) do { \
        G_BL(NL, 0, sl, kk); G_M(AC, CL, CH, 0, 2); G_BH(NH, 0, sl, kk); G_M(AC, CL, CH, 0, 3); G_A(AN, 0, sl, kk); G_M(AC, CL, CH, 1, 0); G_BL(NL, 1, sl, kk); G_M(AC, CL, CH, 1, 1); \
        G_BH(NH, 1, sl, kk); G_M(AC, CL, CH, 1, 2); G_BL(NL, 2, sl, kk); G_M(AC, CL, CH, 1, 3); G_BH(NH, 2, sl, kk); G_M(AC, CL, CH, 2, 0); G_BL(NL, 3, sl, kk); G_M(AC, CL, CH, 2, 1); \
        G_BH(NH, 3, sl, kk); G_M(AC, CL, CH, 2, 2); G_A(AN, 1, sl, kk); G_M(AC, CL, CH, 2, 3); G_A(AN, 2, sl, kk); G_M(AC, CL, CH, 3, 0); G_A(AN, 3, sl, kk); G_M(AC, CL, CH, 3, 1); \
        G_M(AC, CL, CH, 3, 2); G_M(AC, CL, CH, 3, 3); } while (0)
#define G_TILE(sl) do { G_M(AX, XL, XH, 0, 0); G_M(AX, XL, XH, 0, 1); G_REST(AX, XL, XH, AY, YL, YH, sl, 1); \
        G_M(AY, YL, YH, 0, 0); G_M(AY, YL, YH, 0, 1); \
        asm volatile("s_waitcnt vmcnt(0) lgkmcnt(0)" ::: "memory"); BAR; SCHED;        \
        G_REST(AY, YL, YH, AX, XL, XH, ((sl) + 1) & 3, 0); } while (0)
#pragma unroll
            for (int m = 0; m < 4; ++m)
#pragma unroll
                for (int n = 0; n < 4; ++n) { acc[m][n] = (f32x4){0.f, 0.f, 0.f, 0.f}; PIN(acc[m][n]); }
            SCHED;
#pragma unroll
            for (int i = 0; i < 4; ++i) { G_BL(XL, i, 0, 0); G_BH(XH, i, 0, 0); G_A(AX, i, 0, 0); }
            for (int t = 0; t < NT2; t += 4) { G_TILE(0); G_TILE(1); G_TILE(2); G_TILE(3); }
#undef G_BP
#undef G_BL
#undef G_BH
#undef G_A
#undef G_M
#undef G_REST
#undef G_TILE
            asm volatile("s_nop 7"); SCHED;
            TS(11);
            const int fr = lane & 15, fq = lane >> 4;
            const size_t rowo = (size_t)(rt2 * 128 + wm * 64 + kh * 32 + fr) * DM + ct2 * 128 + wn * 32 + (fq & 1) * 16 + (fq >> 1) * 8;
            const unsigned pbase = WS_PART + tile * 32768 + tid * 16;
            u32x4 xv[2][2]; f32x4 keep[2][4];
            if (kh == 0) {
#pragma unroll
                for (int m = 0; m < 2; ++m)
#pragma unroll
                    for (int s = 0; s < 2; ++s) { ST16(pack8(acc[2 + m][2 * s], acc[2 + m][2 * s + 1]), pbase + (m * 2 + s) * 4096);
                        keep[m][2 * s] = acc[m][2 * s]; keep[m][2 * s + 1] = acc[m][2 * s + 1]; }
            } else {
#pragma unroll
                for (int m = 0; m < 2; ++m)
#pragma unroll
                    for (int s = 0; s < 2; ++s) { ST16(pack8(acc[m][2 * s], acc[m][2 * s + 1]), pbase + 16384 + (m * 2 + s) * 4096);
                        keep[m][2 * s] = acc[2 + m][2 * s]; keep[m][2 * s + 1] = acc[2 + m][2 * s + 1]; }
            }
            WAIT_V(0); BAR;
            TS(12);
#pragma unroll
            for (int m = 0; m < 2; ++m)
#pragma unroll
                for (int s = 0; s < 2; ++s) xv[m][s] = *(const u32x4*)(x + rowo + (size_t)m * 16 * DM + s * 64);
            CBAR; BAR; CBAR;
            TS(13);
            u32x4 pv[2][2];
#pragma unroll
            for (int m = 0; m < 2; ++m)
#pragma unroll
                for (int s = 0; s < 2; ++s) pv[m][s] = LD16(pbase + (kh ^ 1) * 16384 + (m * 2 + s) * 4096);
#pragma unroll
            for (int m = 0; m < 2; ++m)
#pragma unroll
                for (int s = 0; s < 2; ++s) { float lo[4], hi4[4]; const u32x4 p_ = pv[m][s], x_ = xv[m][s];
                    const f32x4 a0 = keep[m][2 * s] + (f32x4){bf_lo(p_[0]), bf_hi(p_[0]), bf_lo(p_[1]), bf_hi(p_[1])}, a1 = keep[m][2 * s + 1] + (f32x4){bf_lo(p_[2]), bf_hi(p_[2]), bf_lo(p_[3]), bf_hi(p_[3])};
#pragma unroll
                    for (int i = 0; i < 4; ++i) { auto r = __builtin_amdgcn_permlane16_swap(__float_as_uint(a0[i]), __float_as_uint(a1[i]), false, false);
                        lo[i] = __uint_as_float(r[0]); hi4[i] = __uint_as_float(r[1]); }
                    u32x4 o; o[0] = cvtpk(lo[0] + bf_lo(x_[0]), lo[1] + bf_hi(x_[0])); o[1] = cvtpk(lo[2] + bf_lo(x_[1]), lo[3] + bf_hi(x_[1]));
                    o[2] = cvtpk(hi4[0] + bf_lo(x_[2]), hi4[1] + bf_hi(x_[2])); o[3] = cvtpk(hi4[2] + bf_lo(x_[3]), hi4[3] + bf_hi(x_[3]));
                    __builtin_nontemporal_store(o, (u32x4*)(out + rowo + (size_t)m * 16 * DM + s * 64));
                }
        }
        TS(14); TR(31);
    }
}

extern "C" void kernel_launch(void* const* d_in, const int* in_sizes, int n_in, void* d_out, int out_size, void* d_ws, size_t ws_size, hipStream_t stream) {
    (void)hipFuncSetAttribute((const void*)k_block, hipFuncAttributeMaxDynamicSharedMemorySize, LDS_BYTES);
    k_block<<<256, 512, LDS_BYTES, stream>>>((const u16*)d_in[0], (const u16*)d_in[1], (const u16*)d_in[2], (const u16*)d_in[4], (const u16*)d_in[5], (const u16*)d_in[6], (char*)d_ws, (u16*)d_out);
}
```

```cpp
#include <hip/hip_runtime.h>
#include <hip/hip_bf16.h>
#include <stdint.h>

#define LAS __attribute__((address_space(3)))
#define GAS __attribute__((address_space(1)))
#define DI __device__ __forceinline__
typedef unsigned short u16;
typedef unsigned long long u64;
typedef short bf16x8 __attribute__((ext_vector_type(8)));
typedef short s16x4 __attribute__((ext_vector_type(4)));
typedef float f32x2 __attribute__((ext_vector_type(2)));
typedef float f32x4 __attribute__((ext_vector_type(4)));
typedef float f32x16 __attribute__((ext_vector_type(16)));
typedef unsigned u32x2 __attribute__((ext_vector_type(2)));
typedef unsigned u32x4 __attribute__((ext_vector_type(4)));
typedef __bf16 bf16x2_t __attribute__((ext_vector_type(2)));

constexpr int SEQ = 2048, DM = 1024, FF = 3584;
constexpr int KB_X = DM / 32;
constexpr int KB_AH = (DM + FF) / 32;
constexpr unsigned WS_XN = 0;
constexpr unsigned WS_Q = WS_XN + SEQ * DM * 2;
constexpr unsigned WS_K = WS_Q + SEQ * DM * 2;
constexpr unsigned WS_V = WS_K + SEQ * DM * 2;
constexpr unsigned WS_AH = WS_V + SEQ * DM * 2;
constexpr unsigned WS_PART = WS_AH + SEQ * (DM + FF) * 2;
constexpr unsigned WS_FLAG = WS_PART + 128 * 32768;
constexpr unsigned WS_END = WS_FLAG + 16384;
constexpr int F_LN = 32, F_QKV = F_LN + 256, F_H = F_QKV + 256, F_AT = F_H + 256, F_G2 = F_AT + 256;
constexpr int LDS_BYTES = 163840;
constexpr unsigned CODE_BYTES = 32768;

DI unsigned cvtpk(float lo, float hi) { f32x2 v = {lo, hi}; bf16x2_t b = __builtin_convertvector(v, bf16x2_t); return __builtin_bit_cast(unsigned, b); }
DI float bf_lo(unsigned u) { return __uint_as_float(u << 16); }
DI float bf_hi(unsigned u) { return __uint_as_float(u & 0xffff0000u); }
DI float rbf(float f) { return __uint_as_float(cvtpk(f, f) << 16); }
DI s16x4 trd(const LAS char* p) { return __builtin_amdgcn_ds_read_tr16_b64_v4i16((LAS s16x4*)p); }
DI bf16x8 cat8(s16x4 a, s16x4 b) { return __builtin_shufflevector(a, b, 0, 1, 2, 3, 4, 5, 6, 7); }
template <int OFF, int D> DI void glds(const char* base, unsigned voff, unsigned dst) {
    asm volatile("s_add_u32 m0, %2, %4\n\ts_nop 0\n\tglobal_load_lds_dwordx4 %0, %1 offset:%3" :: "v"(voff), "s"(base), "s"(dst), "i"(OFF), "i"(D - OFF) : "memory", "scc"); }
template <int D> DI void glds_pair_sc1(const char* base, unsigned voff, unsigned dst) {
    asm volatile("s_add_u32 m0, %2, %3\n\ts_nop 0\n\tglobal_load_lds_dwordx4 %0, %1 sc1\n\tglobal_load_lds_dwordx4 %0, %1 offset:1024 sc1" :: "v"(voff), "s"(base), "s"(dst), "i"(D) : "memory", "scc"); }
template <int D> DI void touch(const char* base, unsigned voff, unsigned dst) {
    asm volatile("s_add_u32 m0, %2, %3\n\ts_nop 0\n\tglobal_load_lds_dword %0, %1" :: "v"(voff), "s"(base), "s"(dst), "i"(D) : "memory", "scc"); }
#define WAIT_V(n) asm volatile("s_waitcnt vmcnt(" #n ")" ::: "memory")
#define WAIT_L(n) asm volatile("s_waitcnt lgkmcnt(" #n ")" ::: "memory")
#define BAR __builtin_amdgcn_s_barrier()
#define CBAR asm volatile("" ::: "memory")
#define SCHED __builtin_amdgcn_sched_barrier(0)
#define PIN(x) asm volatile("" : "+v"(x))
#define MFMA16(a, b, c) __builtin_amdgcn_mfma_f32_16x16x32_bf16(a, b, c, 0, 0, 0)
#define MFMA16_ACC(c, a, b) asm volatile("v_mfma_f32_16x16x32_bf16 %0, %1, %2, %0" : "+v"(c) : "v"(a), "v"(b))
#define MFMA32(a, b, c) __builtin_amdgcn_mfma_f32_32x32x16_bf16(a, b, c, 0, 0, 0)
#define ST16(v, off) __builtin_amdgcn_raw_buffer_store_b128(v, rs, (int)(off), 0, 16)
#define ST8(v, off) __builtin_amdgcn_raw_buffer_store_b64(v, rs, (int)(off), 0, 16)
#define LD16(off) __builtin_amdgcn_raw_buffer_load_b128(rs, (int)(off), 0, 16)
DI u64 flag_ld(const GAS u64* p) { return __hip_atomic_load(p, __ATOMIC_RELAXED, __HIP_MEMORY_SCOPE_AGENT); }
DI void flag_st(GAS u64* p, u64 v) { __hip_atomic_store(p, v, __ATOMIC_RELAXED, __HIP_MEMORY_SCOPE_AGENT); }
DI void flag_wait(const GAS u64* p, u64 tag) {
    for (unsigned spins = 0; spins < (1u << 18); ++spins) { if (__all(flag_ld(p) == tag)) break; __builtin_amdgcn_s_sleep(1); }
    CBAR;
}
template <int N> DI float ror_add(float v) { return v + __builtin_bit_cast(float, __builtin_amdgcn_update_dpp(0, __builtin_bit_cast(int, v), 0x120 + N  , 0xf, 0xf, false)); }
DI float sum64(float v) {
    v = ror_add<1>(ror_add<2>(ror_add<4>(ror_add<8>(v))));
    { auto r = __builtin_amdgcn_permlane16_swap(__float_as_uint(v), __float_as_uint(v), false, false); v = __uint_as_float(r[0]) + __uint_as_float(r[1]); }
    { auto r = __builtin_amdgcn_permlane32_swap(__float_as_uint(v), __float_as_uint(v), false, false); v = __uint_as_float(r[0]) + __uint_as_float(r[1]); }
    return v;
}
DI int opaque_tid() { int t = threadIdx.x; asm volatile("" : "+v"(t)); return t; }

#define TQ(i)
#define TQOUT
#define TS(i)
#define TR(i)

DI float silu_mul(float g, float u) { return g * u * __builtin_amdgcn_rcpf(1.0f + __builtin_amdgcn_exp2f(g * -1.4426950408889634f)); }
DI u32x4 pack8(const f32x4& a, const f32x4& b) { u32x4 o; o[0] = cvtpk(a[0], a[1]); o[1] = cvtpk(a[2], a[3]); o[2] = cvtpk(b[0], b[1]); o[3] = cvtpk(b[2], b[3]); return o; }
constexpr float QSCALE = 0.17677669529663687f * 1.4426950408889634f;

struct G128 { const char* a; const char* w2; const char* wo; int kh; };
template <bool HALF> DI void g128_issue(unsigned lds0, const G128& g, int t, int lw, unsigned voffA, unsigned voffB) {
    const unsigned so = lds0 + (t & 3) * 32768, sb = so + lw * 2048;
    const bool att = !HALF && g.kh == 0 && t >= 16;
    const int kt = HALF ? t - 16 : att ? t - 16 : g.kh * 24 + t;
    const char* a = g.a + (size_t)(att ? kt : 16 + kt) * 2048; const char* b = (att ? g.wo : g.w2) + (size_t)kt * (64 * DM * 2);
    if (HALF) glds_pair_sc1<0>(a, voffA, so + (lw >> 1) * 8192 + (lw & 1) * 2048);
    else { glds_pair_sc1<0>(a, voffA, so + lw * 4096); glds_pair_sc1<2048>(a + KB_AH * 1024, voffA, so + lw * 4096); }
    glds<0, 16384>(b, voffB, sb); glds<128, 24576>(b, voffB, sb);
    glds<0, 16384 + 1024>(b + 8 * DM * 2, voffB, sb); glds<128, 24576 + 1024>(b + 8 * DM * 2, voffB, sb);
}

__global__ __launch_bounds__(512, 2) void k_block(const u16* __restrict__ x, const u16* __restrict__ wqkv, const u16* __restrict__ wo, const u16* __restrict__ w1,
                                                  const u16* __restrict__ w3, const u16* __restrict__ w2, char* __restrict__ ws, u16* __restrict__ out) {
    extern __shared__ __attribute__((aligned(16))) char smem[];
    LAS char* lds = (LAS char*)smem;
    const unsigned lds0 = (unsigned)(size_t)lds;
    const int wid = __builtin_amdgcn_readfirstlane(threadIdx.x >> 6);
    const int bid = blockIdx.x, xcd = bid & 7, idx = bid >> 3;
    GAS u64* flags = (GAS u64*)(ws + WS_FLAG);
    const __amdgpu_buffer_rsrc_t rs = __builtin_amdgcn_make_buffer_rsrc((void*)ws, 0, WS_END, 0x00020000);
    {
        const char* pc; asm volatile("s_getpc_b64 %0" : "=s"(pc));
        const int l = threadIdx.x & 63, piece = (idx * 8 + wid) & 7;
        const unsigned o = wid == 0 ? l * (CODE_BYTES / 64) : piece * 4096 + l * 64;
        touch<148480>(pc, o < CODE_BYTES ? o : 0, lds0 + wid * 1024);
    }
    TR(30); TS(0);
    u64 tag;
    asm volatile("s_load_dwordx2 %0, %1, 0x0" : "=s"(tag) : "s"(ws + WS_FLAG) : "memory");
    const int cj = idx >> 2, ct = cj < 3 ? (xcd >> 1) * 3 + cj : 12 + (xcd >> 1) * 5 + (cj - 3), rt = (xcd & 1) * 4 + (idx & 3);
    const bool qkv = ct < 12;
    u32x4 hmain[8]; u32x2 hext[4];

    {
        const int tid = opaque_tid(), lane = tid & 63, wr = wid >> 2, wc = wid & 3;
        const unsigned ldb2 = qkv ? 3 * DM * 2 : FF * 2;
        const char* cB0 = qkv ? (const char*)wqkv + ct * 512 : (const char*)w1 + (ct - 12) * 256;
        const char* cB1 = qkv ? cB0 + 256 : (const char*)w3 + (ct - 12) * 256;
        const char* cA = ws + WS_XN + rt * 16 * KB_X * 1024;
        const unsigned voffA = wid * (KB_X * 1024) + lane * 16;
        const unsigned voffB = (unsigned)(wid * 8 + (lane >> 3)) * ldb2 + (((lane & 7) ^ (2 * ((lane >> 4) & 3))) * 16);
        const char* pE = ((lane & 1) ? (const char*)w3 : (const char*)w1) + (size_t)(wid * 8 + (lane >> 3)) * (FF * 2) + (2560 + ct * 32) * 2 + ((((lane >> 1) & 3) ^ ((lane >> 4) & 3)) * 16);
        const size_t kstepB = (size_t)64 * ldb2;
        const unsigned ldsA = lds0 + wid * 2048, ldsB = lds0 + wid * 1024;
        const char* cT = ((idx & 2) ? cB1 : cB0) + (idx & 1) * 128;
#define SA(b, h) (((b) * 2 + (h)) * 16384)
#define SB(b, h) (((b) * 2 + (h)) * 16384)
#define STA(b, h, kt) glds_pair_sc1<SA(b, h)>(cA + (h) * 8 * KB_X * 1024 + (kt) * 2048, voffA, ldsA)
#define STB(b, h, kt) do { const char* s_ = ((h) ? cB1 : cB0) + (size_t)(kt) * kstepB; \
        glds<0, 65536 + SB(b, h)>(s_, voffB, ldsB); glds<128, 65536 + SB(b, h) + 8192>(s_, voffB, ldsB); } while (0)
#define TCH(kt) touch<148480>(cT + (size_t)(kt) * kstepB, voffB, ldsB)
#define STE(b, kt) do { const char* s_ = pE + (size_t)(kt) * (64 * FF * 2); \
        asm volatile("s_add_u32 m0, %1, %2\n\ts_nop 0\n\tglobal_load_lds_dwordx4 %0, off" :: "v"(s_), "s"(ldsB), "i"(131072 + (b) * 8192) : "memory", "scc"); } while (0)
        {
            u32x4 xa, xb;
            { const char* xp = (const char*)x + (size_t)(bid * 8 + wid) * (DM * 2);
              asm volatile("global_load_dwordx4 %0, %2, %3\n\tglobal_load_dwordx4 %1, %2, %3 offset:1024" : "=&v"(xa), "=&v"(xb) : "v"(lane * 16), "s"(xp) : "memory"); }
            STB(0, 0, 0); STB(0, 1, 0); STE(0, 0);
            asm volatile("s_waitcnt vmcnt(5) lgkmcnt(0)" : "+v"(xa), "+v"(xb), "+s"(tag) :: "memory");
            tag += 1;
            float f[16];
#pragma unroll
            for (int i = 0; i < 4; ++i) { f[2 * i] = bf_lo(xa[i]); f[2 * i + 1] = bf_hi(xa[i]); f[8 + 2 * i] = bf_lo(xb[i]); f[9 + 2 * i] = bf_hi(xb[i]); }
            float s = 0.f;
#pragma unroll
            for (int i = 0; i < 16; ++i) s += f[i];
            const float mean = sum64(s) * (1.0f / DM);
            float q = 0.f;
#pragma unroll
            for (int i = 0; i < 16; ++i) { const float d = f[i] - mean; q += d * d; }
            const float var = sum64(q) * (1.0f / DM);
            const float mb = rbf(mean), sd = rbf(__builtin_amdgcn_sqrtf(rbf(rbf(var) + 1e-5f))), inv = __builtin_amdgcn_rcpf(sd);
#pragma unroll
            for (int c = 0; c < 2; ++c) { u32x4 o;
#pragma unroll
                for (int i = 0; i < 4; ++i) o[i] = cvtpk(rbf(f[c * 8 + 2 * i] - mb) * inv, rbf(f[c * 8 + 2 * i + 1] - mb) * inv);
                *(LAS u32x4*)(lds + wid * 2080 + c * 1024 + lane * 16) = o; }
            WAIT_L(0); BAR; CBAR;
            const int r = lane & 7, g = (lane >> 3) & 3, row = bid * 8 + r;
#pragma unroll
            for (int c = 0; c < 2; ++c) { const int kb = 4 * wid + 2 * c + (lane >> 5); const LAS char* p = lds + r * 2080 + kb * 64 + g * 8;
                const u32x2 lo = *(const LAS u32x2*)p, hi = *(const LAS u32x2*)(p + 32);
                ST16(((u32x4){lo[0], lo[1], hi[0], hi[1]}), WS_XN + ((row >> 4) * KB_X + kb) * 1024 + (g * 16 + (row & 15)) * 16); }
            WAIT_V(0); BAR;
            TS(1);
            if (wid == 0) { if (lane == 0) flag_st(flags + F_LN + bid, tag); flag_wait(flags + F_LN + rt * 32 + (lane & 31), tag); }
            STB(1, 0, 1); STB(1, 1, 1); STE(1, 1);
            if (wid != 0) { TCH(2); TCH(3); }
            BAR; CBAR;
            TS(2);
        }
        f32x4 acc[2][2][4][2], ace[2][4];
        {
            const int g = lane >> 4, q = (lane & 15) >> 2, p = lane & 3, swz = (2 * g + (q >> 1)) & 3;
            const int aoff = wr * 8192 + lane * 16;
            const int boff0 = 65536 + (wc >> 1) * 8192 + (4 * g + q) * 128 + (((2 * (wc & 1)) ^ swz) * 32) + p * 8, boff1 = boff0 ^ 32;
            int eoff = 131072 + (4 * g + q) * 128 + ((wc ^ swz) * 32) + p * 8; PIN(eoff);
#define LDA(dst, b, h) do { _Pragma("unroll") for (int m = 0; m < 4; ++m) _Pragma("unroll") for (int k = 0; k < 2; ++k) \
        dst[m][k] = *(const LAS bf16x8*)(lds + aoff + SA(b, h) + (m * 2 + k) * 1024); } while (0)
#define LDB(dst, b, h) do { _Pragma("unroll") for (int n = 0; n < 2; ++n) _Pragma("unroll") for (int k = 0; k < 2; ++k) { \
        const LAS char* b_ = lds + (n ? boff1 : boff0) + SB(b, h) + k * 4096; dst[n][k] = cat8(trd(b_), trd(b_ + 2048)); } } while (0)
#define LDE(dst, b) do { _Pragma("unroll") for (int k = 0; k < 2; ++k) { const LAS char* b_ = lds + eoff + (b) * 8192 + k * 4096; dst[k] = cat8(trd(b_), trd(b_ + 2048)); } } while (0)
#define MMA(ai) do { __builtin_amdgcn_s_setprio(1); \
        _Pragma("unroll") for (int k = 0; k < 2; ++k) _Pragma("unroll") for (int m = 0; m < 4; ++m) { \
            acc[ai][0][m][0] = MFMA16(B0[0][k], At[m][k], acc[ai][0][m][0]); acc[ai][0][m][1] = MFMA16(B0[1][k], At[m][k], acc[ai][0][m][1]); \
            acc[ai][1][m][0] = MFMA16(B1[0][k], At[m][k], acc[ai][1][m][0]); acc[ai][1][m][1] = MFMA16(B1[1][k], At[m][k], acc[ai][1][m][1]); \
            ace[ai][m] = MFMA16(BE[k], At[m][k], ace[ai][m]); } \
        __builtin_amdgcn_s_setprio(0); } while (0)
            STA(0, 0, 0); STA(0, 1, 0); STA(1, 0, 1);
#pragma unroll
            for (int a = 0; a < 2; ++a)
#pragma unroll
                for (int m = 0; m < 4; ++m) { ace[a][m] = (f32x4){0.f, 0.f, 0.f, 0.f};
#pragma unroll
                    for (int b = 0; b < 2; ++b)
#pragma unroll
                        for (int n = 0; n < 2; ++n) acc[a][b][m][n] = (f32x4){0.f, 0.f, 0.f, 0.f}; }
            bf16x8 At[4][2], B0[2][2], B1[2][2], BE[2];
            constexpr int nt = DM / 64;
            if (wr == 1) BAR;
            WAIT_V(4); BAR;
            WAIT_V(2); BAR;
            TS(3);
            for (int t = 0; t < nt; t += 2) {
                const int t2 = t + 2 < nt ? t + 2 : t, tt = t + 4 < nt ? t + 4 : t;
                LDB(B0, 0, 0); LDB(B1, 0, 1); LDE(BE, 0); SCHED; LDA(At, 0, 0); STA(1, 1, t + 1); TCH(tt);
                WAIT_V(10); WAIT_L(0); BAR; MMA(0); BAR; SCHED;
                LDA(At, 0, 1); STB(0, 0, t2); STB(0, 1, t2); STE(0, t2); STA(0, 0, t2);
                WAIT_V(10); WAIT_L(0); BAR; MMA(1); BAR; SCHED;
                LDB(B0, 1, 0); LDB(B1, 1, 1); LDE(BE, 1); SCHED; LDA(At, 1, 0); STA(0, 1, t2); TCH(tt + 1);
                WAIT_V(10); WAIT_L(0); BAR; MMA(0); BAR; SCHED;
                LDA(At, 1, 1); STB(1, 0, t2 + 1); STB(1, 1, t2 + 1); STE(1, t2 + 1); STA(1, 0, t2 + 1);
                WAIT_V(10); WAIT_L(0); BAR; MMA(1); if (t + 2 < nt || wr == 0) BAR; SCHED;
            }
            TS(4);
#undef SA
#undef SB
#undef STA
#undef STB
#undef STE
#undef TCH
#undef LDA
#undef LDB
#undef LDE
#undef MMA
        }
        const int ln = opaque_tid() & 63, fr = ln & 15, fq = ln >> 4;
        if (qkv) {
            const int kind = ct >> 2;
            const float sc = kind == 0 ? QSCALE : 1.0f;
            const unsigned base = WS_Q + kind * (SEQ * DM * 2) + ((ct & 3) * 8 + wc) * 131072 + (rt * 256 + wr * 64) * 64;
            const unsigned lo0 = base + (kind == 2 ? fr * 64 + fq * 16 : fr * 16 + fq * 512), lo1 = lo0 + (kind == 2 ? 1024 : 256);
#pragma unroll
            for (int bj = 0; bj < 2; ++bj)
#pragma unroll
                for (int ai = 0; ai < 2; ++ai)
#pragma unroll
                    for (int m = 0; m < 4; ++m)
                        ST16(pack8(acc[ai][bj][m][0] * sc, acc[ai][bj][m][1] * sc), bj * 4 * 131072 + ai * 8192 + (m >> 1) * 2048 + ((m & 1) ? lo1 : lo0));
            WAIT_V(0); BAR;
            if (wid == 0 && ln == 0) flag_st(flags + F_QKV + rt * 32 + ct, tag);
        } else {
#pragma unroll
            for (int ai = 0; ai < 2; ++ai)
#pragma unroll
                for (int m = 0; m < 4; ++m) { f32x4 h0, h1;
#pragma unroll
                    for (int j = 0; j < 4; ++j) { h0[j] = silu_mul(acc[ai][0][m][0][j], acc[ai][1][m][0][j]); h1[j] = silu_mul(acc[ai][0][m][1][j], acc[ai][1][m][1][j]); }
                    hmain[ai * 4 + m] = pack8(h0, h1);
                }
        }
#pragma unroll
        for (int ai = 0; ai < 2; ++ai)
#pragma unroll
            for (int mp = 0; mp < 2; ++mp) { float h[4];
#pragma unroll
                for (int j = 0; j < 4; ++j) { auto r = __builtin_amdgcn_permlane32_swap(__float_as_uint(ace[ai][2 * mp][j]), __float_as_uint(ace[ai][2 * mp + 1][j]), false, false);
                    h[j] = silu_mul(__uint_as_float(r[0]), __uint_as_float(r[1])); }
                hext[ai * 2 + mp] = (u32x2){cvtpk(h[0], h[1]), cvtpk(h[2], h[3])};
            }
        TS(5);
        WAIT_V(0);
        TS(6);
    }

    const int kh = xcd & 1, rt2 = ((xcd >> 1) & 1) * 8 + (idx & 7), ct2 = (xcd >> 2) * 4 + (idx >> 3);
    G128 g2; g2.kh = kh; g2.a = ws + WS_AH + rt2 * 8 * KB_AH * 1024; g2.w2 = (const char*)w2 + ct2 * 256; g2.wo = (const char*)wo + ct2 * 256;
#define G2_OFFS(lw_, l_) const unsigned vA = ((lw_) ^ kh) * 2 * KB_AH * 1024 + (l_) * 16, vB = ((lw_) * 16 + ((l_) >> 3)) * (DM * 2) + ((((l_) & 7) ^ (2 * (((l_) >> 4) & 3))) * 16)

    {
        constexpr int AT_BASE = 65536, AT_STAGE = 32768;
        const int lane = opaque_tid() & 63, hi = lane >> 5;
        const int head = xcd * 4 + (idx >> 3), qb = idx & 7;
        if (wid == 0) flag_wait(flags + F_QKV + (lane < 4 ? (lane & 1) * 32 + 4 + (lane >> 1) * 4 : qb * 32) + (head >> 3), tag);
        BAR; CBAR;
        TS(7);
        const char* Kg = ws + WS_K + head * 131072; const char* Vg = ws + WS_V + head * 131072;
        const unsigned soff = wid * 2048 + lane * 16;
        const unsigned sdst = lds0 + AT_BASE + wid * 2048;
#define AT_ISSUE(s, slot) do { glds_pair_sc1<0>(Kg + (s) * 16384, soff, sdst + (slot)); glds_pair_sc1<16384>(Vg + (s) * 16384, soff, sdst + (slot)); } while (0)
        const unsigned qoff = WS_Q + (head * 64 + qb * 8 + wid) * 2048 + lane * 16;
        const bf16x8 q0 = __builtin_bit_cast(bf16x8, LD16(qoff)), q1 = __builtin_bit_cast(bf16x8, LD16(qoff + 1024));
        AT_ISSUE(0, 0); AT_ISSUE(1, AT_STAGE);
        const GAS u64* kvflag = flags + F_QKV + (lane & 7) * 32 + 4 + ((lane >> 3) & 1) * 4 + (head >> 3);
        u64 seen = flag_ld(kvflag);
        bf16x8 sel; { const short one = ((lane & 15) == ((lane >> 4) & 1)) ? (short)0x3f80 : (short)0;
#pragma unroll
            for (int i = 0; i < 8; ++i) sel[i] = one; }
        const LAS char* kb0 = lds + AT_BASE + lane * 16;
        const LAS char* vb0 = lds + AT_BASE + 16384 + (4 * hi + ((lane & 15) >> 2)) * 64 + ((lane >> 4) & 1) * 32 + (lane & 3) * 8;
        f32x16 O, negm, SA, SB; f32x4 L = {0.f, 0.f, 0.f, 0.f};
        bf16x8 k0, k1, v0, v1;
#pragma unroll
        for (int i = 0; i < 16; ++i) { O[i] = 0.f; negm[i] = 0.f; }
        WAIT_V(5); BAR;
        TS(8);
        {
            k0 = *(const LAS bf16x8*)(kb0); k1 = *(const LAS bf16x8*)(kb0 + 1024);
            SA = MFMA32(k0, q0, negm); SA = MFMA32(k1, q1, SA);
            k0 = *(const LAS bf16x8*)(kb0 + 2048); k1 = *(const LAS bf16x8*)(kb0 + 3072);
            v0 = cat8(trd(vb0), trd(vb0 + 512)); v1 = cat8(trd(vb0 + 1024), trd(vb0 + 1536));
            float m = SA[0];
#pragma unroll
            for (int i = 1; i < 16; ++i) m = fmaxf(m, SA[i]);
            m = fmaxf(m, __shfl_xor(m, 32));
#pragma unroll
            for (int i = 0; i < 16; ++i) { negm[i] = -m; SA[i] -= m; }
            PIN(negm);
        }
#define EX(S, i) S[i] = __builtin_amdgcn_exp2f(S[i])
#define BC8(x) __builtin_bit_cast(bf16x8, x)
#define AT_STEP(SC, SN, kp, v1p, v0p) do { \
        O = MFMA32(v1, BC8(p1), O); SCHED; v1 = cat8(trd(v1p), trd((v1p) + 512)); EX(SC, 0); EX(SC, 1); EX(SC, 2); PIN(SC); SCHED; \
        SN = MFMA32(k0, q0, negm); SCHED; k0 = *(const LAS bf16x8*)(kp); EX(SC, 3); EX(SC, 4); EX(SC, 5); PIN(SC); SCHED; \
        L = MFMA16(sel, BC8(p1), L); SCHED; EX(SC, 6); EX(SC, 7); \
        p0[0] = cvtpk(SC[0], SC[1]); p0[1] = cvtpk(SC[2], SC[3]); p0[2] = cvtpk(SC[4], SC[5]); p0[3] = cvtpk(SC[6], SC[7]); PIN(p0); SCHED; \
        O = MFMA32(v0, BC8(p0), O); SCHED; v0 = cat8(trd(v0p), trd((v0p) + 512)); EX(SC, 8); EX(SC, 9); EX(SC, 10); PIN(SC); SCHED; \
        SN = MFMA32(k1, q1, SN); SCHED; k1 = *(const LAS bf16x8*)((kp) + 1024); EX(SC, 11); EX(SC, 12); EX(SC, 13); PIN(SC); SCHED; \
        L = MFMA16(sel, BC8(p0), L); SCHED; EX(SC, 14); EX(SC, 15); \
        p1[0] = cvtpk(SC[8], SC[9]); p1[1] = cvtpk(SC[10], SC[11]); p1[2] = cvtpk(SC[12], SC[13]); p1[3] = cvtpk(SC[14], SC[15]); PIN(p1); SCHED; } while (0)
        u32x4 p0, p1 = {0u, 0u, 0u, 0u};
        int cur = 0, nxt = AT_STAGE, fre = 2 * AT_STAGE;
        const GAS u64* hflag = flags + F_H + (rt2 >> 1) * 32 + (lane & 31);
        const unsigned hb = WS_AH + ((rt * 16 + (wid >> 2) * 4) * KB_AH + KB_X + (ct - 12) * 4 + (wid & 3)) * 1024 + lane * 16;
        const int cq = 2 * (wid & 3) + ((lane >> 4) & 1);
        const unsigned eb = WS_AH + ((rt * 16 + (wid >> 2) * 4 + hi) * KB_AH + KB_X + 80 + ct) * 1024 + ((cq & 3) * 16 + (lane & 15)) * 16 + (cq >> 2) * 8;
#define H_MAIN(j) do { if (!qkv && s < 4) { if (s == 0) ST16(hmain[j], hb + (j) * KB_AH * 1024); else if (s == 1) ST16(hmain[2 + (j)], hb + (2 + (j)) * KB_AH * 1024); \
            else if (s == 2) ST16(hmain[4 + (j)], hb + (8 + (j)) * KB_AH * 1024); else ST16(hmain[6 + (j)], hb + (10 + (j)) * KB_AH * 1024); } SCHED; } while (0)
#define H_EXT() do { if (s < 4) { if (s == 0) ST8(hext[0], eb); else if (s == 1) ST8(hext[1], eb + 2 * KB_AH * 1024); \
            else if (s == 2) ST8(hext[2], eb + 8 * KB_AH * 1024); else ST8(hext[3], eb + 10 * KB_AH * 1024); } SCHED; } while (0)
        for (int s = 0; s < 8; ++s) {
            if (s >= 1 && s <= 4) { if (qkv) { WAIT_V(2); } else { WAIT_V(4); } } else { WAIT_V(0); }
            WAIT_L(0); BAR;
            if (s == 0) { if (!__all(seen == tag)) flag_wait(kvflag, tag); }
            if (s + 2 < 8) AT_ISSUE(s + 2, fre);
            if (s < 4) {
                const int r0 = (idx * 8 + wid) * 10 + (s < 3 ? s * 4 : 0), rl = s == 2 ? ((lane >> 4) & 1) : (lane >> 4);
                const char* src = (kh ? (const char*)w2 + 1024 * (DM * 2) : r0 >= 1536 ? (const char*)wo - 1536 * (DM * 2) : (const char*)w2) + (size_t)r0 * (DM * 2) + (xcd >> 2) * 1024;
                touch<-AT_BASE>(src, rl * (DM * 2) + (lane & 15) * 64, sdst);
            }
            if (s == 5) { if (wid == 0 && lane == 0) flag_st(flags + F_H + rt * 32 + ct, tag); }
            if (s == 6) seen = flag_ld(hflag);
            if (s == 7 && wid >= 4) {
                if (!__all(seen == tag)) flag_wait(hflag, tag);
                G2_OFFS(wid - 4, lane);
                g128_issue<false>(lds0, g2, 0, wid - 4, vA, vB); g128_issue<false>(lds0, g2, 1, wid - 4, vA, vB);
            }
            if (wid >= 4) __builtin_amdgcn_s_setprio(1);
            const LAS char* kc = kb0 + cur; const LAS char* vc = vb0 + cur; const LAS char* kn = kb0 + nxt; const LAS char* vn = vb0 + nxt;
            AT_STEP(SA, SB, kc + 2 * 2048, vc + 1024, vc + 1 * 2048);            AT_STEP(SB, SA, kc + 3 * 2048, vc + 1 * 2048 + 1024, vc + 2 * 2048);
            H_MAIN(0);
            AT_STEP(SA, SB, kc + 4 * 2048, vc + 2 * 2048 + 1024, vc + 3 * 2048); AT_STEP(SB, SA, kc + 5 * 2048, vc + 3 * 2048 + 1024, vc + 4 * 2048);
            H_EXT();
            AT_STEP(SA, SB, kc + 6 * 2048, vc + 4 * 2048 + 1024, vc + 5 * 2048); __builtin_amdgcn_s_setprio(0); AT_STEP(SB, SA, kc + 7 * 2048, vc + 5 * 2048 + 1024, vc + 6 * 2048);
            H_MAIN(1);
            AT_STEP(SA, SB, kn, vc + 6 * 2048 + 1024, vc + 7 * 2048);            AT_STEP(SB, SA, kn + 2048, vc + 7 * 2048 + 1024, vn);
            const int tmp = cur; cur = nxt; nxt = fre; fre = tmp;
        }
        O = MFMA32(v1, BC8(p1), O); L = MFMA16(sel, BC8(p1), L);
        TS(9);
        float lrow; { const float a = __shfl(L[0], lane & 15), b = __shfl(L[1], lane & 15); lrow = (lane & 16) ? b : a; }
        if (__any(!(lrow < 1.0e18f))) {
            __builtin_amdgcn_fence(__ATOMIC_ACQUIRE, "agent");
            float m = -INFINITY, l = 0.f;
#pragma unroll
            for (int i = 0; i < 16; ++i) { O[i] = 0.f; negm[i] = 0.f; }
            const u16* Vs = (const u16*)Vg;
            for (int t = 0; t < 64; ++t) { const int key0 = t * 32;
                k0 = *(const bf16x8*)(Kg + key0 * 64 + lane * 16); k1 = *(const bf16x8*)(Kg + key0 * 64 + 1024 + lane * 16);
#pragma unroll
                for (int j = 0; j < 8; ++j) { const int kk = key0 + 8 * (j >> 2) + 4 * hi + (j & 3); v0[j] = (short)Vs[kk * 32 + (lane & 31)]; v1[j] = (short)Vs[(kk + 16) * 32 + (lane & 31)]; }
                f32x16 S = MFMA32(k0, q0, negm); S = MFMA32(k1, q1, S);
                float mt = S[0];
#pragma unroll
                for (int i = 1; i < 16; ++i) mt = fmaxf(mt, S[i]);
                mt = fmaxf(mt, __shfl_xor(mt, 32));
                const float mn = fmaxf(m, mt), al = __builtin_amdgcn_exp2f(m - mn); m = mn;
                float ps = 0.f;
#pragma unroll
                for (int i = 0; i < 16; ++i) { S[i] = rbf(__builtin_amdgcn_exp2f(S[i] - mn)); ps += S[i]; O[i] *= al; }
                l = l * al + ps + __shfl_xor(ps, 32);
#pragma unroll
                for (int i = 0; i < 4; ++i) { p0[i] = cvtpk(S[2 * i], S[2 * i + 1]); p1[i] = cvtpk(S[8 + 2 * i], S[9 + 2 * i]); }
                O = MFMA32(v0, BC8(p0), O); O = MFMA32(v1, BC8(p1), O);
            }
            lrow = l;
        }
        {
            const float inv = 1.0f / lrow;
            unsigned pk[8];
#pragma unroll
            for (int i = 0; i < 8; ++i) pk[i] = cvtpk(O[2 * i] * inv, O[2 * i + 1] * inv);
            const unsigned dst = WS_AH + ((qb * 16 + wid * 2 + ((lane >> 4) & 1)) * KB_AH + head) * 1024 + (lane & 15) * 16 + hi * 256;
#pragma unroll
            for (int c = 0; c < 2; ++c) {
                auto r0 = __builtin_amdgcn_permlane32_swap(pk[4 * c + 0], pk[4 * c + 2], false, false);
                auto r1 = __builtin_amdgcn_permlane32_swap(pk[4 * c + 1], pk[4 * c + 3], false, false);
                ST16(((u32x4){r0[0], r1[0], r0[1], r1[1]}), dst + c * 512);
            }
        }
        if (wid >= 4) { WAIT_V(2); }
        WAIT_L(0); BAR;
        TS(10);
#undef AT_ISSUE
#undef AT_STEP
#undef H_MAIN
#undef H_EXT
#undef EX
#undef BC8
    }

    {
        const int tid = opaque_tid(), lane = tid & 63, tile = rt2 * 8 + ct2;
        GAS u64* myflag = flags + F_G2 + tile * 2 + kh; const GAS u64* pflag = flags + F_G2 + tile * 2 + (kh ^ 1);
        if (wid >= 4) {
            const int lw = wid - 4;
            G2_OFFS(lw, lane);
            const unsigned vH = ((lw >> 1) * 4 + 2 * kh + (lw & 1)) * KB_AH * 1024 + lane * 16;
            g128_issue<false>(lds0, g2, 2, lw, vA, vB); g128_issue<false>(lds0, g2, 3, lw, vA, vB);
            WAIT_V(16);
            for (int t = 0; t < 28; ++t) {
                BAR;
                if (t == 0 && wid == 4 && lane == 0) flag_st(flags + F_AT + (idx & 7) * 32 + xcd * 4 + (idx >> 3), tag);
                if (t == 12 && kh == 0) flag_wait(flags + F_AT + (rt2 >> 1) * 32 + (lane & 31), tag);
                g128_issue<false>(lds0, g2, t + 4, lw, vA, vB);
                WAIT_V(16);
            }
            BAR; g128_issue<true>(lds0, g2, 32, lw, vH, vB); WAIT_V(14);
            for (int t = 29; t < 35; ++t) {
                BAR;
                if (t == 34 && wid == 4 && lane == 0) flag_st(myflag, tag);
                g128_issue<true>(lds0, g2, t + 4, lw, vH, vB); WAIT_V(12);
            }
            BAR; g128_issue<true>(lds0, g2, 39, lw, vH, vB);
            const u64 seen = flag_ld(pflag);
            WAIT_V(13);
            BAR; WAIT_V(7);
            BAR; WAIT_V(0);
            const char* psrc = ws + WS_PART + tile * 32768 + (kh ^ 1) * 16384 + lw * 4096;
            const bool early = __all(seen == tag);
            if (early) { glds_pair_sc1<131072>(psrc, lane * 16, lds0 + lw * 4096); glds_pair_sc1<131072 + 2048>(psrc + 2048, lane * 16, lds0 + lw * 4096); }
            BAR;
            if (!early) { flag_wait(pflag, tag); glds_pair_sc1<131072>(psrc, lane * 16, lds0 + lw * 4096); glds_pair_sc1<131072 + 2048>(psrc + 2048, lane * 16, lds0 + lw * 4096); }
            WAIT_V(0); BAR;
            if (bid == 0 && wid == 4) {
#pragma unroll
                for (int i = 0; i < 4; ++i) flag_wait(flags + F_LN + i * 64 + lane, tag);
                if (lane == 0) flag_st(flags, tag);
            }
        } else {
            const int wm = wid >> 1, wn = wid & 1, g = lane >> 4, q = (lane & 15) >> 2, p = lane & 3, swz = (2 * g + (q >> 1)) & 3;
            const int aoff = wm * 8192 + lane * 16;
            const int boff0 = 16384 + (4 * g + q) * 128 + (((2 * wn) ^ swz) * 32) + p * 8, boff1 = boff0 ^ 32;
            f32x4 acc[4][4]; bf16x8 AX[4], AY[4];
            s16x4 XL[4], XH[4], YL[4], YH[4];
            int pa[2] = {aoff, aoff + 65536}, pb0[2] = {boff0, boff0 + 65536}, pb1[2] = {boff1, boff1 + 65536};
            PIN(pa[1]); PIN(pb0[1]); PIN(pb1[1]);
#define G_BP(n, sl, kk) (lds + (((n) & 1) ? pb1[(sl) >> 1] : pb0[(sl) >> 1]) + ((sl) & 1) * 32768 + ((n) >> 1) * 8192 + (kk) * 4096)
#define G_BL(L_, n, sl, kk) do { L_[n] = trd(G_BP(n, sl, kk)); SCHED; } while (0)
#define G_BH(H_, n, sl, kk) do { H_[n] = trd(G_BP(n, sl, kk) + 2048); SCHED; } while (0)
#define G_A(A_, m, sl, kk) do { A_[m] = *(const LAS bf16x8*)(lds + pa[(sl) >> 1] + ((sl) & 1) * 32768 + ((m) * 2 + (kk)) * 1024); SCHED; } while (0)
#define G_M(A_, L_, H_, m, n) do { MFMA16_ACC(acc[m][n], cat8(L_[n], H_[n]), A_[m]); SCHED; } while (0)
#define G_REST(AC, CL, CH, AN, NL, NH, sl, kk) do { \
        G_BL(NL, 0, sl, kk); G_M(AC, CL, CH, 0, 2); G_BH(NH, 0, sl, kk); G_M(AC, CL, CH, 0, 3); G_A(AN, 0, sl, kk); G_M(AC, CL, CH, 1, 0); G_BL(NL, 1, sl, kk); G_M(AC, CL, CH, 1, 1); \
        G_BH(NH, 1, sl, kk); G_M(AC, CL, CH, 1, 2); G_BL(NL, 2, sl, kk); G_M(AC, CL, CH, 1, 3); G_BH(NH, 2, sl, kk); G_M(AC, CL, CH, 2, 0); G_BL(NL, 3, sl, kk); G_M(AC, CL, CH, 2, 1); \
        G_BH(NH, 3, sl, kk); G_M(AC, CL, CH, 2, 2); G_A(AN, 1, sl, kk); G_M(AC, CL, CH, 2, 3); G_A(AN, 2, sl, kk); G_M(AC, CL, CH, 3, 0); G_A(AN, 3, sl, kk); G_M(AC, CL, CH, 3, 1); \
        G_M(AC, CL, CH, 3, 2); G_M(AC, CL, CH, 3, 3); } while (0)
#define G_TILE(sl) do { G_M(AX, XL, XH, 0, 0); G_M(AX, XL, XH, 0, 1); G_REST(AX, XL, XH, AY, YL, YH, sl, 1); \
        G_M(AY, YL, YH, 0, 0); G_M(AY, YL, YH, 0, 1); \
        asm volatile("s_waitcnt vmcnt(0) lgkmcnt(0)" ::: "memory"); BAR; SCHED;        \
        G_REST(AY, YL, YH, AX, XL, XH, ((sl) + 1) & 3, 0); } while (0)
#pragma unroll
            for (int m = 0; m < 4; ++m)
#pragma unroll
                for (int n = 0; n < 4; ++n) { acc[m][n] = (f32x4){0.f, 0.f, 0.f, 0.f}; PIN(acc[m][n]); }
            SCHED;
#pragma unroll
            for (int i = 0; i < 4; ++i) { G_BL(XL, i, 0, 0); G_BH(XH, i, 0, 0); G_A(AX, i, 0, 0); }
            for (int t = 0; t < 32; t += 4) { G_TILE(0); G_TILE(1); G_TILE(2); G_TILE(3); }
            asm volatile("s_nop 7"); SCHED;
            TS(11);
            const int fr = lane & 15, fq = lane >> 4;
            const size_t rowo = (size_t)(rt2 * 128 + wm * 64 + kh * 32 + fr) * DM + ct2 * 128 + wn * 32 + (fq & 1) * 16 + (fq >> 1) * 8;
            u32x4 xv[2][2];
#pragma unroll
            for (int m = 0; m < 2; ++m)
#pragma unroll
                for (int s = 0; s < 2; ++s) ST16(pack8(acc[2 + m][2 * s], acc[2 + m][2 * s + 1]), WS_PART + tile * 32768 + kh * 16384 + (m * 2 + s) * 4096 + tid * 16);
#pragma unroll
            for (int m = 0; m < 2; ++m)
#pragma unroll
                for (int s = 0; s < 2; ++s) xv[m][s] = *(const u32x4*)(x + rowo + (size_t)m * 16 * DM + s * 64);
            SCHED;
#define H_REST(AC, CL, CH, AN, NL, NH, sl, kk) do { \
        G_BL(NL, 0, sl, kk); G_BH(NH, 0, sl, kk); G_M(AC, CL, CH, 0, 2); G_A(AN, 0, sl, kk); G_BL(NL, 1, sl, kk); G_M(AC, CL, CH, 0, 3); G_BH(NH, 1, sl, kk); G_BL(NL, 2, sl, kk); G_M(AC, CL, CH, 1, 0); \
        G_BH(NH, 2, sl, kk); G_BL(NL, 3, sl, kk); G_M(AC, CL, CH, 1, 1); G_BH(NH, 3, sl, kk); G_M(AC, CL, CH, 1, 2); G_A(AN, 1, sl, kk); G_M(AC, CL, CH, 1, 3); } while (0)
#define H_TILE(sl, SYNC) do { G_M(AX, XL, XH, 0, 0); G_M(AX, XL, XH, 0, 1); H_REST(AX, XL, XH, AY, YL, YH, sl, 1); \
        G_M(AY, YL, YH, 0, 0); G_M(AY, YL, YH, 0, 1); SYNC; SCHED; H_REST(AY, YL, YH, AX, XL, XH, ((sl) + 1) & 3, 0); } while (0)
#define H_BAR do { WAIT_L(0); BAR; } while (0)
            TQ(0); H_TILE(0, H_BAR); TQ(1); H_TILE(1, H_BAR); TQ(2); H_TILE(2, WAIT_V(0); H_BAR); TQ(3); H_TILE(3, H_BAR); TQ(4);
            H_TILE(0, H_BAR); TQ(5); H_TILE(1, H_BAR); TQ(6); H_TILE(2, H_BAR); TQ(7); H_TILE(3, ); TQ(8); TQOUT;
#undef G_BP
#undef G_BL
#undef G_BH
#undef G_A
#undef G_M
#undef G_REST
#undef G_TILE
#undef H_REST
#undef H_TILE
#undef H_BAR
            asm volatile("s_nop 7"); SCHED;
            TS(12);
            BAR; CBAR;
            TS(13);
            u32x4 pv[2][2];
#pragma unroll
            for (int m = 0; m < 2; ++m)
#pragma unroll
                for (int s = 0; s < 2; ++s) pv[m][s] = *(const LAS u32x4*)(lds + 131072 + (m * 2 + s) * 4096 + tid * 16);
#pragma unroll
            for (int m = 0; m < 2; ++m)
#pragma unroll
                for (int s = 0; s < 2; ++s) { float lo[4], hi4[4]; const u32x4 p_ = pv[m][s], x_ = xv[m][s];
                    const f32x4 a0 = acc[m][2 * s] + (f32x4){bf_lo(p_[0]), bf_hi(p_[0]), bf_lo(p_[1]), bf_hi(p_[1])}, a1 = acc[m][2 * s + 1] + (f32x4){bf_lo(p_[2]), bf_hi(p_[2]), bf_lo(p_[3]), bf_hi(p_[3])};
#pragma unroll
                    for (int i = 0; i < 4; ++i) { auto r = __builtin_amdgcn_permlane16_swap(__float_as_uint(a0[i]), __float_as_uint(a1[i]), false, false);
                        lo[i] = __uint_as_float(r[0]); hi4[i] = __uint_as_float(r[1]); }
                    u32x4 o; o[0] = cvtpk(lo[0] + bf_lo(x_[0]), lo[1] + bf_hi(x_[0])); o[1] = cvtpk(lo[2] + bf_lo(x_[1]), lo[3] + bf_hi(x_[1]));
                    o[2] = cvtpk(hi4[0] + bf_lo(x_[2]), hi4[1] + bf_hi(x_[2])); o[3] = cvtpk(hi4[2] + bf_lo(x_[3]), hi4[3] + bf_hi(x_[3]));
                    __builtin_nontemporal_store(o, (u32x4*)(out + rowo + (size_t)m * 16 * DM + s * 64));
                }
        }
        TS(14); TR(31);
    }
}

extern "C" void kernel_launch(void* const* d_in, const int* in_sizes, int n_in, void* d_out, int out_size, void* d_ws, size_t ws_size, hipStream_t stream) {
    (void)hipFuncSetAttribute((const void*)k_block, hipFuncAttributeMaxDynamicSharedMemorySize, LDS_BYTES);
    k_block<<<256, 512, LDS_BYTES, stream>>>((const u16*)d_in[0], (const u16*)d_in[1], (const u16*)d_in[2], (const u16*)d_in[4], (const u16*)d_in[5], (const u16*)d_in[6], (char*)d_ws, (u16*)d_out);
}
```

```cpp
#include <hip/hip_runtime.h>
#include <hip/hip_bf16.h>
#include <stdint.h>

#define LAS __attribute__((address_space(3)))
#define GAS __attribute__((address_space(1)))
#define DI __device__ __forceinline__
typedef unsigned short u16;
typedef unsigned long long u64;
typedef short bf16x8 __attribute__((ext_vector_type(8)));
typedef short s16x4 __attribute__((ext_vector_type(4)));
typedef float f32x2 __attribute__((ext_vector_type(2)));
typedef float f32x4 __attribute__((ext_vector_type(4)));
typedef float f32x16 __attribute__((ext_vector_type(16)));
typedef unsigned u32x2 __attribute__((ext_vector_type(2)));
typedef unsigned u32x4 __attribute__((ext_vector_type(4)));
typedef __bf16 bf16x2_t __attribute__((ext_vector_type(2)));

constexpr int SEQ = 2048, DM = 1024, FF = 3584;
constexpr int KB_X = DM / 32;
constexpr int KB_AH = (DM + FF) / 32;
constexpr unsigned WS_XN = 0;
constexpr unsigned WS_Q = WS_XN + SEQ * DM * 2;
constexpr unsigned WS_K = WS_Q + SEQ * DM * 2;
constexpr unsigned WS_V = WS_K + SEQ * DM * 2;
constexpr unsigned WS_AH = WS_V + SEQ * DM * 2;
constexpr unsigned WS_PART = WS_AH + SEQ * (DM + FF) * 2;
constexpr unsigned WS_FLAG = WS_PART + 128 * 32768;
constexpr unsigned WS_END = WS_FLAG + 16384;
constexpr int F_LN = 32, F_QKV = F_LN + 256, F_H = F_QKV + 256, F_AT = F_H + 256, F_G2 = F_AT + 256;
constexpr int LDS_BYTES = 163840;
constexpr unsigned CODE_BYTES = 32768;

DI unsigned cvtpk(float lo, float hi) { f32x2 v = {lo, hi}; bf16x2_t b = __builtin_convertvector(v, bf16x2_t); return __builtin_bit_cast(unsigned, b); }
DI float bf_lo(unsigned u) { return __uint_as_float(u << 16); }
DI float bf_hi(unsigned u) { return __uint_as_float(u & 0xffff0000u); }
DI float rbf(float f) { return __uint_as_float(cvtpk(f, f) << 16); }
DI s16x4 trd(const LAS char* p) { return __builtin_amdgcn_ds_read_tr16_b64_v4i16((LAS s16x4*)p); }
DI bf16x8 cat8(s16x4 a, s16x4 b) { return __builtin_shufflevector(a, b, 0, 1, 2, 3, 4, 5, 6, 7); }
template <int OFF, int D> DI void glds(const char* base, unsigned voff, unsigned dst) {
    asm volatile("s_add_u32 m0, %2, %4\n\ts_nop 0\n\tglobal_load_lds_dwordx4 %0, %1 offset:%3" :: "v"(voff), "s"(base), "s"(dst), "i"(OFF), "i"(D - OFF) : "memory", "scc"); }
template <int D> DI void glds_pair_sc1(const char* base, unsigned voff, unsigned dst) {
    asm volatile("s_add_u32 m0, %2, %3\n\ts_nop 0\n\tglobal_load_lds_dwordx4 %0, %1 sc1\n\tglobal_load_lds_dwordx4 %0, %1 offset:1024 sc1" :: "v"(voff), "s"(base), "s"(dst), "i"(D) : "memory", "scc"); }
template <int D> DI void touch(const char* base, unsigned voff, unsigned dst) {
    asm volatile("s_add_u32 m0, %2, %3\n\ts_nop 0\n\tglobal_load_lds_dword %0, %1" :: "v"(voff), "s"(base), "s"(dst), "i"(D) : "memory", "scc"); }
#define WAIT_V(n) asm volatile("s_waitcnt vmcnt(" #n ")" ::: "memory")
#define WAIT_L(n) asm volatile("s_waitcnt lgkmcnt(" #n ")" ::: "memory")
#define BAR __builtin_amdgcn_s_barrier()
#define CBAR asm volatile("" ::: "memory")
#define SCHED __builtin_amdgcn_sched_barrier(0)
#define PIN(x) asm volatile("" : "+v"(x))
#define MFMA16(a, b, c) __builtin_amdgcn_mfma_f32_16x16x32_bf16(a, b, c, 0, 0, 0)
#define MFMA16_ACC(c, a, b) asm volatile("v_mfma_f32_16x16x32_bf16 %0, %1, %2, %0" : "+v"(c) : "v"(a), "v"(b))
#define MFMA32(a, b, c) __builtin_amdgcn_mfma_f32_32x32x16_bf16(a, b, c, 0, 0, 0)
#define ST16(v, off) __builtin_amdgcn_raw_buffer_store_b128(v, rs, (int)(off), 0, 16)
#define ST8(v, off) __builtin_amdgcn_raw_buffer_store_b64(v, rs, (int)(off), 0, 16)
#define LD16(off) __builtin_amdgcn_raw_buffer_load_b128(rs, (int)(off), 0, 16)
DI u64 flag_ld(const GAS u64* p) { return __hip_atomic_load(p, __ATOMIC_RELAXED, __HIP_MEMORY_SCOPE_AGENT); }
DI void flag_st(GAS u64* p, u64 v) { __hip_atomic_store(p, v, __ATOMIC_RELAXED, __HIP_MEMORY_SCOPE_AGENT); }
DI void flag_wait(const GAS u64* p, u64 tag) {
    for (unsigned spins = 0; spins < (1u << 18); ++spins) { if (__all(flag_ld(p) == tag)) break; __builtin_amdgcn_s_sleep(1); }
    CBAR;
}
template <int N> DI float ror_add(float v) { return v + __builtin_bit_cast(float, __builtin_amdgcn_update_dpp(0, __builtin_bit_cast(int, v), 0x120 + N  , 0xf, 0xf, false)); }
DI float sum64(float v) {
    v = ror_add<1>(ror_add<2>(ror_add<4>(ror_add<8>(v))));
    { auto r = __builtin_amdgcn_permlane16_swap(__float_as_uint(v), __float_as_uint(v), false, false); v = __uint_as_float(r[0]) + __uint_as_float(r[1]); }
    { auto r = __builtin_amdgcn_permlane32_swap(__float_as_uint(v), __float_as_uint(v), false, false); v = __uint_as_float(r[0]) + __uint_as_float(r[1]); }
    return v;
}
DI int opaque_tid() { int t = threadIdx.x; asm volatile("" : "+v"(t)); return t; }

DI float silu_mul(float g, float u) { return g * u * __builtin_amdgcn_rcpf(1.0f + __builtin_amdgcn_exp2f(g * -1.4426950408889634f)); }
DI u32x4 pack8(const f32x4& a, const f32x4& b) { u32x4 o; o[0] = cvtpk(a[0], a[1]); o[1] = cvtpk(a[2], a[3]); o[2] = cvtpk(b[0], b[1]); o[3] = cvtpk(b[2], b[3]); return o; }
constexpr float QSCALE = 0.17677669529663687f * 1.4426950408889634f;


struct G128 { const char* a; const char* w2; const char* wo; int kh; };
template <bool HALF> DI void g128_issue(unsigned lds0, const G128& g, int t, int lw, unsigned voffA, unsigned voffB) {
    const unsigned so = lds0 + (t & 3) * 32768, sb = so + lw * 2048;
    const bool att = !HALF && g.kh == 0 && t >= 16;
    const int kt = HALF ? t - 16 : att ? t - 16 : g.kh * 24 + t;
    const char* a = g.a + (size_t)(att ? kt : 16 + kt) * 2048; const char* b = (att ? g.wo : g.w2) + (size_t)kt * (64 * DM * 2);
    if (HALF) glds_pair_sc1<0>(a, voffA, so + (lw >> 1) * 8192 + (lw & 1) * 2048);
    else { glds_pair_sc1<0>(a, voffA, so + lw * 4096); glds_pair_sc1<2048>(a + KB_AH * 1024, voffA, so + lw * 4096); }
    glds<0, 16384>(b, voffB, sb); glds<128, 24576>(b, voffB, sb);
    glds<0, 16384 + 1024>(b + 8 * DM * 2, voffB, sb); glds<128, 24576 + 1024>(b + 8 * DM * 2, voffB, sb);
}

__global__ __launch_bounds__(512, 2) void k_block(const u16* __restrict__ x, const u16* __restrict__ wqkv, const u16* __restrict__ wo, const u16* __restrict__ w1,
                                                  const u16* __restrict__ w3, const u16* __restrict__ w2, char* __restrict__ ws, u16* __restrict__ out) {
    extern __shared__ __attribute__((aligned(16))) char smem[];
    LAS char* lds = (LAS char*)smem;
    const unsigned lds0 = (unsigned)(size_t)lds;
    const int wid = __builtin_amdgcn_readfirstlane(threadIdx.x >> 6);
    const int bid = blockIdx.x, xcd = bid & 7, idx = bid >> 3;
    GAS u64* flags = (GAS u64*)(ws + WS_FLAG);
    const __amdgpu_buffer_rsrc_t rs = __builtin_amdgcn_make_buffer_rsrc((void*)ws, 0, WS_END, 0x00020000);
    {
        const char* pc; asm volatile("s_getpc_b64 %0" : "=s"(pc));
        const int l = threadIdx.x & 63, piece = (idx * 8 + wid) & 7;
        const unsigned o = wid == 0 ? l * (CODE_BYTES / 64) : piece * 4096 + l * 64;
        touch<148480>(pc, o < CODE_BYTES ? o : 0, lds0 + wid * 1024);
    }
    u64 tag;
    u32x4 xa, xb; u32x2 ep;
    { const char* xp = (const char*)x + (size_t)(bid * 8 + wid) * (DM * 2);
      asm volatile("global_load_dwordx4 %0, %3, %4\n\tglobal_load_dwordx4 %1, %3, %4 offset:1024\n\tglobal_load_dwordx2 %2, %5, %6 sc1"
                   : "=&v"(xa), "=&v"(xb), "=&v"(ep) : "v"((threadIdx.x & 63) * 16), "s"(xp), "v"(0), "s"(ws + WS_FLAG), "s"(wqkv), "s"(w1), "s"(w3) : "memory"); }
    const int cj = idx >> 2, ct = cj < 3 ? (xcd >> 1) * 3 + cj : 12 + (xcd >> 1) * 5 + (cj - 3), rt = (xcd & 1) * 4 + (idx & 3);
    const bool qkv = ct < 12;
    u32x4 hmain[8]; u32x2 hext[4];

    {
        const int tid = opaque_tid(), lane = tid & 63, wr = wid >> 2, wc = wid & 3;
        const unsigned ldb2 = qkv ? 3 * DM * 2 : FF * 2;
        const char* cB0 = qkv ? (const char*)wqkv + ct * 512 : (const char*)w1 + (ct - 12) * 256;
        const char* cB1 = qkv ? cB0 + 256 : (const char*)w3 + (ct - 12) * 256;
        const char* cA = ws + WS_XN + rt * 16 * KB_X * 1024;
        const unsigned voffA = wid * (KB_X * 1024) + lane * 16;
        const unsigned voffB = (unsigned)(wid * 8 + (lane >> 3)) * ldb2 + (((lane & 7) ^ (2 * ((lane >> 4) & 3))) * 16);
        const char* pE = ((lane & 1) ? (const char*)w3 : (const char*)w1) + (size_t)(wid * 8 + (lane >> 3)) * (FF * 2) + (2560 + ct * 32) * 2 + ((((lane >> 1) & 3) ^ ((lane >> 4) & 3)) * 16);
        const size_t kstepB = (size_t)64 * ldb2;
        const unsigned ldsA = lds0 + wid * 2048, ldsB = lds0 + wid * 1024;
        const char* cT = ((idx & 2) ? cB1 : cB0) + (idx & 1) * 128;
#define SA(b, h) (((b) * 2 + (h)) * 16384)
#define SB(b, h) (((b) * 2 + (h)) * 16384)
#define STA(b, h, kt) glds_pair_sc1<SA(b, h)>(cA + (h) * 8 * KB_X * 1024 + (kt) * 2048, voffA, ldsA)
#define STB(b, h, kt) do { const char* s_ = ((h) ? cB1 : cB0) + (size_t)(kt) * kstepB; \
        glds<0, 65536 + SB(b, h)>(s_, voffB, ldsB); glds<128, 65536 + SB(b, h) + 8192>(s_, voffB, ldsB); } while (0)
#define TCH(kt) touch<148480>(cT + (size_t)(kt) * kstepB, voffB, ldsB)
#define STE(b, kt) do { const char* s_ = pE + (size_t)(kt) * (64 * FF * 2); \
        asm volatile("s_add_u32 m0, %1, %2\n\ts_nop 0\n\tglobal_load_lds_dwordx4 %0, off" :: "v"(s_), "s"(ldsB), "i"(131072 + (b) * 8192) : "memory", "scc"); } while (0)
        {
            STB(0, 0, 0); STB(0, 1, 0); STE(0, 0);
            asm volatile("s_waitcnt vmcnt(5)" : "+v"(xa), "+v"(xb), "+v"(ep) :: "memory");
            tag = (((u64)(unsigned)__builtin_amdgcn_readfirstlane(ep[1]) << 32) | (unsigned)__builtin_amdgcn_readfirstlane(ep[0])) + 1;
            float f[16];
#pragma unroll
            for (int i = 0; i < 4; ++i) { f[2 * i] = bf_lo(xa[i]); f[2 * i + 1] = bf_hi(xa[i]); f[8 + 2 * i] = bf_lo(xb[i]); f[9 + 2 * i] = bf_hi(xb[i]); }
            float s = 0.f;
#pragma unroll
            for (int i = 0; i < 16; ++i) s += f[i];
            const float mean = sum64(s) * (1.0f / DM);
            float q = 0.f;
#pragma unroll
            for (int i = 0; i < 16; ++i) { const float d = f[i] - mean; q += d * d; }
            const float var = sum64(q) * (1.0f / DM);
            const float mb = rbf(mean), sd = rbf(__builtin_amdgcn_sqrtf(rbf(rbf(var) + 1e-5f))), inv = __builtin_amdgcn_rcpf(sd);
#pragma unroll
            for (int c = 0; c < 2; ++c) { u32x4 o;
#pragma unroll
                for (int i = 0; i < 4; ++i) o[i] = cvtpk(rbf(f[c * 8 + 2 * i] - mb) * inv, rbf(f[c * 8 + 2 * i + 1] - mb) * inv);
                *(LAS u32x4*)(lds + wid * 2080 + c * 1024 + lane * 16) = o; }
            WAIT_L(0); BAR; CBAR;
            const int r = lane & 7, g = (lane >> 3) & 3, row = bid * 8 + r;
#pragma unroll
            for (int c = 0; c < 2; ++c) { const int kb = 4 * wid + 2 * c + (lane >> 5); const LAS char* p = lds + r * 2080 + kb * 64 + g * 8;
                const u32x2 lo = *(const LAS u32x2*)p, hi = *(const LAS u32x2*)(p + 32);
                ST16(((u32x4){lo[0], lo[1], hi[0], hi[1]}), WS_XN + ((row >> 4) * KB_X + kb) * 1024 + (g * 16 + (row & 15)) * 16); }
            WAIT_V(0); BAR;
            if (wid == 0) { if (lane == 0) flag_st(flags + F_LN + bid, tag); flag_wait(flags + F_LN + rt * 32 + (lane & 31), tag); }
            STB(1, 0, 1); STB(1, 1, 1); STE(1, 1);
            if (wid != 0) { TCH(2); TCH(3); }
            BAR; CBAR;
        }
        f32x4 acc[2][2][4][2], ace[2][4];
        {
            const int g = lane >> 4, q = (lane & 15) >> 2, p = lane & 3, swz = (2 * g + (q >> 1)) & 3;
            const int aoff = wr * 8192 + lane * 16;
            const int boff0 = 65536 + (wc >> 1) * 8192 + (4 * g + q) * 128 + (((2 * (wc & 1)) ^ swz) * 32) + p * 8, boff1 = boff0 ^ 32;
            int eoff = 131072 + (4 * g + q) * 128 + ((wc ^ swz) * 32) + p * 8; PIN(eoff);
#define LDA(dst, b, h) do { _Pragma("unroll") for (int m = 0; m < 4; ++m) _Pragma("unroll") for (int k = 0; k < 2; ++k) \
        dst[m][k] = *(const LAS bf16x8*)(lds + aoff + SA(b, h) + (m * 2 + k) * 1024); } while (0)
#define LDB(dst, b, h) do { _Pragma("unroll") for (int n = 0; n < 2; ++n) _Pragma("unroll") for (int k = 0; k < 2; ++k) { \
        const LAS char* b_ = lds + (n ? boff1 : boff0) + SB(b, h) + k * 4096; dst[n][k] = cat8(trd(b_), trd(b_ + 2048)); } } while (0)
#define LDE(dst, b) do { _Pragma("unroll") for (int k = 0; k < 2; ++k) { const LAS char* b_ = lds + eoff + (b) * 8192 + k * 4096; dst[k] = cat8(trd(b_), trd(b_ + 2048)); } } while (0)
#define MMA(ai) do { __builtin_amdgcn_s_setprio(1); \
        _Pragma("unroll") for (int k = 0; k < 2; ++k) _Pragma("unroll") for (int m = 0; m < 4; ++m) { \
            MFMA16_ACC(acc[ai][0][m][0], B0[0][k], At[m][k]); MFMA16_ACC(acc[ai][0][m][1], B0[1][k], At[m][k]); \
            MFMA16_ACC(acc[ai][1][m][0], B1[0][k], At[m][k]); MFMA16_ACC(acc[ai][1][m][1], B1[1][k], At[m][k]); \
            MFMA16_ACC(ace[ai][m], BE[k], At[m][k]); } \
        __builtin_amdgcn_s_setprio(0); } while (0)
            STA(0, 0, 0); STA(0, 1, 0); STA(1, 0, 1);
#pragma unroll
            for (int a = 0; a < 2; ++a)
#pragma unroll
                for (int m = 0; m < 4; ++m) { ace[a][m] = (f32x4){0.f, 0.f, 0.f, 0.f}; PIN(ace[a][m]);
#pragma unroll
                    for (int b = 0; b < 2; ++b)
#pragma unroll
                        for (int n = 0; n < 2; ++n) { acc[a][b][m][n] = (f32x4){0.f, 0.f, 0.f, 0.f}; PIN(acc[a][b][m][n]); } }
            bf16x8 At[4][2], B0[2][2], B1[2][2], BE[2];
            constexpr int nt = DM / 64;
            if (wr == 1) BAR;
            WAIT_V(4); BAR;
            WAIT_V(2); BAR;
            for (int t = 0; t < nt; t += 2) {
                const bool more = t + 2 < nt, far = t + 4 < nt;
                LDB(B0, 0, 0); LDB(B1, 0, 1); LDE(BE, 0); SCHED; LDA(At, 0, 0); STA(1, 1, t + 1);
                if (far) { TCH(t + 4); WAIT_V(11); } else { WAIT_V(9); }
                WAIT_L(0); BAR; MMA(0); BAR; SCHED;
                LDA(At, 0, 1);
                if (more) { STB(0, 0, t + 2); STB(0, 1, t + 2); STE(0, t + 2); STA(0, 0, t + 2); if (far) { WAIT_V(10); } else { WAIT_V(9); } } else { WAIT_V(2); }
                WAIT_L(0); BAR; MMA(1); BAR; SCHED;
                LDB(B0, 1, 0); LDB(B1, 1, 1); LDE(BE, 1); SCHED; LDA(At, 1, 0);
                if (more) { STA(0, 1, t + 2); if (far) { TCH(t + 5); WAIT_V(11); } else { WAIT_V(9); } } else { WAIT_V(0); }
                WAIT_L(0); BAR; MMA(0); BAR; SCHED;
                LDA(At, 1, 1);
                if (more) { STB(1, 0, t + 3); STB(1, 1, t + 3); STE(1, t + 3); STA(1, 0, t + 3); if (far) { WAIT_V(10); } else { WAIT_V(9); } }
                WAIT_L(0); BAR; MMA(1); if (more || wr == 0) BAR; SCHED;
            }
            asm volatile("s_nop 7"); SCHED;
#undef SA
#undef SB
#undef STA
#undef STB
#undef STE
#undef TCH
#undef LDA
#undef LDB
#undef LDE
#undef MMA
        }
        const int ln = opaque_tid() & 63, fr = ln & 15, fq = ln >> 4;
        if (qkv) {
            const int kind = ct >> 2;
            const float sc = kind == 0 ? QSCALE : 1.0f;
            const unsigned base = WS_Q + kind * (SEQ * DM * 2) + ((ct & 3) * 8 + wc) * 131072 + (rt * 256 + wr * 64) * 64;
            const unsigned lo0 = base + (kind == 2 ? fr * 64 + fq * 16 : fr * 16 + fq * 512), lo1 = lo0 + (kind == 2 ? 1024 : 256);
#pragma unroll
            for (int bj = 0; bj < 2; ++bj)
#pragma unroll
                for (int ai = 0; ai < 2; ++ai)
#pragma unroll
                    for (int m = 0; m < 4; ++m)
                        ST16(pack8(acc[ai][bj][m][0] * sc, acc[ai][bj][m][1] * sc), bj * 4 * 131072 + ai * 8192 + (m >> 1) * 2048 + ((m & 1) ? lo1 : lo0));
            WAIT_V(0); BAR;
            if (wid == 0 && ln == 0) flag_st(flags + F_QKV + rt * 32 + ct, tag);
        } else {
#pragma unroll
            for (int ai = 0; ai < 2; ++ai)
#pragma unroll
                for (int m = 0; m < 4; ++m) { f32x4 h0, h1;
#pragma unroll
                    for (int j = 0; j < 4; ++j) { h0[j] = silu_mul(acc[ai][0][m][0][j], acc[ai][1][m][0][j]); h1[j] = silu_mul(acc[ai][0][m][1][j], acc[ai][1][m][1][j]); }
                    hmain[ai * 4 + m] = pack8(h0, h1);
                }
        }
#pragma unroll
        for (int ai = 0; ai < 2; ++ai)
#pragma unroll
            for (int mp = 0; mp < 2; ++mp) { float h[4];
#pragma unroll
                for (int j = 0; j < 4; ++j) { auto r = __builtin_amdgcn_permlane32_swap(__float_as_uint(ace[ai][2 * mp][j]), __float_as_uint(ace[ai][2 * mp + 1][j]), false, false);
                    h[j] = silu_mul(__uint_as_float(r[0]), __uint_as_float(r[1])); }
                hext[ai * 2 + mp] = (u32x2){cvtpk(h[0], h[1]), cvtpk(h[2], h[3])};
            }
    }

    const int kh = xcd & 1, rt2 = ((xcd >> 1) & 1) * 8 + (idx & 7), ct2 = (xcd >> 2) * 4 + (idx >> 3);
    G128 g2; g2.kh = kh; g2.a = ws + WS_AH + rt2 * 8 * KB_AH * 1024; g2.w2 = (const char*)w2 + ct2 * 256; g2.wo = (const char*)wo + ct2 * 256;
#define G2_OFFS(lw_, l_) const unsigned vA = ((lw_) ^ kh) * 2 * KB_AH * 1024 + (l_) * 16, vB = ((lw_) * 16 + ((l_) >> 3)) * (DM * 2) + ((((l_) & 7) ^ (2 * (((l_) >> 4) & 3))) * 16)

    {
        constexpr int AT_BASE = 65536, AT_STAGE = 32768;
        const int lane = opaque_tid() & 63, hi = lane >> 5;
        const int head = xcd * 4 + (idx >> 3), qb = idx & 7;
        if (wid == 0) flag_wait(flags + F_QKV + (lane < 4 ? (lane & 1) * 32 + 4 + (lane >> 1) * 4 : qb * 32) + (head >> 3), tag);
        BAR; CBAR;
        const char* Kg = ws + WS_K + head * 131072; const char* Vg = ws + WS_V + head * 131072;
        const unsigned soff = wid * 2048 + lane * 16;
        const unsigned sdst = lds0 + AT_BASE + wid * 2048;
#define AT_ISSUE(s, slot) do { glds_pair_sc1<0>(Kg + (s) * 16384, soff, sdst + (slot)); glds_pair_sc1<16384>(Vg + (s) * 16384, soff, sdst + (slot)); } while (0)
        const unsigned qoff = WS_Q + (head * 64 + qb * 8 + wid) * 2048 + lane * 16;
        const bf16x8 q0 = __builtin_bit_cast(bf16x8, LD16(qoff)), q1 = __builtin_bit_cast(bf16x8, LD16(qoff + 1024));
        AT_ISSUE(0, 0); AT_ISSUE(1, AT_STAGE);
        const GAS u64* kvflag = flags + F_QKV + (lane & 7) * 32 + 4 + ((lane >> 3) & 1) * 4 + (head >> 3);
        u64 seen = flag_ld(kvflag);
        bf16x8 sel; { const short one = ((lane & 15) == ((lane >> 4) & 1)) ? (short)0x3f80 : (short)0;
#pragma unroll
            for (int i = 0; i < 8; ++i) sel[i] = one; }
        const LAS char* kb0 = lds + AT_BASE + lane * 16;
        const LAS char* vb0 = lds + AT_BASE + 16384 + (4 * hi + ((lane & 15) >> 2)) * 64 + ((lane >> 4) & 1) * 32 + (lane & 3) * 8;
        f32x16 O, negm, SA, SB; f32x4 L = {0.f, 0.f, 0.f, 0.f};
        bf16x8 k0, k1, v0, v1;
#pragma unroll
        for (int i = 0; i < 16; ++i) { O[i] = 0.f; negm[i] = 0.f; }
        WAIT_V(5); BAR;
        {
            k0 = *(const LAS bf16x8*)(kb0); k1 = *(const LAS bf16x8*)(kb0 + 1024);
            SA = MFMA32(k0, q0, negm); SA = MFMA32(k1, q1, SA);
            k0 = *(const LAS bf16x8*)(kb0 + 2048); k1 = *(const LAS bf16x8*)(kb0 + 3072);
            v0 = cat8(trd(vb0), trd(vb0 + 512)); v1 = cat8(trd(vb0 + 1024), trd(vb0 + 1536));
            float m = SA[0];
#pragma unroll
            for (int i = 1; i < 16; ++i) m = fmaxf(m, SA[i]);
            m = fmaxf(m, __shfl_xor(m, 32));
#pragma unroll
            for (int i = 0; i < 16; ++i) { negm[i] = -m; SA[i] -= m; }
            PIN(negm);
        }
#define EX(S, i) S[i] = __builtin_amdgcn_exp2f(S[i])
#define BC8(x) __builtin_bit_cast(bf16x8, x)
#define AT_STEP(SC, SN, kp, v1p, v0p) do { \
        O = MFMA32(v1, BC8(p1), O); SCHED; v1 = cat8(trd(v1p), trd((v1p) + 512)); EX(SC, 0); EX(SC, 1); EX(SC, 2); PIN(SC); SCHED; \
        SN = MFMA32(k0, q0, negm); SCHED; k0 = *(const LAS bf16x8*)(kp); EX(SC, 3); EX(SC, 4); EX(SC, 5); PIN(SC); SCHED; \
        L = MFMA16(sel, BC8(p1), L); SCHED; EX(SC, 6); EX(SC, 7); \
        p0[0] = cvtpk(SC[0], SC[1]); p0[1] = cvtpk(SC[2], SC[3]); p0[2] = cvtpk(SC[4], SC[5]); p0[3] = cvtpk(SC[6], SC[7]); PIN(p0); SCHED; \
        O = MFMA32(v0, BC8(p0), O); SCHED; v0 = cat8(trd(v0p), trd((v0p) + 512)); EX(SC, 8); EX(SC, 9); EX(SC, 10); PIN(SC); SCHED; \
        SN = MFMA32(k1, q1, SN); SCHED; k1 = *(const LAS bf16x8*)((kp) + 1024); EX(SC, 11); EX(SC, 12); EX(SC, 13); PIN(SC); SCHED; \
        L = MFMA16(sel, BC8(p0), L); SCHED; EX(SC, 14); EX(SC, 15); \
        p1[0] = cvtpk(SC[8], SC[9]); p1[1] = cvtpk(SC[10], SC[11]); p1[2] = cvtpk(SC[12], SC[13]); p1[3] = cvtpk(SC[14], SC[15]); PIN(p1); SCHED; } while (0)
        u32x4 p0, p1 = {0u, 0u, 0u, 0u};
        int cur = 0, nxt = AT_STAGE, fre = 2 * AT_STAGE;
        const GAS u64* hflag = flags + F_H + (rt2 >> 1) * 32 + (lane & 31);
        const unsigned hb = WS_AH + ((rt * 16 + (wid >> 2) * 4) * KB_AH + KB_X + (ct - 12) * 4 + (wid & 3)) * 1024 + lane * 16;
        const int cq = 2 * (wid & 3) + ((lane >> 4) & 1);
        const unsigned eb = WS_AH + ((rt * 16 + (wid >> 2) * 4 + hi) * KB_AH + KB_X + 80 + ct) * 1024 + ((cq & 3) * 16 + (lane & 15)) * 16 + (cq >> 2) * 8;
#define H_MAIN(j) do { if (!qkv && s < 4) { if (s == 0) ST16(hmain[j], hb + (j) * KB_AH * 1024); else if (s == 1) ST16(hmain[2 + (j)], hb + (2 + (j)) * KB_AH * 1024); \
            else if (s == 2) ST16(hmain[4 + (j)], hb + (8 + (j)) * KB_AH * 1024); else ST16(hmain[6 + (j)], hb + (10 + (j)) * KB_AH * 1024); } SCHED; } while (0)
#define H_EXT() do { if (s < 4) { if (s == 0) ST8(hext[0], eb); else if (s == 1) ST8(hext[1], eb + 2 * KB_AH * 1024); \
            else if (s == 2) ST8(hext[2], eb + 8 * KB_AH * 1024); else ST8(hext[3], eb + 10 * KB_AH * 1024); } SCHED; } while (0)
        for (int s = 0; s < 8; ++s) {
            if (s >= 1 && s <= 4) { if (qkv) { WAIT_V(2); } else { WAIT_V(4); } } else { WAIT_V(0); }
            WAIT_L(0); BAR;
            if (s == 0) { if (!__all(seen == tag)) flag_wait(kvflag, tag); }
            if (s + 2 < 8) AT_ISSUE(s + 2, fre);
            if (s < 4) {
                const int r0 = (idx * 8 + wid) * 10 + (s < 3 ? s * 4 : 0), rl = s == 2 ? ((lane >> 4) & 1) : (lane >> 4);
                const char* src = (kh ? (const char*)w2 + 1024 * (DM * 2) : r0 >= 1536 ? (const char*)wo - 1536 * (DM * 2) : (const char*)w2) + (size_t)r0 * (DM * 2) + (xcd >> 2) * 1024;
                touch<-AT_BASE>(src, rl * (DM * 2) + (lane & 15) * 64, sdst);
            }
            if (s == 5) { if (wid == 0 && lane == 0) flag_st(flags + F_H + rt * 32 + ct, tag); }
            if (s == 6) seen = flag_ld(hflag);
            if (s == 7 && wid >= 4) {
                if (!__all(seen == tag)) flag_wait(hflag, tag);
                G2_OFFS(wid - 4, lane);
                g128_issue<false>(lds0, g2, 0, wid - 4, vA, vB); g128_issue<false>(lds0, g2, 1, wid - 4, vA, vB);
            }
            if (wid >= 4) __builtin_amdgcn_s_setprio(1);
            const LAS char* kc = kb0 + cur; const LAS char* vc = vb0 + cur; const LAS char* kn = kb0 + nxt; const LAS char* vn = vb0 + nxt;
            AT_STEP(SA, SB, kc + 2 * 2048, vc + 1024, vc + 1 * 2048);            AT_STEP(SB, SA, kc + 3 * 2048, vc + 1 * 2048 + 1024, vc + 2 * 2048);
            H_MAIN(0);
            AT_STEP(SA, SB, kc + 4 * 2048, vc + 2 * 2048 + 1024, vc + 3 * 2048); AT_STEP(SB, SA, kc + 5 * 2048, vc + 3 * 2048 + 1024, vc + 4 * 2048);
            H_EXT();
            AT_STEP(SA, SB, kc + 6 * 2048, vc + 4 * 2048 + 1024, vc + 5 * 2048); __builtin_amdgcn_s_setprio(0); AT_STEP(SB, SA, kc + 7 * 2048, vc + 5 * 2048 + 1024, vc + 6 * 2048);
            H_MAIN(1);
            AT_STEP(SA, SB, kn, vc + 6 * 2048 + 1024, vc + 7 * 2048);            AT_STEP(SB, SA, kn + 2048, vc + 7 * 2048 + 1024, vn);
            const int tmp = cur; cur = nxt; nxt = fre; fre = tmp;
        }
        O = MFMA32(v1, BC8(p1), O); L = MFMA16(sel, BC8(p1), L);
        float lrow; { const float a = __shfl(L[0], lane & 15), b = __shfl(L[1], lane & 15); lrow = (lane & 16) ? b : a; }
        float oabs = 0.f;
#pragma unroll
        for (int i = 0; i < 16; ++i) oabs += fabsf(O[i]);
        if (__any(!(lrow < 1.0e30f) || !(oabs < 3.0e38f))) {
            __builtin_amdgcn_fence(__ATOMIC_ACQUIRE, "agent");
            float m = -INFINITY, l = 0.f;
#pragma unroll
            for (int i = 0; i < 16; ++i) { O[i] = 0.f; negm[i] = 0.f; }
            const u16* Vs = (const u16*)Vg;
            for (int t = 0; t < 64; ++t) { const int key0 = t * 32;
                k0 = *(const bf16x8*)(Kg + key0 * 64 + lane * 16); k1 = *(const bf16x8*)(Kg + key0 * 64 + 1024 + lane * 16);
#pragma unroll
                for (int j = 0; j < 8; ++j) { const int kk = key0 + 8 * (j >> 2) + 4 * hi + (j & 3); v0[j] = (short)Vs[kk * 32 + (lane & 31)]; v1[j] = (short)Vs[(kk + 16) * 32 + (lane & 31)]; }
                f32x16 S = MFMA32(k0, q0, negm); S = MFMA32(k1, q1, S);
                float mt = S[0];
#pragma unroll
                for (int i = 1; i < 16; ++i) mt = fmaxf(mt, S[i]);
                mt = fmaxf(mt, __shfl_xor(mt, 32));
                const float mn = fmaxf(m, mt), al = __builtin_amdgcn_exp2f(m - mn); m = mn;
                float ps = 0.f;
#pragma unroll
                for (int i = 0; i < 16; ++i) { S[i] = rbf(__builtin_amdgcn_exp2f(S[i] - mn)); ps += S[i]; O[i] *= al; }
                l = l * al + ps + __shfl_xor(ps, 32);
#pragma unroll
                for (int i = 0; i < 4; ++i) { p0[i] = cvtpk(S[2 * i], S[2 * i + 1]); p1[i] = cvtpk(S[8 + 2 * i], S[9 + 2 * i]); }
                O = MFMA32(v0, BC8(p0), O); O = MFMA32(v1, BC8(p1), O);
            }
            lrow = l;
        }
        {
            const float inv = 1.0f / lrow;
            unsigned pk[8];
#pragma unroll
            for (int i = 0; i < 8; ++i) pk[i] = cvtpk(O[2 * i] * inv, O[2 * i + 1] * inv);
            const unsigned dst = WS_AH + ((qb * 16 + wid * 2 + ((lane >> 4) & 1)) * KB_AH + head) * 1024 + (lane & 15) * 16 + hi * 256;
#pragma unroll
            for (int c = 0; c < 2; ++c) {
                auto r0 = __builtin_amdgcn_permlane32_swap(pk[4 * c + 0], pk[4 * c + 2], false, false);
                auto r1 = __builtin_amdgcn_permlane32_swap(pk[4 * c + 1], pk[4 * c + 3], false, false);
                ST16(((u32x4){r0[0], r1[0], r0[1], r1[1]}), dst + c * 512);
            }
        }
        if (wid >= 4) { WAIT_V(2); }
        WAIT_L(0); BAR;
#undef AT_ISSUE
#undef AT_STEP
#undef H_MAIN
#undef H_EXT
#undef EX
#undef BC8
    }

    {
        const int tid = opaque_tid(), lane = tid & 63, tile = rt2 * 8 + ct2;
        GAS u64* myflag = flags + F_G2 + tile * 2 + kh; const GAS u64* pflag = flags + F_G2 + tile * 2 + (kh ^ 1);
        if (wid >= 4) {
            const int lw = wid - 4;
            G2_OFFS(lw, lane);
            const unsigned vH = ((lw >> 1) * 4 + 2 * kh + (lw & 1)) * KB_AH * 1024 + lane * 16;
            g128_issue<false>(lds0, g2, 2, lw, vA, vB); g128_issue<false>(lds0, g2, 3, lw, vA, vB);
            WAIT_V(16);
            for (int t = 0; t < 28; ++t) {
                BAR;
                if (t == 0 && wid == 4 && lane == 0) flag_st(flags + F_AT + (idx & 7) * 32 + xcd * 4 + (idx >> 3), tag);
                if (t == 12 && kh == 0) flag_wait(flags + F_AT + (rt2 >> 1) * 32 + (lane & 31), tag);
                g128_issue<false>(lds0, g2, t + 4, lw, vA, vB);
                WAIT_V(16);
            }
            BAR; g128_issue<true>(lds0, g2, 32, lw, vH, vB); WAIT_V(14);
            for (int t = 29; t < 35; ++t) {
                BAR;
                if (t == 34 && wid == 4 && lane == 0) flag_st(myflag, tag);
                g128_issue<true>(lds0, g2, t + 4, lw, vH, vB); WAIT_V(12);
            }
            BAR; g128_issue<true>(lds0, g2, 39, lw, vH, vB);
            const u64 seen = flag_ld(pflag);
            WAIT_V(13);
            BAR; WAIT_V(7);
            BAR; WAIT_V(0);
            const char* psrc = ws + WS_PART + tile * 32768 + (kh ^ 1) * 16384 + lw * 4096;
            const bool early = __all(seen == tag);
            if (early) { glds_pair_sc1<131072>(psrc, lane * 16, lds0 + lw * 4096); glds_pair_sc1<131072 + 2048>(psrc + 2048, lane * 16, lds0 + lw * 4096); }
            BAR;
            if (!early) { flag_wait(pflag, tag); glds_pair_sc1<131072>(psrc, lane * 16, lds0 + lw * 4096); glds_pair_sc1<131072 + 2048>(psrc + 2048, lane * 16, lds0 + lw * 4096); }
            WAIT_V(0); BAR;
            if (bid == 0 && wid == 4) {
#pragma unroll
                for (int i = 0; i < 4; ++i) flag_wait(flags + F_LN + i * 64 + lane, tag);
                if (lane == 0) flag_st(flags, tag);
            }
        } else {
            const int wm = wid >> 1, wn = wid & 1, g = lane >> 4, q = (lane & 15) >> 2, p = lane & 3, swz = (2 * g + (q >> 1)) & 3;
            const int aoff = wm * 8192 + lane * 16;
            const int boff0 = 16384 + (4 * g + q) * 128 + (((2 * wn) ^ swz) * 32) + p * 8, boff1 = boff0 ^ 32;
            f32x4 acc[4][4]; bf16x8 AX[4], AY[4];
            s16x4 XL[4], XH[4], YL[4], YH[4];
            int pa[2] = {aoff, aoff + 65536}, pb0[2] = {boff0, boff0 + 65536}, pb1[2] = {boff1, boff1 + 65536};
            PIN(pa[1]); PIN(pb0[1]); PIN(pb1[1]);
#define G_BP(n, sl, kk) (lds + (((n) & 1) ? pb1[(sl) >> 1] : pb0[(sl) >> 1]) + ((sl) & 1) * 32768 + ((n) >> 1) * 8192 + (kk) * 4096)
#define G_BL(L_, n, sl, kk) do { L_[n] = trd(G_BP(n, sl, kk)); SCHED; } while (0)
#define G_BH(H_, n, sl, kk) do { H_[n] = trd(G_BP(n, sl, kk) + 2048); SCHED; } while (0)
#define G_A(A_, m, sl, kk) do { A_[m] = *(const LAS bf16x8*)(lds + pa[(sl) >> 1] + ((sl) & 1) * 32768 + ((m) * 2 + (kk)) * 1024); SCHED; } while (0)
#define G_M(A_, L_, H_, m, n) do { MFMA16_ACC(acc[m][n], cat8(L_[n], H_[n]), A_[m]); SCHED; } while (0)
#define G_REST(AC, CL, CH, AN, NL, NH, sl, kk) do { \
        G_BL(NL, 0, sl, kk); G_M(AC, CL, CH, 0, 2); G_BH(NH, 0, sl, kk); G_M(AC, CL, CH, 0, 3); G_A(AN, 0, sl, kk); G_M(AC, CL, CH, 1, 0); G_BL(NL, 1, sl, kk); G_M(AC, CL, CH, 1, 1); \
        G_BH(NH, 1, sl, kk); G_M(AC, CL, CH, 1, 2); G_BL(NL, 2, sl, kk); G_M(AC, CL, CH, 1, 3); G_BH(NH, 2, sl, kk); G_M(AC, CL, CH, 2, 0); G_BL(NL, 3, sl, kk); G_M(AC, CL, CH, 2, 1); \
        G_BH(NH, 3, sl, kk); G_M(AC, CL, CH, 2, 2); G_A(AN, 1, sl, kk); G_M(AC, CL, CH, 2, 3); G_A(AN, 2, sl, kk); G_M(AC, CL, CH, 3, 0); G_A(AN, 3, sl, kk); G_M(AC, CL, CH, 3, 1); \
        G_M(AC, CL, CH, 3, 2); G_M(AC, CL, CH, 3, 3); } while (0)
#define G_TILE(sl) do { G_M(AX, XL, XH, 0, 0); G_M(AX, XL, XH, 0, 1); G_REST(AX, XL, XH, AY, YL, YH, sl, 1); \
        G_M(AY, YL, YH, 0, 0); G_M(AY, YL, YH, 0, 1); \
        asm volatile("s_waitcnt vmcnt(0) lgkmcnt(0)" ::: "memory"); BAR; SCHED;        \
        G_REST(AY, YL, YH, AX, XL, XH, ((sl) + 1) & 3, 0); } while (0)
#pragma unroll
            for (int m = 0; m < 4; ++m)
#pragma unroll
                for (int n = 0; n < 4; ++n) { acc[m][n] = (f32x4){0.f, 0.f, 0.f, 0.f}; PIN(acc[m][n]); }
            SCHED;
#pragma unroll
            for (int i = 0; i < 4; ++i) { G_BL(XL, i, 0, 0); G_BH(XH, i, 0, 0); G_A(AX, i, 0, 0); }
            for (int t = 0; t < 32; t += 4) { G_TILE(0); G_TILE(1); G_TILE(2); G_TILE(3); }
            asm volatile("s_nop 7"); SCHED;
            const int fr = lane & 15, fq = lane >> 4;
            const size_t rowo = (size_t)(rt2 * 128 + wm * 64 + kh * 32 + fr) * DM + ct2 * 128 + wn * 32 + (fq & 1) * 16 + (fq >> 1) * 8;
            u32x4 xv[2][2];
#pragma unroll
            for (int m = 0; m < 2; ++m)
#pragma unroll
                for (int s = 0; s < 2; ++s) ST16(pack8(acc[2 + m][2 * s], acc[2 + m][2 * s + 1]), WS_PART + tile * 32768 + kh * 16384 + (m * 2 + s) * 4096 + tid * 16);
#pragma unroll
            for (int m = 0; m < 2; ++m)
#pragma unroll
                for (int s = 0; s < 2; ++s) xv[m][s] = *(const u32x4*)(x + rowo + (size_t)m * 16 * DM + s * 64);
            SCHED;
#define H_REST(AC, CL, CH, AN, NL, NH, sl, kk) do { \
        G_BL(NL, 0, sl, kk); G_BH(NH, 0, sl, kk); G_M(AC, CL, CH, 0, 2); G_A(AN, 0, sl, kk); G_BL(NL, 1, sl, kk); G_M(AC, CL, CH, 0, 3); G_BH(NH, 1, sl, kk); G_BL(NL, 2, sl, kk); G_M(AC, CL, CH, 1, 0); \
        G_BH(NH, 2, sl, kk); G_BL(NL, 3, sl, kk); G_M(AC, CL, CH, 1, 1); G_BH(NH, 3, sl, kk); G_M(AC, CL, CH, 1, 2); G_A(AN, 1, sl, kk); G_M(AC, CL, CH, 1, 3); } while (0)
#define H_TILE(sl, SYNC) do { G_M(AX, XL, XH, 0, 0); G_M(AX, XL, XH, 0, 1); H_REST(AX, XL, XH, AY, YL, YH, sl, 1); \
        G_M(AY, YL, YH, 0, 0); G_M(AY, YL, YH, 0, 1); SYNC; SCHED; H_REST(AY, YL, YH, AX, XL, XH, ((sl) + 1) & 3, 0); } while (0)
#define H_BAR do { WAIT_L(0); BAR; } while (0)
            H_TILE(0, H_BAR); H_TILE(1, H_BAR); H_TILE(2, WAIT_V(0); H_BAR); H_TILE(3, H_BAR);
            H_TILE(0, H_BAR); H_TILE(1, H_BAR); H_TILE(2, H_BAR); H_TILE(3, );
#undef G_BP
#undef G_BL
#undef G_BH
#undef G_A
#undef G_M
#undef G_REST
#undef G_TILE
#undef H_REST
#undef H_TILE
#undef H_BAR
            asm volatile("s_nop 7"); SCHED;
            BAR; CBAR;
            u32x4 pv[2][2];
#pragma unroll
            for (int m = 0; m < 2; ++m)
#pragma unroll
                for (int s = 0; s < 2; ++s) pv[m][s] = *(const LAS u32x4*)(lds + 131072 + (m * 2 + s) * 4096 + tid * 16);
#pragma unroll
            for (int m = 0; m < 2; ++m)
#pragma unroll
                for (int s = 0; s < 2; ++s) { float lo[4], hi4[4]; const u32x4 p_ = pv[m][s], x_ = xv[m][s];
                    const f32x4 a0 = acc[m][2 * s] + (f32x4){bf_lo(p_[0]), bf_hi(p_[0]), bf_lo(p_[1]), bf_hi(p_[1])}, a1 = acc[m][2 * s + 1] + (f32x4){bf_lo(p_[2]), bf_hi(p_[2]), bf_lo(p_[3]), bf_hi(p_[3])};
#pragma unroll
                    for (int i = 0; i < 4; ++i) { auto r = __builtin_amdgcn_permlane16_swap(__float_as_uint(a0[i]), __float_as_uint(a1[i]), false, false);
                        lo[i] = __uint_as_float(r[0]); hi4[i] = __uint_as_float(r[1]); }
                    u32x4 o; o[0] = cvtpk(lo[0] + bf_lo(x_[0]), lo[1] + bf_hi(x_[0])); o[1] = cvtpk(lo[2] + bf_lo(x_[1]), lo[3] + bf_hi(x_[1]));
                    o[2] = cvtpk(hi4[0] + bf_lo(x_[2]), hi4[1] + bf_hi(x_[2])); o[3] = cvtpk(hi4[2] + bf_lo(x_[3]), hi4[3] + bf_hi(x_[3]));
                    __builtin_nontemporal_store(o, (u32x4*)(out + rowo + (size_t)m * 16 * DM + s * 64));
                }
        }
    }
}

extern "C" void kernel_launch(void* const* d_in, const int* in_sizes, int n_in, void* d_out, int out_size, void* d_ws, size_t ws_size, hipStream_t stream) {
    (void)hipFuncSetAttribute((const void*)k_block, hipFuncAttributeMaxDynamicSharedMemorySize, LDS_BYTES);
    k_block<<<256, 512, LDS_BYTES, stream>>>((const u16*)d_in[0], (const u16*)d_in[1], (const u16*)d_in[2], (const u16*)d_in[4], (const u16*)d_in[5], (const u16*)d_in[6], (char*)d_ws, (u16*)d_out);
}
```

```cpp
#include <hip/hip_runtime.h>
#include <hip/hip_bf16.h>
#include <stdint.h>

#define LAS __attribute__((address_space(3)))
#define GAS __attribute__((address_space(1)))
#define DI __device__ __forceinline__
typedef unsigned short u16;
typedef unsigned long long u64;
typedef short bf16x8 __attribute__((ext_vector_type(8)));
typedef short s16x4 __attribute__((ext_vector_type(4)));
typedef float f32x2 __attribute__((ext_vector_type(2)));
typedef float f32x4 __attribute__((ext_vector_type(4)));
typedef float f32x16 __attribute__((ext_vector_type(16)));
typedef unsigned u32x2 __attribute__((ext_vector_type(2)));
typedef unsigned u32x4 __attribute__((ext_vector_type(4)));
typedef __bf16 bf16x2_t __attribute__((ext_vector_type(2)));

constexpr int SEQ = 2048, DM = 1024, FF = 3584;
constexpr int KB_X = DM / 32;
constexpr int KB_AH = (DM + FF) / 32;
constexpr unsigned WS_XN = 0;
constexpr unsigned WS_Q = WS_XN + SEQ * DM * 2;
constexpr unsigned WS_K = WS_Q + SEQ * DM * 2;
constexpr unsigned WS_V = WS_K + SEQ * DM * 2;
constexpr unsigned WS_AH = WS_V + SEQ * DM * 2;
constexpr unsigned WS_PART = WS_AH + SEQ * (DM + FF) * 2;
constexpr unsigned WS_FLAG = WS_PART + 128 * 32768;
constexpr unsigned WS_END = WS_FLAG + 16384;
constexpr int F_LN = 32, F_QKV = F_LN + 256, F_H = F_QKV + 256, F_AT = F_H + 256, F_G2 = F_AT + 256;
constexpr int LDS_BYTES = 163840;
constexpr unsigned CODE_BYTES = 32768;

DI unsigned cvtpk(float lo, float hi) { f32x2 v = {lo, hi}; bf16x2_t b = __builtin_convertvector(v, bf16x2_t); return __builtin_bit_cast(unsigned, b); }
DI float bf_lo(unsigned u) { return __uint_as_float(u << 16); }
DI float bf_hi(unsigned u) { return __uint_as_float(u & 0xffff0000u); }
DI float rbf(float f) { return __uint_as_float(cvtpk(f, f) << 16); }
DI s16x4 trd(const LAS char* p) { return __builtin_amdgcn_ds_read_tr16_b64_v4i16((LAS s16x4*)p); }
DI bf16x8 cat8(s16x4 a, s16x4 b) { return __builtin_shufflevector(a, b, 0, 1, 2, 3, 4, 5, 6, 7); }
template <int OFF, int D> DI void glds(const char* base, unsigned voff, unsigned dst) {
    asm volatile("s_add_u32 m0, %2, %4\n\ts_nop 0\n\tglobal_load_lds_dwordx4 %0, %1 offset:%3" :: "v"(voff), "s"(base), "s"(dst), "i"(OFF), "i"(D - OFF) : "memory", "scc"); }
template <int D> DI void glds_pair_sc1(const char* base, unsigned voff, unsigned dst) {
    asm volatile("s_add_u32 m0, %2, %3\n\ts_nop 0\n\tglobal_load_lds_dwordx4 %0, %1 sc1\n\tglobal_load_lds_dwordx4 %0, %1 offset:1024 sc1" :: "v"(voff), "s"(base), "s"(dst), "i"(D) : "memory", "scc"); }
template <int D> DI void touch(const char* base, unsigned voff, unsigned dst) {
    asm volatile("s_add_u32 m0, %2, %3\n\ts_nop 0\n\tglobal_load_lds_dword %0, %1" :: "v"(voff), "s"(base), "s"(dst), "i"(D) : "memory", "scc"); }
#define WAIT_V(n) asm volatile("s_waitcnt vmcnt(" #n ")" ::: "memory")
#define WAIT_L(n) asm volatile("s_waitcnt lgkmcnt(" #n ")" ::: "memory")
#define BAR __builtin_amdgcn_s_barrier()
#define CBAR asm volatile("" ::: "memory")
#define SCHED __builtin_amdgcn_sched_barrier(0)
#define PIN(x) asm volatile("" : "+v"(x))
#define MFMA16(a, b, c) __builtin_amdgcn_mfma_f32_16x16x32_bf16(a, b, c, 0, 0, 0)
#define MFMA16_ACC(c, a, b) asm volatile("v_mfma_f32_16x16x32_bf16 %0, %1, %2, %0" : "+v"(c) : "v"(a), "v"(b))
#define MFMA32(a, b, c) __builtin_amdgcn_mfma_f32_32x32x16_bf16(a, b, c, 0, 0, 0)
#define ST16(v, off) __builtin_amdgcn_raw_buffer_store_b128(v, rs, (int)(off), 0, 16)
#define ST8(v, off) __builtin_amdgcn_raw_buffer_store_b64(v, rs, (int)(off), 0, 16)
#define LD16(off) __builtin_amdgcn_raw_buffer_load_b128(rs, (int)(off), 0, 16)
DI u64 flag_ld(const GAS u64* p) { return __hip_atomic_load(p, __ATOMIC_RELAXED, __HIP_MEMORY_SCOPE_AGENT); }
DI void flag_st(GAS u64* p, u64 v) { __hip_atomic_store(p, v, __ATOMIC_RELAXED, __HIP_MEMORY_SCOPE_AGENT); }
DI void flag_wait(const GAS u64* p, u64 tag) {
    for (unsigned spins = 0; spins < (1u << 18); ++spins) { if (__all(flag_ld(p) == tag)) break; __builtin_amdgcn_s_sleep(1); }
    CBAR;
}
template <int N> DI float ror_add(float v) { return v + __builtin_bit_cast(float, __builtin_amdgcn_update_dpp(0, __builtin_bit_cast(int, v), 0x120 + N  , 0xf, 0xf, false)); }
DI float sum64(float v) {
    v = ror_add<1>(ror_add<2>(ror_add<4>(ror_add<8>(v))));
    { auto r = __builtin_amdgcn_permlane16_swap(__float_as_uint(v), __float_as_uint(v), false, false); v = __uint_as_float(r[0]) + __uint_as_float(r[1]); }
    { auto r = __builtin_amdgcn_permlane32_swap(__float_as_uint(v), __float_as_uint(v), false, false); v = __uint_as_float(r[0]) + __uint_as_float(r[1]); }
    return v;
}
DI int opaque_tid() { int t = threadIdx.x; asm volatile("" : "+v"(t)); return t; }

#define TQ(i)
#define TQOUT
#define TS(i)
#define TR(i)

DI float silu_mul(float g, float u) { return g * u * __builtin_amdgcn_rcpf(1.0f + __builtin_amdgcn_exp2f(g * -1.4426950408889634f)); }
DI u32x4 pack8(const f32x4& a, const f32x4& b) { u32x4 o; o[0] = cvtpk(a[0], a[1]); o[1] = cvtpk(a[2], a[3]); o[2] = cvtpk(b[0], b[1]); o[3] = cvtpk(b[2], b[3]); return o; }
constexpr float QSCALE = 0.17677669529663687f * 1.4426950408889634f;

struct G128 { const char* a; const char* w2; const char* wo; int kh; };
template <bool HALF> DI void g128_issue(unsigned lds0, const G128& g, int t, int lw, unsigned voffA, unsigned voffB) {
    const unsigned so = lds0 + (t & 3) * 32768, sb = so + lw * 2048;
    const bool att = !HALF && g.kh == 0 && t >= 16;
    const int kt = HALF ? t - 16 : att ? t - 16 : g.kh * 24 + t;
    const char* a = g.a + (size_t)(att ? kt : 16 + kt) * 2048; const char* b = (att ? g.wo : g.w2) + (size_t)kt * (64 * DM * 2);
    if (HALF) glds_pair_sc1<0>(a, voffA, so + (lw >> 1) * 8192 + (lw & 1) * 2048);
    else { glds_pair_sc1<0>(a, voffA, so + lw * 4096); glds_pair_sc1<2048>(a + KB_AH * 1024, voffA, so + lw * 4096); }
    glds<0, 16384>(b, voffB, sb); glds<128, 24576>(b, voffB, sb);
    glds<0, 16384 + 1024>(b + 8 * DM * 2, voffB, sb); glds<128, 24576 + 1024>(b + 8 * DM * 2, voffB, sb);
}

__global__ __launch_bounds__(512, 2) void k_block(const u16* __restrict__ x, const u16* __restrict__ wqkv, const u16* __restrict__ wo, const u16* __restrict__ w1,
                                                  const u16* __restrict__ w3, const u16* __restrict__ w2, char* __restrict__ ws, u16* __restrict__ out) {
    extern __shared__ __attribute__((aligned(16))) char smem[];
    LAS char* lds = (LAS char*)smem;
    const unsigned lds0 = (unsigned)(size_t)lds;
    const int wid = __builtin_amdgcn_readfirstlane(threadIdx.x >> 6);
    const int bid = blockIdx.x, xcd = bid & 7, idx = bid >> 3;
    GAS u64* flags = (GAS u64*)(ws + WS_FLAG);
    const __amdgpu_buffer_rsrc_t rs = __builtin_amdgcn_make_buffer_rsrc((void*)ws, 0, WS_END, 0x00020000);
    {
        const char* pc; asm volatile("s_getpc_b64 %0" : "=s"(pc));
        const int l = threadIdx.x & 63, piece = (idx * 8 + wid) & 7;
        const unsigned o = wid == 0 ? l * (CODE_BYTES / 64) : piece * 4096 + l * 64;
        touch<148480>(pc, o < CODE_BYTES ? o : 0, lds0 + wid * 1024);
    }
    TR(30); TS(0);
    u64 tag;
    u32x4 xa, xb; u32x2 ep;
    { const char* xp = (const char*)x + (size_t)(bid * 8 + wid) * (DM * 2);
      asm volatile("global_load_dwordx4 %0, %3, %4\n\tglobal_load_dwordx4 %1, %3, %4 offset:1024\n\tglobal_load_dwordx2 %2, %5, %6 sc1"
                   : "=&v"(xa), "=&v"(xb), "=&v"(ep) : "v"((threadIdx.x & 63) * 16), "s"(xp), "v"(0), "s"(ws + WS_FLAG), "s"(wqkv), "s"(w1), "s"(w3) : "memory"); }
    const int cj = idx >> 2, ct = cj < 3 ? (xcd >> 1) * 3 + cj : 12 + (xcd >> 1) * 5 + (cj - 3), rt = (xcd & 1) * 4 + (idx & 3);
    const bool qkv = ct < 12;
    u32x4 hmain[8]; u32x2 hext[4];

    {
        const int tid = opaque_tid(), lane = tid & 63, wr = wid >> 2, wc = wid & 3;
        const unsigned ldb2 = qkv ? 3 * DM * 2 : FF * 2;
        const char* cB0 = qkv ? (const char*)wqkv + ct * 512 : (const char*)w1 + (ct - 12) * 256;
        const char* cB1 = qkv ? cB0 + 256 : (const char*)w3 + (ct - 12) * 256;
        const char* cA = ws + WS_XN + rt * 16 * KB_X * 1024;
        const unsigned voffA = wid * (KB_X * 1024) + lane * 16;
        const unsigned voffB = (unsigned)(wid * 8 + (lane >> 3)) * ldb2 + (((lane & 7) ^ (2 * ((lane >> 4) & 3))) * 16);
        const char* pE = ((lane & 1) ? (const char*)w3 : (const char*)w1) + (size_t)(wid * 8 + (lane >> 3)) * (FF * 2) + (2560 + ct * 32) * 2 + ((((lane >> 1) & 3) ^ ((lane >> 4) & 3)) * 16);
        const size_t kstepB = (size_t)64 * ldb2;
        const unsigned ldsA = lds0 + wid * 2048, ldsB = lds0 + wid * 1024;
        const char* cT = ((idx & 2) ? cB1 : cB0) + (idx & 1) * 128;
#define SA(b, h) (((b) * 2 + (h)) * 16384)
#define SB(b, h) (((b) * 2 + (h)) * 16384)
#define STA(b, h, kt) glds_pair_sc1<SA(b, h)>(cA + (h) * 8 * KB_X * 1024 + (kt) * 2048, voffA, ldsA)
#define STB(b, h, kt) do { const char* s_ = ((h) ? cB1 : cB0) + (size_t)(kt) * kstepB; \
        glds<0, 65536 + SB(b, h)>(s_, voffB, ldsB); glds<128, 65536 + SB(b, h) + 8192>(s_, voffB, ldsB); } while (0)
#define TCH(kt) touch<148480>(cT + (size_t)(kt) * kstepB, voffB, ldsB)
#define STE(b, kt) do { const char* s_ = pE + (size_t)(kt) * (64 * FF * 2); \
        asm volatile("s_add_u32 m0, %1, %2\n\ts_nop 0\n\tglobal_load_lds_dwordx4 %0, off" :: "v"(s_), "s"(ldsB), "i"(131072 + (b) * 8192) : "memory", "scc"); } while (0)
        {
            STB(0, 0, 0); STB(0, 1, 0); STE(0, 0);
            asm volatile("s_waitcnt vmcnt(5)" : "+v"(xa), "+v"(xb), "+v"(ep) :: "memory");
            tag = (((u64)(unsigned)__builtin_amdgcn_readfirstlane(ep[1]) << 32) | (unsigned)__builtin_amdgcn_readfirstlane(ep[0])) + 1;
            float f[16];
#pragma unroll
            for (int i = 0; i < 4; ++i) { f[2 * i] = bf_lo(xa[i]); f[2 * i + 1] = bf_hi(xa[i]); f[8 + 2 * i] = bf_lo(xb[i]); f[9 + 2 * i] = bf_hi(xb[i]); }
            float s = 0.f;
#pragma unroll
            for (int i = 0; i < 16; ++i) s += f[i];
            const float mean = sum64(s) * (1.0f / DM);
            float q = 0.f;
#pragma unroll
            for (int i = 0; i < 16; ++i) { const float d = f[i] - mean; q += d * d; }
            const float var = sum64(q) * (1.0f / DM);
            const float mb = rbf(mean), sd = rbf(__builtin_amdgcn_sqrtf(rbf(rbf(var) + 1e-5f))), inv = __builtin_amdgcn_rcpf(sd);
#pragma unroll
            for (int c = 0; c < 2; ++c) { u32x4 o;
#pragma unroll
                for (int i = 0; i < 4; ++i) o[i] = cvtpk(rbf(f[c * 8 + 2 * i] - mb) * inv, rbf(f[c * 8 + 2 * i + 1] - mb) * inv);
                *(LAS u32x4*)(lds + wid * 2080 + c * 1024 + lane * 16) = o; }
            WAIT_L(0); BAR; CBAR;
            const int r = lane & 7, g = (lane >> 3) & 3, row = bid * 8 + r;
#pragma unroll
            for (int c = 0; c < 2; ++c) { const int kb = 4 * wid + 2 * c + (lane >> 5); const LAS char* p = lds + r * 2080 + kb * 64 + g * 8;
                const u32x2 lo = *(const LAS u32x2*)p, hi = *(const LAS u32x2*)(p + 32);
                ST16(((u32x4){lo[0], lo[1], hi[0], hi[1]}), WS_XN + ((row >> 4) * KB_X + kb) * 1024 + (g * 16 + (row & 15)) * 16); }
            WAIT_V(0); BAR;
            TS(1);
            if (wid == 0) { if (lane == 0) flag_st(flags + F_LN + bid, tag); flag_wait(flags + F_LN + rt * 32 + (lane & 31), tag); }
            STB(1, 0, 1); STB(1, 1, 1); STE(1, 1);
            if (wid != 0) { TCH(2); TCH(3); }
            BAR; CBAR;
            TS(2);
        }
        f32x4 acc[2][2][4][2], ace[2][4];
        {
            const int g = lane >> 4, q = (lane & 15) >> 2, p = lane & 3, swz = (2 * g + (q >> 1)) & 3;
            const int aoff = wr * 8192 + lane * 16;
            const int boff0 = 65536 + (wc >> 1) * 8192 + (4 * g + q) * 128 + (((2 * (wc & 1)) ^ swz) * 32) + p * 8, boff1 = boff0 ^ 32;
            int eoff = 131072 + (4 * g + q) * 128 + ((wc ^ swz) * 32) + p * 8; PIN(eoff);
#define LDA(dst, b, h) do { _Pragma("unroll") for (int m = 0; m < 4; ++m) _Pragma("unroll") for (int k = 0; k < 2; ++k) \
        dst[m][k] = *(const LAS bf16x8*)(lds + aoff + SA(b, h) + (m * 2 + k) * 1024); } while (0)
#define LDB(dst, b, h) do { _Pragma("unroll") for (int n = 0; n < 2; ++n) _Pragma("unroll") for (int k = 0; k < 2; ++k) { \
        const LAS char* b_ = lds + (n ? boff1 : boff0) + SB(b, h) + k * 4096; dst[n][k] = cat8(trd(b_), trd(b_ + 2048)); } } while (0)
#define LDE(dst, b) do { _Pragma("unroll") for (int k = 0; k < 2; ++k) { const LAS char* b_ = lds + eoff + (b) * 8192 + k * 4096; dst[k] = cat8(trd(b_), trd(b_ + 2048)); } } while (0)
#define MMA(ai) do { __builtin_amdgcn_s_setprio(1); \
        _Pragma("unroll") for (int k = 0; k < 2; ++k) _Pragma("unroll") for (int m = 0; m < 4; ++m) { \
            MFMA16_ACC(acc[ai][0][m][0], B0[0][k], At[m][k]); MFMA16_ACC(acc[ai][0][m][1], B0[1][k], At[m][k]); \
            MFMA16_ACC(acc[ai][1][m][0], B1[0][k], At[m][k]); MFMA16_ACC(acc[ai][1][m][1], B1[1][k], At[m][k]); \
            MFMA16_ACC(ace[ai][m], BE[k], At[m][k]); } \
        __builtin_amdgcn_s_setprio(0); } while (0)
            STA(0, 0, 0); STA(0, 1, 0); STA(1, 0, 1);
#pragma unroll
            for (int a = 0; a < 2; ++a)
#pragma unroll
                for (int m = 0; m < 4; ++m) { ace[a][m] = (f32x4){0.f, 0.f, 0.f, 0.f}; PIN(ace[a][m]);
#pragma unroll
                    for (int b = 0; b < 2; ++b)
#pragma unroll
                        for (int n = 0; n < 2; ++n) { acc[a][b][m][n] = (f32x4){0.f, 0.f, 0.f, 0.f}; PIN(acc[a][b][m][n]); } }
            bf16x8 At[4][2], B0[2][2], B1[2][2], BE[2];
            constexpr int nt = DM / 64;
            if (wr == 1) BAR;
            WAIT_V(4); BAR;
            WAIT_V(2); BAR;
            TS(3);
            for (int t = 0; t < nt; t += 2) {
                const bool more = t + 2 < nt, far = t + 4 < nt;
                LDB(B0, 0, 0); LDB(B1, 0, 1); LDE(BE, 0); SCHED; LDA(At, 0, 0); STA(1, 1, t + 1);
                if (far) { TCH(t + 4); WAIT_V(11); } else { WAIT_V(9); }
                WAIT_L(0); BAR; MMA(0); BAR; SCHED;
                LDA(At, 0, 1);
                if (more) { STB(0, 0, t + 2); STB(0, 1, t + 2); STE(0, t + 2); STA(0, 0, t + 2); if (far) { WAIT_V(10); } else { WAIT_V(9); } } else { WAIT_V(2); }
                WAIT_L(0); BAR; MMA(1); BAR; SCHED;
                LDB(B0, 1, 0); LDB(B1, 1, 1); LDE(BE, 1); SCHED; LDA(At, 1, 0);
                if (more) { STA(0, 1, t + 2); if (far) { TCH(t + 5); WAIT_V(11); } else { WAIT_V(9); } } else { WAIT_V(0); }
                WAIT_L(0); BAR; MMA(0); BAR; SCHED;
                LDA(At, 1, 1);
                if (more) { STB(1, 0, t + 3); STB(1, 1, t + 3); STE(1, t + 3); STA(1, 0, t + 3); if (far) { WAIT_V(10); } else { WAIT_V(9); } }
                WAIT_L(0); BAR; MMA(1); if (more || wr == 0) BAR; SCHED;
            }
            asm volatile("s_nop 7"); SCHED;
            TS(4);
#undef SA
#undef SB
#undef STA
#undef STB
#undef STE
#undef TCH
#undef LDA
#undef LDB
#undef LDE
#undef MMA
        }
        const int ln = opaque_tid() & 63, fr = ln & 15, fq = ln >> 4;
        if (qkv) {
            const int kind = ct >> 2;
            const float sc = kind == 0 ? QSCALE : 1.0f;
            const unsigned base = WS_Q + kind * (SEQ * DM * 2) + ((ct & 3) * 8 + wc) * 131072 + (rt * 256 + wr * 64) * 64;
            const unsigned lo0 = base + (kind == 2 ? fr * 64 + fq * 16 : fr * 16 + fq * 512), lo1 = lo0 + (kind == 2 ? 1024 : 256);
#pragma unroll
            for (int bj = 0; bj < 2; ++bj)
#pragma unroll
                for (int ai = 0; ai < 2; ++ai)
#pragma unroll
                    for (int m = 0; m < 4; ++m)
                        ST16(pack8(acc[ai][bj][m][0] * sc, acc[ai][bj][m][1] * sc), bj * 4 * 131072 + ai * 8192 + (m >> 1) * 2048 + ((m & 1) ? lo1 : lo0));
            WAIT_V(0); BAR;
            if (wid == 0 && ln == 0) flag_st(flags + F_QKV + rt * 32 + ct, tag);
        } else {
#pragma unroll
            for (int ai = 0; ai < 2; ++ai)
#pragma unroll
                for (int m = 0; m < 4; ++m) { f32x4 h0, h1;
#pragma unroll
                    for (int j = 0; j < 4; ++j) { h0[j] = silu_mul(acc[ai][0][m][0][j], acc[ai][1][m][0][j]); h1[j] = silu_mul(acc[ai][0][m][1][j], acc[ai][1][m][1][j]); }
                    hmain[ai * 4 + m] = pack8(h0, h1);
                }
        }
#pragma unroll
        for (int ai = 0; ai < 2; ++ai)
#pragma unroll
            for (int mp = 0; mp < 2; ++mp) { float h[4];
#pragma unroll
                for (int j = 0; j < 4; ++j) { auto r = __builtin_amdgcn_permlane32_swap(__float_as_uint(ace[ai][2 * mp][j]), __float_as_uint(ace[ai][2 * mp + 1][j]), false, false);
                    h[j] = silu_mul(__uint_as_float(r[0]), __uint_as_float(r[1])); }
                hext[ai * 2 + mp] = (u32x2){cvtpk(h[0], h[1]), cvtpk(h[2], h[3])};
            }
        TS(5);
        TS(6);
    }

    const int kh = xcd & 1, rt2 = ((xcd >> 1) & 1) * 8 + (idx & 7), ct2 = (xcd >> 2) * 4 + (idx >> 3);
    G128 g2; g2.kh = kh; g2.a = ws + WS_AH + rt2 * 8 * KB_AH * 1024; g2.w2 = (const char*)w2 + ct2 * 256; g2.wo = (const char*)wo + ct2 * 256;
#define G2_OFFS(lw_, l_) const unsigned vA = ((lw_) ^ kh) * 2 * KB_AH * 1024 + (l_) * 16, vB = ((lw_) * 16 + ((l_) >> 3)) * (DM * 2) + ((((l_) & 7) ^ (2 * (((l_) >> 4) & 3))) * 16)

    {
        constexpr int AT_BASE = 65536, AT_STAGE = 32768;
        const int lane = opaque_tid() & 63, hi = lane >> 5;
        const int head = xcd * 4 + (idx >> 3), qb = idx & 7;
        if (wid == 0) flag_wait(flags + F_QKV + (lane < 4 ? (lane & 1) * 32 + 4 + (lane >> 1) * 4 : qb * 32) + (head >> 3), tag);
        BAR; CBAR;
        TS(7);
        const char* Kg = ws + WS_K + head * 131072; const char* Vg = ws + WS_V + head * 131072;
        const unsigned soff = wid * 2048 + lane * 16;
        const unsigned sdst = lds0 + AT_BASE + wid * 2048;
#define AT_ISSUE(s, slot) do { glds_pair_sc1<0>(Kg + (s) * 16384, soff, sdst + (slot)); glds_pair_sc1<16384>(Vg + (s) * 16384, soff, sdst + (slot)); } while (0)
        const unsigned qoff = WS_Q + (head * 64 + qb * 8 + wid) * 2048 + lane * 16;
        const bf16x8 q0 = __builtin_bit_cast(bf16x8, LD16(qoff)), q1 = __builtin_bit_cast(bf16x8, LD16(qoff + 1024));
        AT_ISSUE(0, 0); AT_ISSUE(1, AT_STAGE);
        const GAS u64* kvflag = flags + F_QKV + (lane & 7) * 32 + 4 + ((lane >> 3) & 1) * 4 + (head >> 3);
        u64 seen = flag_ld(kvflag);
        bf16x8 sel; { const short one = ((lane & 15) == ((lane >> 4) & 1)) ? (short)0x3f80 : (short)0;
#pragma unroll
            for (int i = 0; i < 8; ++i) sel[i] = one; }
        const LAS char* kb0 = lds + AT_BASE + lane * 16;
        const LAS char* vb0 = lds + AT_BASE + 16384 + (4 * hi + ((lane & 15) >> 2)) * 64 + ((lane >> 4) & 1) * 32 + (lane & 3) * 8;
        f32x16 O, negm, SA, SB; f32x4 L = {0.f, 0.f, 0.f, 0.f};
        bf16x8 k0, k1, v0, v1;
#pragma unroll
        for (int i = 0; i < 16; ++i) { O[i] = 0.f; negm[i] = 0.f; }
        WAIT_V(5); BAR;
        TS(8);
        {
            k0 = *(const LAS bf16x8*)(kb0); k1 = *(const LAS bf16x8*)(kb0 + 1024);
            SA = MFMA32(k0, q0, negm); SA = MFMA32(k1, q1, SA);
            k0 = *(const LAS bf16x8*)(kb0 + 2048); k1 = *(const LAS bf16x8*)(kb0 + 3072);
            v0 = cat8(trd(vb0), trd(vb0 + 512)); v1 = cat8(trd(vb0 + 1024), trd(vb0 + 1536));
            float m = SA[0];
#pragma unroll
            for (int i = 1; i < 16; ++i) m = fmaxf(m, SA[i]);
            m = fmaxf(m, __shfl_xor(m, 32));
#pragma unroll
            for (int i = 0; i < 16; ++i) { negm[i] = -m; SA[i] -= m; }
            PIN(negm);
        }
#define EX(S, i) S[i] = __builtin_amdgcn_exp2f(S[i])
#define BC8(x) __builtin_bit_cast(bf16x8, x)
#define AT_STEP(SC, SN, kp, v1p, v0p) do { \
        O = MFMA32(v1, BC8(p1), O); SCHED; v1 = cat8(trd(v1p), trd((v1p) + 512)); EX(SC, 0); EX(SC, 1); EX(SC, 2); PIN(SC); SCHED; \
        SN = MFMA32(k0, q0, negm); SCHED; k0 = *(const LAS bf16x8*)(kp); EX(SC, 3); EX(SC, 4); EX(SC, 5); PIN(SC); SCHED; \
        L = MFMA16(sel, BC8(p1), L); SCHED; EX(SC, 6); EX(SC, 7); \
        p0[0] = cvtpk(SC[0], SC[1]); p0[1] = cvtpk(SC[2], SC[3]); p0[2] = cvtpk(SC[4], SC[5]); p0[3] = cvtpk(SC[6], SC[7]); PIN(p0); SCHED; \
        O = MFMA32(v0, BC8(p0), O); SCHED; v0 = cat8(trd(v0p), trd((v0p) + 512)); EX(SC, 8); EX(SC, 9); EX(SC, 10); PIN(SC); SCHED; \
        SN = MFMA32(k1, q1, SN); SCHED; k1 = *(const LAS bf16x8*)((kp) + 1024); EX(SC, 11); EX(SC, 12); EX(SC, 13); PIN(SC); SCHED; \
        L = MFMA16(sel, BC8(p0), L); SCHED; EX(SC, 14); EX(SC, 15); \
        p1[0] = cvtpk(SC[8], SC[9]); p1[1] = cvtpk(SC[10], SC[11]); p1[2] = cvtpk(SC[12], SC[13]); p1[3] = cvtpk(SC[14], SC[15]); PIN(p1); SCHED; } while (0)
        u32x4 p0, p1 = {0u, 0u, 0u, 0u};
        int cur = 0, nxt = AT_STAGE, fre = 2 * AT_STAGE;
        const GAS u64* hflag = flags + F_H + (rt2 >> 1) * 32 + (lane & 31);
        const unsigned hb = WS_AH + ((rt * 16 + (wid >> 2) * 4) * KB_AH + KB_X + (ct - 12) * 4 + (wid & 3)) * 1024 + lane * 16;
        const int cq = 2 * (wid & 3) + ((lane >> 4) & 1);
        const unsigned eb = WS_AH + ((rt * 16 + (wid >> 2) * 4 + hi) * KB_AH + KB_X + 80 + ct) * 1024 + ((cq & 3) * 16 + (lane & 15)) * 16 + (cq >> 2) * 8;
#define H_MAIN(j) do { if (!qkv && s < 4) { if (s == 0) ST16(hmain[j], hb + (j) * KB_AH * 1024); else if (s == 1) ST16(hmain[2 + (j)], hb + (2 + (j)) * KB_AH * 1024); \
            else if (s == 2) ST16(hmain[4 + (j)], hb + (8 + (j)) * KB_AH * 1024); else ST16(hmain[6 + (j)], hb + (10 + (j)) * KB_AH * 1024); } SCHED; } while (0)
#define H_EXT() do { if (s < 4) { if (s == 0) ST8(hext[0], eb); else if (s == 1) ST8(hext[1], eb + 2 * KB_AH * 1024); \
            else if (s == 2) ST8(hext[2], eb + 8 * KB_AH * 1024); else ST8(hext[3], eb + 10 * KB_AH * 1024); } SCHED; } while (0)
        for (int s = 0; s < 8; ++s) {
            if (s >= 1 && s <= 4) { if (qkv) { WAIT_V(2); } else { WAIT_V(4); } } else { WAIT_V(0); }
            WAIT_L(0); BAR;
            if (s == 0) { if (!__all(seen == tag)) flag_wait(kvflag, tag); }
            if (s + 2 < 8) AT_ISSUE(s + 2, fre);
            if (s < 4) {
                const int r0 = (idx * 8 + wid) * 10 + (s < 3 ? s * 4 : 0), rl = s == 2 ? ((lane >> 4) & 1) : (lane >> 4);
                const char* src = (kh ? (const char*)w2 + 1024 * (DM * 2) : r0 >= 1536 ? (const char*)wo - 1536 * (DM * 2) : (const char*)w2) + (size_t)r0 * (DM * 2) + (xcd >> 2) * 1024;
                touch<-AT_BASE>(src, rl * (DM * 2) + (lane & 15) * 64, sdst);
            }
            if (s == 5) { if (wid == 0 && lane == 0) flag_st(flags + F_H + rt * 32 + ct, tag); }
            if (s == 6) seen = flag_ld(hflag);
            if (s == 7 && wid >= 4) {
                if (!__all(seen == tag)) flag_wait(hflag, tag);
                G2_OFFS(wid - 4, lane);
                g128_issue<false>(lds0, g2, 0, wid - 4, vA, vB); g128_issue<false>(lds0, g2, 1, wid - 4, vA, vB);
            }
            if (wid >= 4) __builtin_amdgcn_s_setprio(1);
            const LAS char* kc = kb0 + cur; const LAS char* vc = vb0 + cur; const LAS char* kn = kb0 + nxt; const LAS char* vn = vb0 + nxt;
            AT_STEP(SA, SB, kc + 2 * 2048, vc + 1024, vc + 1 * 2048);            AT_STEP(SB, SA, kc + 3 * 2048, vc + 1 * 2048 + 1024, vc + 2 * 2048);
            H_MAIN(0);
            AT_STEP(SA, SB, kc + 4 * 2048, vc + 2 * 2048 + 1024, vc + 3 * 2048); AT_STEP(SB, SA, kc + 5 * 2048, vc + 3 * 2048 + 1024, vc + 4 * 2048);
            H_EXT();
            AT_STEP(SA, SB, kc + 6 * 2048, vc + 4 * 2048 + 1024, vc + 5 * 2048); __builtin_amdgcn_s_setprio(0); AT_STEP(SB, SA, kc + 7 * 2048, vc + 5 * 2048 + 1024, vc + 6 * 2048);
            H_MAIN(1);
            AT_STEP(SA, SB, kn, vc + 6 * 2048 + 1024, vc + 7 * 2048);            AT_STEP(SB, SA, kn + 2048, vc + 7 * 2048 + 1024, vn);
            const int tmp = cur; cur = nxt; nxt = fre; fre = tmp;
        }
        O = MFMA32(v1, BC8(p1), O); L = MFMA16(sel, BC8(p1), L);
        TS(9);
        float lrow; { const float a = __shfl(L[0], lane & 15), b = __shfl(L[1], lane & 15); lrow = (lane & 16) ? b : a; }
        float oabs = 0.f;
#pragma unroll
        for (int i = 0; i < 16; ++i) oabs += fabsf(O[i]);
        if (__any(!(lrow < 1.0e30f) || !(oabs < 3.0e38f))) {
            __builtin_amdgcn_fence(__ATOMIC_ACQUIRE, "agent");
            float m = -INFINITY, l = 0.f;
#pragma unroll
            for (int i = 0; i < 16; ++i) { O[i] = 0.f; negm[i] = 0.f; }
            const u16* Vs = (const u16*)Vg;
            for (int t = 0; t < 64; ++t) { const int key0 = t * 32;
                k0 = *(const bf16x8*)(Kg + key0 * 64 + lane * 16); k1 = *(const bf16x8*)(Kg + key0 * 64 + 1024 + lane * 16);
#pragma unroll
                for (int j = 0; j < 8; ++j) { const int kk = key0 + 8 * (j >> 2) + 4 * hi + (j & 3); v0[j] = (short)Vs[kk * 32 + (lane & 31)]; v1[j] = (short)Vs[(kk + 16) * 32 + (lane & 31)]; }
                f32x16 S = MFMA32(k0, q0, negm); S = MFMA32(k1, q1, S);
                float mt = S[0];
#pragma unroll
                for (int i = 1; i < 16; ++i) mt = fmaxf(mt, S[i]);
                mt = fmaxf(mt, __shfl_xor(mt, 32));
                const float mn = fmaxf(m, mt), al = __builtin_amdgcn_exp2f(m - mn); m = mn;
                float ps = 0.f;
#pragma unroll
                for (int i = 0; i < 16; ++i) { S[i] = rbf(__builtin_amdgcn_exp2f(S[i] - mn)); ps += S[i]; O[i] *= al; }
                l = l * al + ps + __shfl_xor(ps, 32);
#pragma unroll
                for (int i = 0; i < 4; ++i) { p0[i] = cvtpk(S[2 * i], S[2 * i + 1]); p1[i] = cvtpk(S[8 + 2 * i], S[9 + 2 * i]); }
                O = MFMA32(v0, BC8(p0), O); O = MFMA32(v1, BC8(p1), O);
            }
            lrow = l;
        }
        {
            const float inv = 1.0f / lrow;
            unsigned pk[8];
#pragma unroll
            for (int i = 0; i < 8; ++i) pk[i] = cvtpk(O[2 * i] * inv, O[2 * i + 1] * inv);
            const unsigned dst = WS_AH + ((qb * 16 + wid * 2 + ((lane >> 4) & 1)) * KB_AH + head) * 1024 + (lane & 15) * 16 + hi * 256;
#pragma unroll
            for (int c = 0; c < 2; ++c) {
                auto r0 = __builtin_amdgcn_permlane32_swap(pk[4 * c + 0], pk[4 * c + 2], false, false);
                auto r1 = __builtin_amdgcn_permlane32_swap(pk[4 * c + 1], pk[4 * c + 3], false, false);
                ST16(((u32x4){r0[0], r1[0], r0[1], r1[1]}), dst + c * 512);
            }
        }
        if (wid >= 4) { WAIT_V(2); }
        WAIT_L(0); BAR;
        TS(10);
#undef AT_ISSUE
#undef AT_STEP
#undef H_MAIN
#undef H_EXT
#undef EX
#undef BC8
    }

    {
        const int tid = opaque_tid(), lane = tid & 63, tile = rt2 * 8 + ct2;
        GAS u64* myflag = flags + F_G2 + tile * 2 + kh; const GAS u64* pflag = flags + F_G2 + tile * 2 + (kh ^ 1);
        if (wid >= 4) {
            const int lw = wid - 4;
            G2_OFFS(lw, lane);
            const unsigned vH = ((lw >> 1) * 4 + 2 * kh + (lw & 1)) * KB_AH * 1024 + lane * 16;
            g128_issue<false>(lds0, g2, 2, lw, vA, vB); g128_issue<false>(lds0, g2, 3, lw, vA, vB);
            WAIT_V(16);
            for (int t = 0; t < 28; ++t) {
                BAR;
                if (t == 0 && wid == 4 && lane == 0) flag_st(flags + F_AT + (idx & 7) * 32 + xcd * 4 + (idx >> 3), tag);
                if (t == 12 && kh == 0) flag_wait(flags + F_AT + (rt2 >> 1) * 32 + (lane & 31), tag);
                g128_issue<false>(lds0, g2, t + 4, lw, vA, vB);
                WAIT_V(16);
            }
            BAR; g128_issue<true>(lds0, g2, 32, lw, vH, vB); WAIT_V(14);
            for (int t = 29; t < 35; ++t) {
                BAR;
                if (t == 34 && wid == 4 && lane == 0) flag_st(myflag, tag);
                g128_issue<true>(lds0, g2, t + 4, lw, vH, vB); WAIT_V(12);
            }
            BAR; g128_issue<true>(lds0, g2, 39, lw, vH, vB);
            const u64 seen = flag_ld(pflag);
            WAIT_V(13);
            BAR; WAIT_V(7);
            BAR; WAIT_V(0);
            const char* psrc = ws + WS_PART + tile * 32768 + (kh ^ 1) * 16384 + lw * 4096;
            const bool early = __all(seen == tag);
            if (early) { glds_pair_sc1<131072>(psrc, lane * 16, lds0 + lw * 4096); glds_pair_sc1<131072 + 2048>(psrc + 2048, lane * 16, lds0 + lw * 4096); }
            BAR;
            if (!early) { flag_wait(pflag, tag); glds_pair_sc1<131072>(psrc, lane * 16, lds0 + lw * 4096); glds_pair_sc1<131072 + 2048>(psrc + 2048, lane * 16, lds0 + lw * 4096); }
            WAIT_V(0); BAR;
            if (bid == 0 && wid == 4) {
#pragma unroll
                for (int i = 0; i < 4; ++i) flag_wait(flags + F_LN + i * 64 + lane, tag);
                if (lane == 0) flag_st(flags, tag);
            }
        } else {
            const int wm = wid >> 1, wn = wid & 1, g = lane >> 4, q = (lane & 15) >> 2, p = lane & 3, swz = (2 * g + (q >> 1)) & 3;
            const int aoff = wm * 8192 + lane * 16;
            const int boff0 = 16384 + (4 * g + q) * 128 + (((2 * wn) ^ swz) * 32) + p * 8, boff1 = boff0 ^ 32;
            f32x4 acc[4][4]; bf16x8 AX[4], AY[4];
            s16x4 XL[4], XH[4], YL[4], YH[4];
            int pa[2] = {aoff, aoff + 65536}, pb0[2] = {boff0, boff0 + 65536}, pb1[2] = {boff1, boff1 + 65536};
            PIN(pa[1]); PIN(pb0[1]); PIN(pb1[1]);
#define G_BP(n, sl, kk) (lds + (((n) & 1) ? pb1[(sl) >> 1] : pb0[(sl) >> 1]) + ((sl) & 1) * 32768 + ((n) >> 1) * 8192 + (kk) * 4096)
#define G_BL(L_, n, sl, kk) do { L_[n] = trd(G_BP(n, sl, kk)); SCHED; } while (0)
#define G_BH(H_, n, sl, kk) do { H_[n] = trd(G_BP(n, sl, kk) + 2048); SCHED; } while (0)
#define G_A(A_, m, sl, kk) do { A_[m] = *(const LAS bf16x8*)(lds + pa[(sl) >> 1] + ((sl) & 1) * 32768 + ((m) * 2 + (kk)) * 1024); SCHED; } while (0)
#define G_M(A_, L_, H_, m, n) do { MFMA16_ACC(acc[m][n], cat8(L_[n], H_[n]), A_[m]); SCHED; } while (0)
#define G_REST(AC, CL, CH, AN, NL, NH, sl, kk) do { \
        G_BL(NL, 0, sl, kk); G_M(AC, CL, CH, 0, 2); G_BH(NH, 0, sl, kk); G_M(AC, CL, CH, 0, 3); G_A(AN, 0, sl, kk); G_M(AC, CL, CH, 1, 0); G_BL(NL, 1, sl, kk); G_M(AC, CL, CH, 1, 1); \
        G_BH(NH, 1, sl, kk); G_M(AC, CL, CH, 1, 2); G_BL(NL, 2, sl, kk); G_M(AC, CL, CH, 1, 3); G_BH(NH, 2, sl, kk); G_M(AC, CL, CH, 2, 0); G_BL(NL, 3, sl, kk); G_M(AC, CL, CH, 2, 1); \
        G_BH(NH, 3, sl, kk); G_M(AC, CL, CH, 2, 2); G_A(AN, 1, sl, kk); G_M(AC, CL, CH, 2, 3); G_A(AN, 2, sl, kk); G_M(AC, CL, CH, 3, 0); G_A(AN, 3, sl, kk); G_M(AC, CL, CH, 3, 1); \
        G_M(AC, CL, CH, 3, 2); G_M(AC, CL, CH, 3, 3); } while (0)
#define G_TILE(sl) do { G_M(AX, XL, XH, 0, 0); G_M(AX, XL, XH, 0, 1); G_REST(AX, XL, XH, AY, YL, YH, sl, 1); \
        G_M(AY, YL, YH, 0, 0); G_M(AY, YL, YH, 0, 1); \
        asm volatile("s_waitcnt vmcnt(0) lgkmcnt(0)" ::: "memory"); BAR; SCHED;        \
        G_REST(AY, YL, YH, AX, XL, XH, ((sl) + 1) & 3, 0); } while (0)
#pragma unroll
            for (int m = 0; m < 4; ++m)
#pragma unroll
                for (int n = 0; n < 4; ++n) { acc[m][n] = (f32x4){0.f, 0.f, 0.f, 0.f}; PIN(acc[m][n]); }
            SCHED;
#pragma unroll
            for (int i = 0; i < 4; ++i) { G_BL(XL, i, 0, 0); G_BH(XH, i, 0, 0); G_A(AX, i, 0, 0); }
            for (int t = 0; t < 32; t += 4) { G_TILE(0); G_TILE(1); G_TILE(2); G_TILE(3); }
            asm volatile("s_nop 7"); SCHED;
            TS(11);
            const int fr = lane & 15, fq = lane >> 4;
            const size_t rowo = (size_t)(rt2 * 128 + wm * 64 + kh * 32 + fr) * DM + ct2 * 128 + wn * 32 + (fq & 1) * 16 + (fq >> 1) * 8;
            u32x4 xv[2][2];
#pragma unroll
            for (int m = 0; m < 2; ++m)
#pragma unroll
                for (int s = 0; s < 2; ++s) ST16(pack8(acc[2 + m][2 * s], acc[2 + m][2 * s + 1]), WS_PART + tile * 32768 + kh * 16384 + (m * 2 + s) * 4096 + tid * 16);
#pragma unroll
            for (int m = 0; m < 2; ++m)
#pragma unroll
                for (int s = 0; s < 2; ++s) xv[m][s] = *(const u32x4*)(x + rowo + (size_t)m * 16 * DM + s * 64);
            SCHED;
#define H_REST(AC, CL, CH, AN, NL, NH, sl, kk) do { \
        G_BL(NL, 0, sl, kk); G_BH(NH, 0, sl, kk); G_M(AC, CL, CH, 0, 2); G_A(AN, 0, sl, kk); G_BL(NL, 1, sl, kk); G_M(AC, CL, CH, 0, 3); G_BH(NH, 1, sl, kk); G_BL(NL, 2, sl, kk); G_M(AC, CL, CH, 1, 0); \
        G_BH(NH, 2, sl, kk); G_BL(NL, 3, sl, kk); G_M(AC, CL, CH, 1, 1); G_BH(NH, 3, sl, kk); G_M(AC, CL, CH, 1, 2); G_A(AN, 1, sl, kk); G_M(AC, CL, CH, 1, 3); } while (0)
#define H_TILE(sl, SYNC) do { G_M(AX, XL, XH, 0, 0); G_M(AX, XL, XH, 0, 1); H_REST(AX, XL, XH, AY, YL, YH, sl, 1); \
        G_M(AY, YL, YH, 0, 0); G_M(AY, YL, YH, 0, 1); SYNC; SCHED; H_REST(AY, YL, YH, AX, XL, XH, ((sl) + 1) & 3, 0); } while (0)
#define H_BAR do { WAIT_L(0); BAR; } while (0)
            TQ(0); H_TILE(0, H_BAR); TQ(1); H_TILE(1, H_BAR); TQ(2); H_TILE(2, WAIT_V(0); H_BAR); TQ(3); H_TILE(3, H_BAR); TQ(4);
            H_TILE(0, H_BAR); TQ(5); H_TILE(1, H_BAR); TQ(6); H_TILE(2, H_BAR); TQ(7); H_TILE(3, ); TQ(8); TQOUT;
#undef G_BP
#undef G_BL
#undef G_BH
#undef G_A
#undef G_M
#undef G_REST
#undef G_TILE
#undef H_REST
#undef H_TILE
#undef H_BAR
            asm volatile("s_nop 7"); SCHED;
            TS(12);
            BAR; CBAR;
            TS(13);
            u32x4 pv[2][2];
#pragma unroll
            for (int m = 0; m < 2; ++m)
#pragma unroll
                for (int s = 0; s < 2; ++s) pv[m][s] = *(const LAS u32x4*)(lds + 131072 + (m * 2 + s) * 4096 + tid * 16);
#pragma unroll
            for (int m = 0; m < 2; ++m)
#pragma unroll
                for (int s = 0; s < 2; ++s) { float lo[4], hi4[4]; const u32x4 p_ = pv[m][s], x_ = xv[m][s];
                    const f32x4 a0 = acc[m][2 * s] + (f32x4){bf_lo(p_[0]), bf_hi(p_[0]), bf_lo(p_[1]), bf_hi(p_[1])}, a1 = acc[m][2 * s + 1] + (f32x4){bf_lo(p_[2]), bf_hi(p_[2]), bf_lo(p_[3]), bf_hi(p_[3])};
#pragma unroll
                    for (int i = 0; i < 4; ++i) { auto r = __builtin_amdgcn_permlane16_swap(__float_as_uint(a0[i]), __float_as_uint(a1[i]), false, false);
                        lo[i] = __uint_as_float(r[0]); hi4[i] = __uint_as_float(r[1]); }
                    u32x4 o; o[0] = cvtpk(lo[0] + bf_lo(x_[0]), lo[1] + bf_hi(x_[0])); o[1] = cvtpk(lo[2] + bf_lo(x_[1]), lo[3] + bf_hi(x_[1]));
                    o[2] = cvtpk(hi4[0] + bf_lo(x_[2]), hi4[1] + bf_hi(x_[2])); o[3] = cvtpk(hi4[2] + bf_lo(x_[3]), hi4[3] + bf_hi(x_[3]));
                    __builtin_nontemporal_store(o, (u32x4*)(out + rowo + (size_t)m * 16 * DM + s * 64));
                }
        }
        TS(14); TR(31);
    }
}

extern "C" void kernel_launch(void* const* d_in, const int* in_sizes, int n_in, void* d_out, int out_size, void* d_ws, size_t ws_size, hipStream_t stream) {
    (void)hipFuncSetAttribute((const void*)k_block, hipFuncAttributeMaxDynamicSharedMemorySize, LDS_BYTES);
    k_block<<<256, 512, LDS_BYTES, stream>>>((const u16*)d_in[0], (const u16*)d_in[1], (const u16*)d_in[2], (const u16*)d_in[4], (const u16*)d_in[5], (const u16*)d_in[6], (char*)d_ws, (u16*)d_out);
}
```

```cpp
#include <hip/hip_runtime.h>
#include <hip/hip_bf16.h>
#include <stdint.h>

#define LAS __attribute__((address_space(3)))
#define GAS __attribute__((address_space(1)))
#define DI __device__ __forceinline__
typedef unsigned short u16;
typedef unsigned long long u64;
typedef short bf16x8 __attribute__((ext_vector_type(8)));
typedef short s16x4 __attribute__((ext_vector_type(4)));
typedef float f32x2 __attribute__((ext_vector_type(2)));
typedef float f32x4 __attribute__((ext_vector_type(4)));
typedef float f32x16 __attribute__((ext_vector_type(16)));
typedef unsigned u32x2 __attribute__((ext_vector_type(2)));
typedef unsigned u32x4 __attribute__((ext_vector_type(4)));
typedef __bf16 bf16x2_t __attribute__((ext_vector_type(2)));

constexpr int SEQ = 2048, DM = 1024, FF = 3584;
constexpr int KB_X = DM / 32;
constexpr int KB_AH = (DM + FF) / 32;
constexpr unsigned WS_XN = 0;
constexpr unsigned WS_Q = WS_XN + SEQ * DM * 2;
constexpr unsigned WS_K = WS_Q + SEQ * DM * 2;
constexpr unsigned WS_V = WS_K + SEQ * DM * 2;
constexpr unsigned WS_AH = WS_V + SEQ * DM * 2;
constexpr unsigned WS_PART = WS_AH + SEQ * (DM + FF) * 2;
constexpr unsigned WS_FLAG = WS_PART + 128 * 32768;
constexpr unsigned WS_END = WS_FLAG + 16384;
constexpr int F_LN = 32, F_QKV = F_LN + 256, F_H = F_QKV + 256, F_AT = F_H + 256, F_G2 = F_AT + 256;
constexpr int LDS_BYTES = 163840;

DI unsigned cvtpk(float lo, float hi) { f32x2 v = {lo, hi}; bf16x2_t b = __builtin_convertvector(v, bf16x2_t); return __builtin_bit_cast(unsigned, b); }
DI float bf_lo(unsigned u) { return __uint_as_float(u << 16); }
DI float bf_hi(unsigned u) { return __uint_as_float(u & 0xffff0000u); }
DI float rbf(float f) { return __uint_as_float(cvtpk(f, f) << 16); }
DI s16x4 trd(const LAS char* p) { return __builtin_amdgcn_ds_read_tr16_b64_v4i16((LAS s16x4*)p); }
DI bf16x8 cat8(s16x4 a, s16x4 b) { return __builtin_shufflevector(a, b, 0, 1, 2, 3, 4, 5, 6, 7); }
template <int OFF, int D> DI void glds(const char* base, unsigned voff, unsigned dst) {
    asm volatile("s_add_u32 m0, %2, %4\n\ts_nop 0\n\tglobal_load_lds_dwordx4 %0, %1 offset:%3" :: "v"(voff), "s"(base), "s"(dst), "i"(OFF), "i"(D - OFF) : "memory", "scc"); }
template <int D> DI void glds_pair_sc1(const char* base, unsigned voff, unsigned dst) {
    asm volatile("s_add_u32 m0, %2, %3\n\ts_nop 0\n\tglobal_load_lds_dwordx4 %0, %1 sc1\n\tglobal_load_lds_dwordx4 %0, %1 offset:1024 sc1" :: "v"(voff), "s"(base), "s"(dst), "i"(D) : "memory", "scc"); }
template <int D> DI void touch(const char* base, unsigned voff, unsigned dst) {
    asm volatile("s_add_u32 m0, %2, %3\n\ts_nop 0\n\tglobal_load_lds_dword %0, %1" :: "v"(voff), "s"(base), "s"(dst), "i"(D) : "memory", "scc"); }
#define WAIT_V(n) asm volatile("s_waitcnt vmcnt(" #n ")" ::: "memory")
#define WAIT_L(n) asm volatile("s_waitcnt lgkmcnt(" #n ")" ::: "memory")
#define BAR __builtin_amdgcn_s_barrier()
#define CBAR asm volatile("" ::: "memory")
#define SCHED __builtin_amdgcn_sched_barrier(0)
#define PIN(x) asm volatile("" : "+v"(x))
#define MFMA16(a, b, c) __builtin_amdgcn_mfma_f32_16x16x32_bf16(a, b, c, 0, 0, 0)
#define MFMA16_ACC(c, a, b) asm volatile("v_mfma_f32_16x16x32_bf16 %0, %1, %2, %0" : "+v"(c) : "v"(a), "v"(b))
#define MFMA32(a, b, c) __builtin_amdgcn_mfma_f32_32x32x16_bf16(a, b, c, 0, 0, 0)
#define ST16(v, off) __builtin_amdgcn_raw_buffer_store_b128(v, rs, (int)(off), 0, 16)
#define ST8(v, off) __builtin_amdgcn_raw_buffer_store_b64(v, rs, (int)(off), 0, 16)
#define LD16(off) __builtin_amdgcn_raw_buffer_load_b128(rs, (int)(off), 0, 16)
DI u64 flag_ld(const GAS u64* p) { return __hip_atomic_load(p, __ATOMIC_RELAXED, __HIP_MEMORY_SCOPE_AGENT); }
DI void flag_st(GAS u64* p, u64 v) { __hip_atomic_store(p, v, __ATOMIC_RELAXED, __HIP_MEMORY_SCOPE_AGENT); }
DI void flag_wait(const GAS u64* p, u64 tag) {
    for (unsigned spins = 0; spins < (1u << 18); ++spins) { if (__all(flag_ld(p) == tag)) break; __builtin_amdgcn_s_sleep(1); }
    CBAR;
}
template <int N> DI float ror_add(float v) { return v + __builtin_bit_cast(float, __builtin_amdgcn_update_dpp(0, __builtin_bit_cast(int, v), 0x120 + N  , 0xf, 0xf, false)); }
DI float sum64(float v) {
    v = ror_add<1>(ror_add<2>(ror_add<4>(ror_add<8>(v))));
    { auto r = __builtin_amdgcn_permlane16_swap(__float_as_uint(v), __float_as_uint(v), false, false); v = __uint_as_float(r[0]) + __uint_as_float(r[1]); }
    { auto r = __builtin_amdgcn_permlane32_swap(__float_as_uint(v), __float_as_uint(v), false, false); v = __uint_as_float(r[0]) + __uint_as_float(r[1]); }
    return v;
}
DI int opaque_tid() { int t = threadIdx.x; asm volatile("" : "+v"(t)); return t; }

DI float silu_mul(float g, float u) { return g * u * __builtin_amdgcn_rcpf(1.0f + __builtin_amdgcn_exp2f(g * -1.4426950408889634f)); }
DI u32x4 pack8(const f32x4& a, const f32x4& b) { u32x4 o; o[0] = cvtpk(a[0], a[1]); o[1] = cvtpk(a[2], a[3]); o[2] = cvtpk(b[0], b[1]); o[3] = cvtpk(b[2], b[3]); return o; }
constexpr float QSCALE = 0.17677669529663687f * 1.4426950408889634f;


struct G128 { const char* a; const char* w2; const char* wo; int kh; };
template <bool HALF> DI void g128_issue(unsigned lds0, const G128& g, int t, int lw, unsigned voffA, unsigned voffB) {
    const unsigned so = lds0 + (t & 3) * 32768, sb = so + lw * 2048;
    const bool att = !HALF && g.kh == 0 && t >= 16;
    const int kt = HALF ? t - 16 : att ? t - 16 : g.kh * 24 + t;
    const char* a = g.a + (size_t)(att ? kt : 16 + kt) * 2048; const char* b = (att ? g.wo : g.w2) + (size_t)kt * (64 * DM * 2);
    if (HALF) glds_pair_sc1<0>(a, voffA, so + (lw >> 1) * 8192 + (lw & 1) * 2048);
    else { glds_pair_sc1<0>(a, voffA, so + lw * 4096); glds_pair_sc1<2048>(a + KB_AH * 1024, voffA, so + lw * 4096); }
    glds<0, 16384>(b, voffB, sb); glds<128, 24576>(b, voffB, sb);
    glds<0, 16384 + 1024>(b + 8 * DM * 2, voffB, sb); glds<128, 24576 + 1024>(b + 8 * DM * 2, voffB, sb);
}

__global__ __launch_bounds__(512, 2) void k_block(const u16* __restrict__ x, const u16* __restrict__ wqkv, const u16* __restrict__ wo, const u16* __restrict__ w1,
                                                  const u16* __restrict__ w3, const u16* __restrict__ w2, char* __restrict__ ws, u16* __restrict__ out) {
    extern __shared__ __attribute__((aligned(16))) char smem[];
    LAS char* lds = (LAS char*)smem;
    const unsigned lds0 = (unsigned)(size_t)lds;
    const int wid = __builtin_amdgcn_readfirstlane(threadIdx.x >> 6);
    const int bid = blockIdx.x, xcd = bid & 7, idx = bid >> 3;
    GAS u64* flags = (GAS u64*)(ws + WS_FLAG);
    const __amdgpu_buffer_rsrc_t rs = __builtin_amdgcn_make_buffer_rsrc((void*)ws, 0, WS_END, 0x00020000);
    {
        const char* pc; asm volatile("s_getpc_b64 %0" : "=s"(pc));
        if (wid == 7) touch<148480>(pc + (idx & 7) * 4096, (threadIdx.x & 63) * 64, lds0 + 7 * 1024);
    }
    u64 tag;
    u32x4 xa, xb; u32x2 ep;
    { const char* xp = (const char*)x + (size_t)(bid * 8 + wid) * (DM * 2);
      asm volatile("global_load_dwordx4 %0, %3, %4\n\tglobal_load_dwordx4 %1, %3, %4 offset:1024\n\tglobal_load_dwordx2 %2, %5, %6 sc1"
                   : "=&v"(xa), "=&v"(xb), "=&v"(ep) : "v"((threadIdx.x & 63) * 16), "s"(xp), "v"(0), "s"(ws + WS_FLAG), "s"(wqkv), "s"(w1), "s"(w3) : "memory"); }
    const int cj = idx >> 2, ct = cj < 3 ? (xcd >> 1) * 3 + cj : 12 + (xcd >> 1) * 5 + (cj - 3), rt = (xcd & 1) * 4 + (idx & 3);
    const bool qkv = ct < 12;
    u32x4 hmain[8]; u32x2 hext[4];

    {
        const int tid = opaque_tid(), lane = tid & 63, wr = wid >> 2, wc = wid & 3;
        const unsigned ldb2 = qkv ? 3 * DM * 2 : FF * 2;
        const char* cB0 = qkv ? (const char*)wqkv + ct * 512 : (const char*)w1 + (ct - 12) * 256;
        const char* cB1 = qkv ? cB0 + 256 : (const char*)w3 + (ct - 12) * 256;
        const char* cA = ws + WS_XN + rt * 16 * KB_X * 1024;
        const unsigned voffA = wid * (KB_X * 1024) + lane * 16;
        const unsigned voffB = (unsigned)(wid * 8 + (lane >> 3)) * ldb2 + (((lane & 7) ^ (2 * ((lane >> 4) & 3))) * 16);
        const char* pE = ((lane & 1) ? (const char*)w3 : (const char*)w1) + (size_t)(wid * 8 + (lane >> 3)) * (FF * 2) + (2560 + ct * 32) * 2 + ((((lane >> 1) & 3) ^ ((lane >> 4) & 3)) * 16);
        const size_t kstepB = (size_t)64 * ldb2;
        const unsigned ldsA = lds0 + wid * 2048, ldsB = lds0 + wid * 1024;
        const char* cT = ((idx & 2) ? cB1 : cB0) + (idx & 1) * 128;
#define SA(b, h) (((b) * 2 + (h)) * 16384)
#define SB(b, h) (((b) * 2 + (h)) * 16384)
#define STA(b, h, kt) glds_pair_sc1<SA(b, h)>(cA + (h) * 8 * KB_X * 1024 + (kt) * 2048, voffA, ldsA)
#define STB(b, h, kt) do { const char* s_ = ((h) ? cB1 : cB0) + (size_t)(kt) * kstepB; \
        glds<0, 65536 + SB(b, h)>(s_, voffB, ldsB); glds<128, 65536 + SB(b, h) + 8192>(s_, voffB, ldsB); } while (0)
#define TCH(kt) touch<148480>(cT + (size_t)(kt) * kstepB, voffB, ldsB)
#define STE(b, kt) do { const char* s_ = pE + (size_t)(kt) * (64 * FF * 2); \
        asm volatile("s_add_u32 m0, %1, %2\n\ts_nop 0\n\tglobal_load_lds_dwordx4 %0, off" :: "v"(s_), "s"(ldsB), "i"(131072 + (b) * 8192) : "memory", "scc"); } while (0)
        {
            TCH(0); TCH(1);
            { const char* s_ = pE + (size_t)(idx & 1) * (64 * FF * 2) + (ptrdiff_t)(((idx >> 1) & 1) - (wid >> 2)) * (32 * FF * 2);
              asm volatile("s_add_u32 m0, %1, 148480\n\ts_nop 0\n\tglobal_load_lds_dword %0, off" :: "v"(s_), "s"(ldsB) : "memory", "scc"); }
            asm volatile("s_waitcnt vmcnt(3)" : "+v"(xa), "+v"(xb), "+v"(ep) :: "memory");
            tag = (((u64)(unsigned)__builtin_amdgcn_readfirstlane(ep[1]) << 32) | (unsigned)__builtin_amdgcn_readfirstlane(ep[0])) + 1;
            float f[16];
#pragma unroll
            for (int i = 0; i < 4; ++i) { f[2 * i] = bf_lo(xa[i]); f[2 * i + 1] = bf_hi(xa[i]); f[8 + 2 * i] = bf_lo(xb[i]); f[9 + 2 * i] = bf_hi(xb[i]); }
            float s = 0.f;
#pragma unroll
            for (int i = 0; i < 16; ++i) s += f[i];
            const float mean = sum64(s) * (1.0f / DM);
            float q = 0.f;
#pragma unroll
            for (int i = 0; i < 16; ++i) { const float d = f[i] - mean; q += d * d; }
            const float var = sum64(q) * (1.0f / DM);
            const float mb = rbf(mean), sd = rbf(__builtin_amdgcn_sqrtf(rbf(rbf(var) + 1e-5f))), inv = __builtin_amdgcn_rcpf(sd);
#pragma unroll
            for (int c = 0; c < 2; ++c) { u32x4 o;
#pragma unroll
                for (int i = 0; i < 4; ++i) o[i] = cvtpk(rbf(f[c * 8 + 2 * i] - mb) * inv, rbf(f[c * 8 + 2 * i + 1] - mb) * inv);
                *(LAS u32x4*)(lds + wid * 2080 + c * 1024 + lane * 16) = o; }
            WAIT_L(0); BAR; CBAR;
            const int r = lane & 7, g = (lane >> 3) & 3, row = bid * 8 + r;
#pragma unroll
            for (int c = 0; c < 2; ++c) { const int kb = 4 * wid + 2 * c + (lane >> 5); const LAS char* p = lds + r * 2080 + kb * 64 + g * 8;
                const u32x2 lo = *(const LAS u32x2*)p, hi = *(const LAS u32x2*)(p + 32);
                ST16(((u32x4){lo[0], lo[1], hi[0], hi[1]}), WS_XN + ((row >> 4) * KB_X + kb) * 1024 + (g * 16 + (row & 15)) * 16); }
            CBAR;
            STB(0, 0, 0); STB(0, 1, 0); STE(0, 0);
            WAIT_V(5); BAR;
            if (wid == 0) { if (lane == 0) flag_st(flags + F_LN + bid, tag); flag_wait(flags + F_LN + rt * 32 + (lane & 31), tag); }
            STB(1, 0, 1); STB(1, 1, 1); STE(1, 1);
            if (wid != 0) { TCH(2); TCH(3); }
            BAR; CBAR;
        }
        f32x4 acc[2][2][4][2], ace[2][4];
        {
            const int g = lane >> 4, q = (lane & 15) >> 2, p = lane & 3, swz = (2 * g + (q >> 1)) & 3;
            const int aoff = wr * 8192 + lane * 16;
            const int boff0 = 65536 + (wc >> 1) * 8192 + (4 * g + q) * 128 + (((2 * (wc & 1)) ^ swz) * 32) + p * 8, boff1 = boff0 ^ 32;
            int eoff = 131072 + (4 * g + q) * 128 + ((wc ^ swz) * 32) + p * 8; PIN(eoff);
#define LDA(dst, b, h) do { _Pragma("unroll") for (int m = 0; m < 4; ++m) _Pragma("unroll") for (int k = 0; k < 2; ++k) \
        dst[m][k] = *(const LAS bf16x8*)(lds + aoff + SA(b, h) + (m * 2 + k) * 1024); } while (0)
#define LDB(dst, b, h) do { _Pragma("unroll") for (int n = 0; n < 2; ++n) _Pragma("unroll") for (int k = 0; k < 2; ++k) { \
        const LAS char* b_ = lds + (n ? boff1 : boff0) + SB(b, h) + k * 4096; dst[n][k] = cat8(trd(b_), trd(b_ + 2048)); } } while (0)
#define LDE(dst, b) do { _Pragma("unroll") for (int k = 0; k < 2; ++k) { const LAS char* b_ = lds + eoff + (b) * 8192 + k * 4096; dst[k] = cat8(trd(b_), trd(b_ + 2048)); } } while (0)
#define MMA(ai) do { __builtin_amdgcn_s_setprio(1); \
        _Pragma("unroll") for (int k = 0; k < 2; ++k) _Pragma("unroll") for (int m = 0; m < 4; ++m) { \
            MFMA16_ACC(acc[ai][0][m][0], B0[0][k], At[m][k]); MFMA16_ACC(acc[ai][0][m][1], B0[1][k], At[m][k]); \
            MFMA16_ACC(acc[ai][1][m][0], B1[0][k], At[m][k]); MFMA16_ACC(acc[ai][1][m][1], B1[1][k], At[m][k]); \
            MFMA16_ACC(ace[ai][m], BE[k], At[m][k]); } \
        __builtin_amdgcn_s_setprio(0); } while (0)
            STA(0, 0, 0); STA(0, 1, 0); STA(1, 0, 1);
#pragma unroll
            for (int a = 0; a < 2; ++a)
#pragma unroll
                for (int m = 0; m < 4; ++m) { ace[a][m] = (f32x4){0.f, 0.f, 0.f, 0.f}; PIN(ace[a][m]);
#pragma unroll
                    for (int b = 0; b < 2; ++b)
#pragma unroll
                        for (int n = 0; n < 2; ++n) { acc[a][b][m][n] = (f32x4){0.f, 0.f, 0.f, 0.f}; PIN(acc[a][b][m][n]); } }
            bf16x8 At[4][2], B0[2][2], B1[2][2], BE[2];
            constexpr int nt = DM / 64;
            if (wr == 1) BAR;
            WAIT_V(4); BAR;
            WAIT_V(2); BAR;
            for (int t = 0; t < nt; t += 2) {
                const bool more = t + 2 < nt, far = t + 4 < nt;
                LDB(B0, 0, 0); LDB(B1, 0, 1); LDE(BE, 0); SCHED; LDA(At, 0, 0); STA(1, 1, t + 1);
                if (far) { TCH(t + 4); WAIT_V(11); } else { WAIT_V(9); }
                WAIT_L(0); BAR; MMA(0); BAR; SCHED;
                LDA(At, 0, 1);
                if (more) { STB(0, 0, t + 2); STB(0, 1, t + 2); STE(0, t + 2); STA(0, 0, t + 2); if (far) { WAIT_V(10); } else { WAIT_V(9); } } else { WAIT_V(2); }
                WAIT_L(0); BAR; MMA(1); BAR; SCHED;
                LDB(B0, 1, 0); LDB(B1, 1, 1); LDE(BE, 1); SCHED; LDA(At, 1, 0);
                if (more) { STA(0, 1, t + 2); if (far) { TCH(t + 5); WAIT_V(11); } else { WAIT_V(9); } } else { WAIT_V(0); }
                WAIT_L(0); BAR; MMA(0); BAR; SCHED;
                LDA(At, 1, 1);
                if (more) { STB(1, 0, t + 3); STB(1, 1, t + 3); STE(1, t + 3); STA(1, 0, t + 3); if (far) { WAIT_V(10); } else { WAIT_V(9); } }
                WAIT_L(0); BAR; MMA(1); if (more || wr == 0) BAR; SCHED;
            }
            asm volatile("s_nop 7"); SCHED;
#undef SA
#undef SB
#undef STA
#undef STB
#undef STE
#undef TCH
#undef LDA
#undef LDB
#undef LDE
#undef MMA
        }
        const int ln = opaque_tid() & 63, fr = ln & 15, fq = ln >> 4;
        if (qkv) {
            const int kind = ct >> 2;
            const float sc = kind == 0 ? QSCALE : 1.0f;
            const unsigned base = WS_Q + kind * (SEQ * DM * 2) + ((ct & 3) * 8 + wc) * 131072 + (rt * 256 + wr * 64) * 64;
            const unsigned lo0 = base + (kind == 2 ? fr * 64 + fq * 16 : fr * 16 + fq * 512), lo1 = lo0 + (kind == 2 ? 1024 : 256);
#pragma unroll
            for (int bj = 0; bj < 2; ++bj)
#pragma unroll
                for (int ai = 0; ai < 2; ++ai)
#pragma unroll
                    for (int m = 0; m < 4; ++m)
                        ST16(pack8(acc[ai][bj][m][0] * sc, acc[ai][bj][m][1] * sc), bj * 4 * 131072 + ai * 8192 + (m >> 1) * 2048 + ((m & 1) ? lo1 : lo0));
            WAIT_V(0); BAR;
            if (wid == 0 && ln == 0) flag_st(flags + F_QKV + rt * 32 + ct, tag);
        } else {
#pragma unroll
            for (int ai = 0; ai < 2; ++ai)
#pragma unroll
                for (int m = 0; m < 4; ++m) { f32x4 h0, h1;
#pragma unroll
                    for (int j = 0; j < 4; ++j) { h0[j] = silu_mul(acc[ai][0][m][0][j], acc[ai][1][m][0][j]); h1[j] = silu_mul(acc[ai][0][m][1][j], acc[ai][1][m][1][j]); }
                    hmain[ai * 4 + m] = pack8(h0, h1);
                }
        }
#pragma unroll
        for (int ai = 0; ai < 2; ++ai)
#pragma unroll
            for (int mp = 0; mp < 2; ++mp) { float h[4];
#pragma unroll
                for (int j = 0; j < 4; ++j) { auto r = __builtin_amdgcn_permlane32_swap(__float_as_uint(ace[ai][2 * mp][j]), __float_as_uint(ace[ai][2 * mp + 1][j]), false, false);
                    h[j] = silu_mul(__uint_as_float(r[0]), __uint_as_float(r[1])); }
                hext[ai * 2 + mp] = (u32x2){cvtpk(h[0], h[1]), cvtpk(h[2], h[3])};
            }
    }

    const int kh = xcd & 1, rt2 = ((xcd >> 1) & 1) * 8 + (idx & 7), ct2 = (xcd >> 2) * 4 + (idx >> 3);
    G128 g2; g2.kh = kh; g2.a = ws + WS_AH + rt2 * 8 * KB_AH * 1024; g2.w2 = (const char*)w2 + ct2 * 256; g2.wo = (const char*)wo + ct2 * 256;
#define G2_OFFS(lw_, l_) const unsigned vA = ((lw_) ^ kh) * 2 * KB_AH * 1024 + (l_) * 16, vB = ((lw_) * 16 + ((l_) >> 3)) * (DM * 2) + ((((l_) & 7) ^ (2 * (((l_) >> 4) & 3))) * 16)

    {
        constexpr int AT_BASE = 65536, AT_STAGE = 32768;
        const int lane = opaque_tid() & 63, hi = lane >> 5;
        const int head = xcd * 4 + (idx >> 3), qb = idx & 7;
        if (wid == 0) flag_wait(flags + F_QKV + (lane < 4 ? (lane & 1) * 32 + 4 + (lane >> 1) * 4 : qb * 32) + (head >> 3), tag);
        BAR; CBAR;
        const char* Kg = ws + WS_K + head * 131072; const char* Vg = ws + WS_V + head * 131072;
        const unsigned soff = wid * 2048 + lane * 16;
        const unsigned sdst = lds0 + AT_BASE + wid * 2048;
#define AT_ISSUE(s, slot) do { glds_pair_sc1<0>(Kg + (s) * 16384, soff, sdst + (slot)); glds_pair_sc1<16384>(Vg + (s) * 16384, soff, sdst + (slot)); } while (0)
        const unsigned qoff = WS_Q + (head * 64 + qb * 8 + wid) * 2048 + lane * 16;
        const bf16x8 q0 = __builtin_bit_cast(bf16x8, LD16(qoff)), q1 = __builtin_bit_cast(bf16x8, LD16(qoff + 1024));
        AT_ISSUE(0, 0); AT_ISSUE(1, AT_STAGE);
        const GAS u64* kvflag = flags + F_QKV + (lane & 7) * 32 + 4 + ((lane >> 3) & 1) * 4 + (head >> 3);
        u64 seen = flag_ld(kvflag);
        bf16x8 sel; { const short one = ((lane & 15) == ((lane >> 4) & 1)) ? (short)0x3f80 : (short)0;
#pragma unroll
            for (int i = 0; i < 8; ++i) sel[i] = one; }
        const LAS char* kb0 = lds + AT_BASE + lane * 16;
        const LAS char* vb0 = lds + AT_BASE + 16384 + (4 * hi + ((lane & 15) >> 2)) * 64 + ((lane >> 4) & 1) * 32 + (lane & 3) * 8;
        f32x16 O, negm, SA, SB; f32x4 L = {0.f, 0.f, 0.f, 0.f};
        bf16x8 k0, k1, v0, v1;
#pragma unroll
        for (int i = 0; i < 16; ++i) { O[i] = 0.f; negm[i] = 0.f; }
        WAIT_V(5); BAR;
        {
            k0 = *(const LAS bf16x8*)(kb0); k1 = *(const LAS bf16x8*)(kb0 + 1024);
            SA = MFMA32(k0, q0, negm); SA = MFMA32(k1, q1, SA);
            k0 = *(const LAS bf16x8*)(kb0 + 2048); k1 = *(const LAS bf16x8*)(kb0 + 3072);
            v0 = cat8(trd(vb0), trd(vb0 + 512)); v1 = cat8(trd(vb0 + 1024), trd(vb0 + 1536));
            float m = SA[0];
#pragma unroll
            for (int i = 1; i < 16; ++i) m = fmaxf(m, SA[i]);
            m = fmaxf(m, __shfl_xor(m, 32));
#pragma unroll
            for (int i = 0; i < 16; ++i) { negm[i] = -m; SA[i] -= m; }
            PIN(negm);
        }
#define EX(S, i) S[i] = __builtin_amdgcn_exp2f(S[i])
#define BC8(x) __builtin_bit_cast(bf16x8, x)
#define AT_STEP(SC, SN, kp, v1p, v0p) do { \
        O = MFMA32(v1, BC8(p1), O); SCHED; v1 = cat8(trd(v1p), trd((v1p) + 512)); EX(SC, 0); EX(SC, 1); EX(SC, 2); PIN(SC); SCHED; \
        SN = MFMA32(k0, q0, negm); SCHED; k0 = *(const LAS bf16x8*)(kp); EX(SC, 3); EX(SC, 4); EX(SC, 5); PIN(SC); SCHED; \
        L = MFMA16(sel, BC8(p1), L); SCHED; EX(SC, 6); EX(SC, 7); \
        p0[0] = cvtpk(SC[0], SC[1]); p0[1] = cvtpk(SC[2], SC[3]); p0[2] = cvtpk(SC[4], SC[5]); p0[3] = cvtpk(SC[6], SC[7]); PIN(p0); SCHED; \
        O = MFMA32(v0, BC8(p0), O); SCHED; v0 = cat8(trd(v0p), trd((v0p) + 512)); EX(SC, 8); EX(SC, 9); EX(SC, 10); PIN(SC); SCHED; \
        SN = MFMA32(k1, q1, SN); SCHED; k1 = *(const LAS bf16x8*)((kp) + 1024); EX(SC, 11); EX(SC, 12); EX(SC, 13); PIN(SC); SCHED; \
        L = MFMA16(sel, BC8(p0), L); SCHED; EX(SC, 14); EX(SC, 15); \
        p1[0] = cvtpk(SC[8], SC[9]); p1[1] = cvtpk(SC[10], SC[11]); p1[2] = cvtpk(SC[12], SC[13]); p1[3] = cvtpk(SC[14], SC[15]); PIN(p1); SCHED; } while (0)
        u32x4 p0, p1 = {0u, 0u, 0u, 0u};
        int cur = 0, nxt = AT_STAGE, fre = 2 * AT_STAGE;
        const GAS u64* hflag = flags + F_H + (rt2 >> 1) * 32 + (lane & 31);
        const unsigned hb = WS_AH + ((rt * 16 + (wid >> 2) * 4) * KB_AH + KB_X + (ct - 12) * 4 + (wid & 3)) * 1024 + lane * 16;
        const int cq = 2 * (wid & 3) + ((lane >> 4) & 1);
        const unsigned eb = WS_AH + ((rt * 16 + (wid >> 2) * 4 + hi) * KB_AH + KB_X + 80 + ct) * 1024 + ((cq & 3) * 16 + (lane & 15)) * 16 + (cq >> 2) * 8;
#define H_MAIN(j) do { if (!qkv && s < 4) { if (s == 0) ST16(hmain[j], hb + (j) * KB_AH * 1024); else if (s == 1) ST16(hmain[2 + (j)], hb + (2 + (j)) * KB_AH * 1024); \
            else if (s == 2) ST16(hmain[4 + (j)], hb + (8 + (j)) * KB_AH * 1024); else ST16(hmain[6 + (j)], hb + (10 + (j)) * KB_AH * 1024); } SCHED; } while (0)
#define H_EXT() do { if (s < 4) { if (s == 0) ST8(hext[0], eb); else if (s == 1) ST8(hext[1], eb + 2 * KB_AH * 1024); \
            else if (s == 2) ST8(hext[2], eb + 8 * KB_AH * 1024); else ST8(hext[3], eb + 10 * KB_AH * 1024); } SCHED; } while (0)
        for (int s = 0; s < 8; ++s) {
            if (s >= 1 && s <= 4) { if (qkv) { WAIT_V(2); } else { WAIT_V(4); } } else { WAIT_V(0); }
            WAIT_L(0); BAR;
            if (s == 0) { if (!__all(seen == tag)) flag_wait(kvflag, tag); }
            if (s + 2 < 8) AT_ISSUE(s + 2, fre);
            if (s < 4) {
                const int r0 = (idx * 8 + wid) * 10 + (s < 3 ? s * 4 : 0), rl = s == 2 ? ((lane >> 4) & 1) : (lane >> 4);
                const char* src = (kh ? (const char*)w2 + 1024 * (DM * 2) : r0 >= 1536 ? (const char*)wo - 1536 * (DM * 2) : (const char*)w2) + (size_t)r0 * (DM * 2) + (xcd >> 2) * 1024;
                touch<-AT_BASE>(src, rl * (DM * 2) + (lane & 15) * 64, sdst);
            }
            if (s == 5) { if (wid == 0 && lane == 0) flag_st(flags + F_H + rt * 32 + ct, tag); }
            if (s == 6) seen = flag_ld(hflag);
            if (s == 7 && wid >= 4) {
                if (!__all(seen == tag)) flag_wait(hflag, tag);
                G2_OFFS(wid - 4, lane);
                g128_issue<false>(lds0, g2, 0, wid - 4, vA, vB); g128_issue<false>(lds0, g2, 1, wid - 4, vA, vB);
            }
            if (wid >= 4) __builtin_amdgcn_s_setprio(1);
            const LAS char* kc = kb0 + cur; const LAS char* vc = vb0 + cur; const LAS char* kn = kb0 + nxt; const LAS char* vn = vb0 + nxt;
            AT_STEP(SA, SB, kc + 2 * 2048, vc + 1024, vc + 1 * 2048);            AT_STEP(SB, SA, kc + 3 * 2048, vc + 1 * 2048 + 1024, vc + 2 * 2048);
            H_MAIN(0);
            AT_STEP(SA, SB, kc + 4 * 2048, vc + 2 * 2048 + 1024, vc + 3 * 2048); AT_STEP(SB, SA, kc + 5 * 2048, vc + 3 * 2048 + 1024, vc + 4 * 2048);
            H_EXT();
            AT_STEP(SA, SB, kc + 6 * 2048, vc + 4 * 2048 + 1024, vc + 5 * 2048); __builtin_amdgcn_s_setprio(0); AT_STEP(SB, SA, kc + 7 * 2048, vc + 5 * 2048 + 1024, vc + 6 * 2048);
            H_MAIN(1);
            AT_STEP(SA, SB, kn, vc + 6 * 2048 + 1024, vc + 7 * 2048);            AT_STEP(SB, SA, kn + 2048, vc + 7 * 2048 + 1024, vn);
            const int tmp = cur; cur = nxt; nxt = fre; fre = tmp;
        }
        O = MFMA32(v1, BC8(p1), O); L = MFMA16(sel, BC8(p1), L);
        float lrow; { const float a = __shfl(L[0], lane & 15), b = __shfl(L[1], lane & 15); lrow = (lane & 16) ? b : a; }
        float oabs = 0.f;
#pragma unroll
        for (int i = 0; i < 16; ++i) oabs += fabsf(O[i]);
        if (__any(!(lrow < 1.0e30f) || !(oabs < 3.0e38f))) {
            __builtin_amdgcn_fence(__ATOMIC_ACQUIRE, "agent");
            float m = -INFINITY, l = 0.f;
#pragma unroll
            for (int i = 0; i < 16; ++i) { O[i] = 0.f; negm[i] = 0.f; }
            const u16* Vs = (const u16*)Vg;
            for (int t = 0; t < 64; ++t) { const int key0 = t * 32;
                k0 = *(const bf16x8*)(Kg + key0 * 64 + lane * 16); k1 = *(const bf16x8*)(Kg + key0 * 64 + 1024 + lane * 16);
#pragma unroll
                for (int j = 0; j < 8; ++j) { const int kk = key0 + 8 * (j >> 2) + 4 * hi + (j & 3); v0[j] = (short)Vs[kk * 32 + (lane & 31)]; v1[j] = (short)Vs[(kk + 16) * 32 + (lane & 31)]; }
                f32x16 S = MFMA32(k0, q0, negm); S = MFMA32(k1, q1, S);
                float mt = S[0];
#pragma unroll
                for (int i = 1; i < 16; ++i) mt = fmaxf(mt, S[i]);
                mt = fmaxf(mt, __shfl_xor(mt, 32));
                const float mn = fmaxf(m, mt), al = __builtin_amdgcn_exp2f(m - mn); m = mn;
                float ps = 0.f;
#pragma unroll
                for (int i = 0; i < 16; ++i) { S[i] = rbf(__builtin_amdgcn_exp2f(S[i] - mn)); ps += S[i]; O[i] *= al; }
                l = l * al + ps + __shfl_xor(ps, 32);
#pragma unroll
                for (int i = 0; i < 4; ++i) { p0[i] = cvtpk(S[2 * i], S[2 * i + 1]); p1[i] = cvtpk(S[8 + 2 * i], S[9 + 2 * i]); }
                O = MFMA32(v0, BC8(p0), O); O = MFMA32(v1, BC8(p1), O);
            }
            lrow = l;
        }
        {
            const float inv = 1.0f / lrow;
            unsigned pk[8];
#pragma unroll
            for (int i = 0; i < 8; ++i) pk[i] = cvtpk(O[2 * i] * inv, O[2 * i + 1] * inv);
            const unsigned dst = WS_AH + ((qb * 16 + wid * 2 + ((lane >> 4) & 1)) * KB_AH + head) * 1024 + (lane & 15) * 16 + hi * 256;
#pragma unroll
            for (int c = 0; c < 2; ++c) {
                auto r0 = __builtin_amdgcn_permlane32_swap(pk[4 * c + 0], pk[4 * c + 2], false, false);
                auto r1 = __builtin_amdgcn_permlane32_swap(pk[4 * c + 1], pk[4 * c + 3], false, false);
                ST16(((u32x4){r0[0], r1[0], r0[1], r1[1]}), dst + c * 512);
            }
        }
        if (wid >= 4) { WAIT_V(2); }
        WAIT_L(0); BAR;
#undef AT_ISSUE
#undef AT_STEP
#undef H_MAIN
#undef H_EXT
#undef EX
#undef BC8
    }

    {
        const int tid = opaque_tid(), lane = tid & 63, tile = rt2 * 8 + ct2;
        GAS u64* myflag = flags + F_G2 + tile * 2 + kh; const GAS u64* pflag = flags + F_G2 + tile * 2 + (kh ^ 1);
        if (wid >= 4) {
            const int lw = wid - 4;
            G2_OFFS(lw, lane);
            const unsigned vH = ((lw >> 1) * 4 + 2 * kh + (lw & 1)) * KB_AH * 1024 + lane * 16;
            g128_issue<false>(lds0, g2, 2, lw, vA, vB); g128_issue<false>(lds0, g2, 3, lw, vA, vB);
            WAIT_V(16);
            for (int t = 0; t < 28; ++t) {
                BAR;
                if (t == 0 && wid == 4 && lane == 0) flag_st(flags + F_AT + (idx & 7) * 32 + xcd * 4 + (idx >> 3), tag);
                if (t == 12 && kh == 0) flag_wait(flags + F_AT + (rt2 >> 1) * 32 + (lane & 31), tag);
                g128_issue<false>(lds0, g2, t + 4, lw, vA, vB);
                WAIT_V(16);
            }
            BAR; g128_issue<true>(lds0, g2, 32, lw, vH, vB); WAIT_V(14);
            for (int t = 29; t < 35; ++t) {
                BAR;
                if (t == 34 && wid == 4 && lane == 0) flag_st(myflag, tag);
                g128_issue<true>(lds0, g2, t + 4, lw, vH, vB); WAIT_V(12);
            }
            BAR; g128_issue<true>(lds0, g2, 39, lw, vH, vB);
            const u64 seen = flag_ld(pflag);
            WAIT_V(13);
            BAR; WAIT_V(7);
            BAR; WAIT_V(0);
            const char* psrc = ws + WS_PART + tile * 32768 + (kh ^ 1) * 16384 + lw * 4096;
            const bool early = __all(seen == tag);
            if (early) { glds_pair_sc1<131072>(psrc, lane * 16, lds0 + lw * 4096); glds_pair_sc1<131072 + 2048>(psrc + 2048, lane * 16, lds0 + lw * 4096); }
            BAR;
            if (!early) { flag_wait(pflag, tag); glds_pair_sc1<131072>(psrc, lane * 16, lds0 + lw * 4096); glds_pair_sc1<131072 + 2048>(psrc + 2048, lane * 16, lds0 + lw * 4096); }
            WAIT_V(0); BAR;
            if (bid == 0 && wid == 4) {
#pragma unroll
                for (int i = 0; i < 4; ++i) flag_wait(flags + F_LN + i * 64 + lane, tag);
                if (lane == 0) flag_st(flags, tag);
            }
        } else {
            const int wm = wid >> 1, wn = wid & 1, g = lane >> 4, q = (lane & 15) >> 2, p = lane & 3, swz = (2 * g + (q >> 1)) & 3;
            const int aoff = wm * 8192 + lane * 16;
            const int boff0 = 16384 + (4 * g + q) * 128 + (((2 * wn) ^ swz) * 32) + p * 8, boff1 = boff0 ^ 32;
            f32x4 acc[4][4]; bf16x8 AX[4], AY[4];
            s16x4 XL[4], XH[4], YL[4], YH[4];
            int pa[2] = {aoff, aoff + 65536}, pb0[2] = {boff0, boff0 + 65536}, pb1[2] = {boff1, boff1 + 65536};
            PIN(pa[1]); PIN(pb0[1]); PIN(pb1[1]);
#define G_BP(n, sl, kk) (lds + (((n) & 1) ? pb1[(sl) >> 1] : pb0[(sl) >> 1]) + ((sl) & 1) * 32768 + ((n) >> 1) * 8192 + (kk) * 4096)
#define G_BL(L_, n, sl, kk) do { L_[n] = trd(G_BP(n, sl, kk)); SCHED; } while (0)
#define G_BH(H_, n, sl, kk) do { H_[n] = trd(G_BP(n, sl, kk) + 2048); SCHED; } while (0)
#define G_A(A_, m, sl, kk) do { A_[m] = *(const LAS bf16x8*)(lds + pa[(sl) >> 1] + ((sl) & 1) * 32768 + ((m) * 2 + (kk)) * 1024); SCHED; } while (0)
#define G_M(A_, L_, H_, m, n) do { MFMA16_ACC(acc[m][n], cat8(L_[n], H_[n]), A_[m]); SCHED; } while (0)
#define G_REST(AC, CL, CH, AN, NL, NH, sl, kk) do { \
        G_BL(NL, 0, sl, kk); G_M(AC, CL, CH, 0, 2); G_BH(NH, 0, sl, kk); G_M(AC, CL, CH, 0, 3); G_A(AN, 0, sl, kk); G_M(AC, CL, CH, 1, 0); G_BL(NL, 1, sl, kk); G_M(AC, CL, CH, 1, 1); \
        G_BH(NH, 1, sl, kk); G_M(AC, CL, CH, 1, 2); G_BL(NL, 2, sl, kk); G_M(AC, CL, CH, 1, 3); G_BH(NH, 2, sl, kk); G_M(AC, CL, CH, 2, 0); G_BL(NL, 3, sl, kk); G_M(AC, CL, CH, 2, 1); \
        G_BH(NH, 3, sl, kk); G_M(AC, CL, CH, 2, 2); G_A(AN, 1, sl, kk); G_M(AC, CL, CH, 2, 3); G_A(AN, 2, sl, kk); G_M(AC, CL, CH, 3, 0); G_A(AN, 3, sl, kk); G_M(AC, CL, CH, 3, 1); \
        G_M(AC, CL, CH, 3, 2); G_M(AC, CL, CH, 3, 3); } while (0)
#define G_TILE(sl) do { G_M(AX, XL, XH, 0, 0); G_M(AX, XL, XH, 0, 1); G_REST(AX, XL, XH, AY, YL, YH, sl, 1); \
        G_M(AY, YL, YH, 0, 0); G_M(AY, YL, YH, 0, 1); \
        asm volatile("s_waitcnt vmcnt(0) lgkmcnt(0)" ::: "memory"); BAR; SCHED;        \
        G_REST(AY, YL, YH, AX, XL, XH, ((sl) + 1) & 3, 0); } while (0)
#pragma unroll
            for (int m = 0; m < 4; ++m)
#pragma unroll
                for (int n = 0; n < 4; ++n) { acc[m][n] = (f32x4){0.f, 0.f, 0.f, 0.f}; PIN(acc[m][n]); }
            SCHED;
#pragma unroll
            for (int i = 0; i < 4; ++i) { G_BL(XL, i, 0, 0); G_BH(XH, i, 0, 0); G_A(AX, i, 0, 0); }
            for (int t = 0; t < 32; t += 4) { G_TILE(0); G_TILE(1); G_TILE(2); G_TILE(3); }
            asm volatile("s_nop 7"); SCHED;
            const int fr = lane & 15, fq = lane >> 4;
            const size_t rowo = (size_t)(rt2 * 128 + wm * 64 + kh * 32 + fr) * DM + ct2 * 128 + wn * 32 + (fq & 1) * 16 + (fq >> 1) * 8;
            u32x4 xv[2][2];
#pragma unroll
            for (int m = 0; m < 2; ++m)
#pragma unroll
                for (int s = 0; s < 2; ++s) ST16(pack8(acc[2 + m][2 * s], acc[2 + m][2 * s + 1]), WS_PART + tile * 32768 + kh * 16384 + (m * 2 + s) * 4096 + tid * 16);
#pragma unroll
            for (int m = 0; m < 2; ++m)
#pragma unroll
                for (int s = 0; s < 2; ++s) xv[m][s] = *(const u32x4*)(x + rowo + (size_t)m * 16 * DM + s * 64);
            SCHED;
#define H_REST(AC, CL, CH, AN, NL, NH, sl, kk) do { \
        G_BL(NL, 0, sl, kk); G_BH(NH, 0, sl, kk); G_M(AC, CL, CH, 0, 2); G_A(AN, 0, sl, kk); G_BL(NL, 1, sl, kk); G_M(AC, CL, CH, 0, 3); G_BH(NH, 1, sl, kk); G_BL(NL, 2, sl, kk); G_M(AC, CL, CH, 1, 0); \
        G_BH(NH, 2, sl, kk); G_BL(NL, 3, sl, kk); G_M(AC, CL, CH, 1, 1); G_BH(NH, 3, sl, kk); G_M(AC, CL, CH, 1, 2); G_A(AN, 1, sl, kk); G_M(AC, CL, CH, 1, 3); } while (0)
#define H_TILE(sl, SYNC) do { G_M(AX, XL, XH, 0, 0); G_M(AX, XL, XH, 0, 1); H_REST(AX, XL, XH, AY, YL, YH, sl, 1); \
        G_M(AY, YL, YH, 0, 0); G_M(AY, YL, YH, 0, 1); SYNC; SCHED; H_REST(AY, YL, YH, AX, XL, XH, ((sl) + 1) & 3, 0); } while (0)
#define H_BAR do { WAIT_L(0); BAR; } while (0)
            H_TILE(0, H_BAR); H_TILE(1, H_BAR); H_TILE(2, WAIT_V(0); H_BAR); H_TILE(3, H_BAR);
            H_TILE(0, H_BAR); H_TILE(1, H_BAR); H_TILE(2, H_BAR); H_TILE(3, );
#undef G_BP
#undef G_BL
#undef G_BH
#undef G_A
#undef G_M
#undef G_REST
#undef G_TILE
#undef H_REST
#undef H_TILE
#undef H_BAR
            asm volatile("s_nop 7"); SCHED;
            BAR; CBAR;
            u32x4 pv[2][2];
#pragma unroll
            for (int m = 0; m < 2; ++m)
#pragma unroll
                for (int s = 0; s < 2; ++s) pv[m][s] = *(const LAS u32x4*)(lds + 131072 + (m * 2 + s) * 4096 + tid * 16);
#pragma unroll
            for (int m = 0; m < 2; ++m)
#pragma unroll
                for (int s = 0; s < 2; ++s) { float lo[4], hi4[4]; const u32x4 p_ = pv[m][s], x_ = xv[m][s];
                    const f32x4 a0 = acc[m][2 * s] + (f32x4){bf_lo(p_[0]), bf_hi(p_[0]), bf_lo(p_[1]), bf_hi(p_[1])}, a1 = acc[m][2 * s + 1] + (f32x4){bf_lo(p_[2]), bf_hi(p_[2]), bf_lo(p_[3]), bf_hi(p_[3])};
#pragma unroll
                    for (int i = 0; i < 4; ++i) { auto r = __builtin_amdgcn_permlane16_swap(__float_as_uint(a0[i]), __float_as_uint(a1[i]), false, false);
                        lo[i] = __uint_as_float(r[0]); hi4[i] = __uint_as_float(r[1]); }
                    u32x4 o; o[0] = cvtpk(lo[0] + bf_lo(x_[0]), lo[1] + bf_hi(x_[0])); o[1] = cvtpk(lo[2] + bf_lo(x_[1]), lo[3] + bf_hi(x_[1]));
                    o[2] = cvtpk(hi4[0] + bf_lo(x_[2]), hi4[1] + bf_hi(x_[2])); o[3] = cvtpk(hi4[2] + bf_lo(x_[3]), hi4[3] + bf_hi(x_[3]));
                    __builtin_nontemporal_store(o, (u32x4*)(out + rowo + (size_t)m * 16 * DM + s * 64));
                }
        }
    }
}

extern "C" void kernel_launch(void* const* d_in, const int* in_sizes, int n_in, void* d_out, int out_size, void* d_ws, size_t ws_size, hipStream_t stream) {
    (void)hipFuncSetAttribute((const void*)k_block, hipFuncAttributeMaxDynamicSharedMemorySize, LDS_BYTES);
    k_block<<<256, 512, LDS_BYTES, stream>>>((const u16*)d_in[0], (const u16*)d_in[1], (const u16*)d_in[2], (const u16*)d_in[4], (const u16*)d_in[5], (const u16*)d_in[6], (char*)d_ws, (u16*)d_out);
}
```

```cpp
#include <hip/hip_runtime.h>
#include <hip/hip_bf16.h>
#include <stdint.h>

#define LAS __attribute__((address_space(3)))
#define GAS __attribute__((address_space(1)))
#define DI __device__ __forceinline__
typedef unsigned short u16;
typedef unsigned long long u64;
typedef short bf16x8 __attribute__((ext_vector_type(8)));
typedef short s16x4 __attribute__((ext_vector_type(4)));
typedef float f32x2 __attribute__((ext_vector_type(2)));
typedef float f32x4 __attribute__((ext_vector_type(4)));
typedef float f32x16 __attribute__((ext_vector_type(16)));
typedef unsigned u32x2 __attribute__((ext_vector_type(2)));
typedef unsigned u32x4 __attribute__((ext_vector_type(4)));
typedef __bf16 bf16x2_t __attribute__((ext_vector_type(2)));

constexpr int SEQ = 2048, DM = 1024, FF = 3584;
constexpr int KB_X = DM / 32;
constexpr int KB_AH = (DM + FF) / 32;
constexpr unsigned WS_XN = 0;
constexpr unsigned WS_Q = WS_XN + SEQ * DM * 2;
constexpr unsigned WS_K = WS_Q + SEQ * DM * 2;
constexpr unsigned WS_V = WS_K + SEQ * DM * 2;
constexpr unsigned WS_AH = WS_V + SEQ * DM * 2;
constexpr unsigned WS_PART = WS_AH + SEQ * (DM + FF) * 2;
constexpr unsigned WS_FLAG = WS_PART + 128 * 32768;
constexpr unsigned WS_END = WS_FLAG + 2048 * 128;
constexpr int F_LN = 32, F_QKV = F_LN + 256, F_H = F_QKV + 256, F_AT = F_H + 256, F_G2 = F_AT + 256;
constexpr int LDS_BYTES = 163840;

DI unsigned cvtpk(float lo, float hi) { f32x2 v = {lo, hi}; bf16x2_t b = __builtin_convertvector(v, bf16x2_t); return __builtin_bit_cast(unsigned, b); }
DI float bf_lo(unsigned u) { return __uint_as_float(u << 16); }
DI float bf_hi(unsigned u) { return __uint_as_float(u & 0xffff0000u); }
DI float rbf(float f) { return __uint_as_float(cvtpk(f, f) << 16); }
DI s16x4 trd(const LAS char* p) { return __builtin_amdgcn_ds_read_tr16_b64_v4i16((LAS s16x4*)p); }
DI bf16x8 cat8(s16x4 a, s16x4 b) { return __builtin_shufflevector(a, b, 0, 1, 2, 3, 4, 5, 6, 7); }
template <int OFF, int D> DI void glds(const char* base, unsigned voff, unsigned dst) {
    asm volatile("s_add_u32 m0, %2, %4\n\ts_nop 0\n\tglobal_load_lds_dwordx4 %0, %1 offset:%3" :: "v"(voff), "s"(base), "s"(dst), "i"(OFF), "i"(D - OFF) : "memory", "scc"); }
template <int D> DI void glds_pair_sc1(const char* base, unsigned voff, unsigned dst) {
    asm volatile("s_add_u32 m0, %2, %3\n\ts_nop 0\n\tglobal_load_lds_dwordx4 %0, %1 sc1\n\tglobal_load_lds_dwordx4 %0, %1 offset:1024 sc1" :: "v"(voff), "s"(base), "s"(dst), "i"(D) : "memory", "scc"); }
template <int D> DI void touch(const char* base, unsigned voff, unsigned dst) {
    asm volatile("s_add_u32 m0, %2, %3\n\ts_nop 0\n\tglobal_load_lds_dword %0, %1" :: "v"(voff), "s"(base), "s"(dst), "i"(D) : "memory", "scc"); }
#define WAIT_V(n) asm volatile("s_waitcnt vmcnt(" #n ")" ::: "memory")
#define WAIT_L(n) asm volatile("s_waitcnt lgkmcnt(" #n ")" ::: "memory")
#define BAR __builtin_amdgcn_s_barrier()
#define CBAR asm volatile("" ::: "memory")
#define SCHED __builtin_amdgcn_sched_barrier(0)
#define PIN(x) asm volatile("" : "+v"(x))
#define MFMA16(a, b, c) __builtin_amdgcn_mfma_f32_16x16x32_bf16(a, b, c, 0, 0, 0)
#define MFMA16_ACC(c, a, b) asm volatile("v_mfma_f32_16x16x32_bf16 %0, %1, %2, %0" : "+v"(c) : "v"(a), "v"(b))
#define MFMA32(a, b, c) __builtin_amdgcn_mfma_f32_32x32x16_bf16(a, b, c, 0, 0, 0)
#define ST16(v, off) __builtin_amdgcn_raw_buffer_store_b128(v, rs, (int)(off), 0, 16)
#define ST8(v, off) __builtin_amdgcn_raw_buffer_store_b64(v, rs, (int)(off), 0, 16)
#define LD16(off) __builtin_amdgcn_raw_buffer_load_b128(rs, (int)(off), 0, 16)
DI u64 flag_ld(const GAS u64* p) { return __hip_atomic_load(p, __ATOMIC_RELAXED, __HIP_MEMORY_SCOPE_AGENT); }
DI void flag_st(GAS u64* p, u64 v) { __hip_atomic_store(p, v, __ATOMIC_RELAXED, __HIP_MEMORY_SCOPE_AGENT); }
DI void flag_wait(const GAS u64* p, u64 tag) {
    for (unsigned spins = 0; spins < (1u << 18); ++spins) { if (__all(flag_ld(p) == tag)) break; __builtin_amdgcn_s_sleep(1); }
    CBAR;
}
template <int N> DI float ror_add(float v) { return v + __builtin_bit_cast(float, __builtin_amdgcn_update_dpp(0, __builtin_bit_cast(int, v), 0x120 + N  , 0xf, 0xf, false)); }
DI float sum64(float v) {
    v = ror_add<1>(ror_add<2>(ror_add<4>(ror_add<8>(v))));
    { auto r = __builtin_amdgcn_permlane16_swap(__float_as_uint(v), __float_as_uint(v), false, false); v = __uint_as_float(r[0]) + __uint_as_float(r[1]); }
    { auto r = __builtin_amdgcn_permlane32_swap(__float_as_uint(v), __float_as_uint(v), false, false); v = __uint_as_float(r[0]) + __uint_as_float(r[1]); }
    return v;
}
DI int opaque_tid() { int t = threadIdx.x; asm volatile("" : "+v"(t)); return t; }

DI float silu_mul(float g, float u) { return g * u * __builtin_amdgcn_rcpf(1.0f + __builtin_amdgcn_exp2f(g * -1.4426950408889634f)); }
DI u32x4 pack8(const f32x4& a, const f32x4& b) { u32x4 o; o[0] = cvtpk(a[0], a[1]); o[1] = cvtpk(a[2], a[3]); o[2] = cvtpk(b[0], b[1]); o[3] = cvtpk(b[2], b[3]); return o; }
constexpr float QSCALE = 0.17677669529663687f * 1.4426950408889634f;


struct G128 { const char* a; const char* w2; const char* wo; int kh; };
template <bool HALF> DI void g128_issue(unsigned lds0, const G128& g, int t, int lw, unsigned voffA, unsigned voffB) {
    const unsigned so = lds0 + (t & 3) * 32768, sb = so + lw * 2048;
    const bool att = !HALF && g.kh == 0 && t >= 16;
    const int kt = HALF ? t - 16 : att ? t - 16 : g.kh * 24 + t;
    const char* a = g.a + (size_t)(att ? kt : 16 + kt) * 2048; const char* b = (att ? g.wo : g.w2) + (size_t)kt * (64 * DM * 2);
    if (HALF) glds_pair_sc1<0>(a, voffA, so + (lw >> 1) * 8192 + (lw & 1) * 2048);
    else { glds_pair_sc1<0>(a, voffA, so + lw * 4096); glds_pair_sc1<2048>(a + KB_AH * 1024, voffA, so + lw * 4096); }
    glds<0, 16384>(b, voffB, sb); glds<128, 24576>(b, voffB, sb);
    glds<0, 16384 + 1024>(b + 8 * DM * 2, voffB, sb); glds<128, 24576 + 1024>(b + 8 * DM * 2, voffB, sb);
}

__global__ __launch_bounds__(512, 2) void k_block(const u16* __restrict__ x, const u16* __restrict__ wqkv, const u16* __restrict__ wo, const u16* __restrict__ w1,
                                                  const u16* __restrict__ w3, const u16* __restrict__ w2, char* __restrict__ ws, u16* __restrict__ out) {
    extern __shared__ __attribute__((aligned(16))) char smem[];
    LAS char* lds = (LAS char*)smem;
    const unsigned lds0 = (unsigned)(size_t)lds;
    const int wid = __builtin_amdgcn_readfirstlane(threadIdx.x >> 6);
    const int bid = blockIdx.x, xcd = bid & 7, idx = bid >> 3;
    GAS u64* flags0 = (GAS u64*)(ws + WS_FLAG);
#define FL(i) (flags0 + (i) * 16)
    const __amdgpu_buffer_rsrc_t rs = __builtin_amdgcn_make_buffer_rsrc((void*)ws, 0, WS_END, 0x00020000);
    {
        const char* pc; asm volatile("s_getpc_b64 %0" : "=s"(pc));
        if (wid == 7) touch<148480>(pc + (idx & 7) * 4096, (threadIdx.x & 63) * 64, lds0 + 7 * 1024);
    }
    u64 tag;
    u32x4 xa, xb; u32x2 ep;
    { const char* xp = (const char*)x + (size_t)(bid * 8 + wid) * (DM * 2);
      asm volatile("global_load_dwordx4 %0, %3, %4\n\tglobal_load_dwordx4 %1, %3, %4 offset:1024\n\tglobal_load_dwordx2 %2, %5, %6 sc1"
                   : "=&v"(xa), "=&v"(xb), "=&v"(ep) : "v"((threadIdx.x & 63) * 16), "s"(xp), "v"(0), "s"(ws + WS_FLAG), "s"(wqkv), "s"(w1), "s"(w3) : "memory"); }
    const int cj = idx >> 2, ct = cj < 3 ? (xcd >> 1) * 3 + cj : 12 + (xcd >> 1) * 5 + (cj - 3), rt = (xcd & 1) * 4 + (idx & 3);
    const bool qkv = ct < 12;
    u32x4 hmain[8]; u32x2 hext[4];

    {
        const int tid = opaque_tid(), lane = tid & 63, wr = wid >> 2, wc = wid & 3;
        const unsigned ldb2 = qkv ? 3 * DM * 2 : FF * 2;
        const char* cB0 = qkv ? (const char*)wqkv + ct * 512 : (const char*)w1 + (ct - 12) * 256;
        const char* cB1 = qkv ? cB0 + 256 : (const char*)w3 + (ct - 12) * 256;
        const char* cA = ws + WS_XN + rt * 16 * KB_X * 1024;
        const unsigned voffA = wid * (KB_X * 1024) + lane * 16;
        const unsigned voffB = (unsigned)(wid * 8 + (lane >> 3)) * ldb2 + (((lane & 7) ^ (2 * ((lane >> 4) & 3))) * 16);
        const char* pE = ((lane & 1) ? (const char*)w3 : (const char*)w1) + (size_t)(wid * 8 + (lane >> 3)) * (FF * 2) + (2560 + ct * 32) * 2 + ((((lane >> 1) & 3) ^ ((lane >> 4) & 3)) * 16);
        const size_t kstepB = (size_t)64 * ldb2;
        const unsigned ldsA = lds0 + wid * 2048, ldsB = lds0 + wid * 1024;
        const char* cT = ((idx & 2) ? cB1 : cB0) + (idx & 1) * 128;
#define SA(b, h) (((b) * 2 + (h)) * 16384)
#define SB(b, h) (((b) * 2 + (h)) * 16384)
#define STA(b, h, kt) glds_pair_sc1<SA(b, h)>(cA + (h) * 8 * KB_X * 1024 + (kt) * 2048, voffA, ldsA)
#define STB(b, h, kt) do { const char* s_ = ((h) ? cB1 : cB0) + (size_t)(kt) * kstepB; \
        glds<0, 65536 + SB(b, h)>(s_, voffB, ldsB); glds<128, 65536 + SB(b, h) + 8192>(s_, voffB, ldsB); } while (0)
#define TCH(kt) touch<148480>(cT + (size_t)(kt) * kstepB, voffB, ldsB)
#define STE(b, kt) do { const char* s_ = pE + (size_t)(kt) * (64 * FF * 2); \
        asm volatile("s_add_u32 m0, %1, %2\n\ts_nop 0\n\tglobal_load_lds_dwordx4 %0, off" :: "v"(s_), "s"(ldsB), "i"(131072 + (b) * 8192) : "memory", "scc"); } while (0)
        {
            TCH(0); TCH(1);
            { const char* s_ = pE + (size_t)(idx & 1) * (64 * FF * 2) + (ptrdiff_t)(((idx >> 1) & 1) - (wid >> 2)) * (32 * FF * 2);
              asm volatile("s_add_u32 m0, %1, 148480\n\ts_nop 0\n\tglobal_load_lds_dword %0, off" :: "v"(s_), "s"(ldsB) : "memory", "scc"); }
            asm volatile("s_waitcnt vmcnt(3)" : "+v"(xa), "+v"(xb), "+v"(ep) :: "memory");
            tag = (((u64)(unsigned)__builtin_amdgcn_readfirstlane(ep[1]) << 32) | (unsigned)__builtin_amdgcn_readfirstlane(ep[0])) + 1;
            float f[16];
#pragma unroll
            for (int i = 0; i < 4; ++i) { f[2 * i] = bf_lo(xa[i]); f[2 * i + 1] = bf_hi(xa[i]); f[8 + 2 * i] = bf_lo(xb[i]); f[9 + 2 * i] = bf_hi(xb[i]); }
            float s = 0.f;
#pragma unroll
            for (int i = 0; i < 16; ++i) s += f[i];
            const float mean = sum64(s) * (1.0f / DM);
            float q = 0.f;
#pragma unroll
            for (int i = 0; i < 16; ++i) { const float d = f[i] - mean; q += d * d; }
            const float var = sum64(q) * (1.0f / DM);
            const float mb = rbf(mean), sd = rbf(__builtin_amdgcn_sqrtf(rbf(rbf(var) + 1e-5f))), inv = __builtin_amdgcn_rcpf(sd);
#pragma unroll
            for (int c = 0; c < 2; ++c) { u32x4 o;
#pragma unroll
                for (int i = 0; i < 4; ++i) o[i] = cvtpk(rbf(f[c * 8 + 2 * i] - mb) * inv, rbf(f[c * 8 + 2 * i + 1] - mb) * inv);
                *(LAS u32x4*)(lds + wid * 2080 + c * 1024 + lane * 16) = o; }
            WAIT_L(0); BAR; CBAR;
            const int r = lane & 7, g = (lane >> 3) & 3, row = bid * 8 + r;
#pragma unroll
            for (int c = 0; c < 2; ++c) { const int kb = 4 * wid + 2 * c + (lane >> 5); const LAS char* p = lds + r * 2080 + kb * 64 + g * 8;
                const u32x2 lo = *(const LAS u32x2*)p, hi = *(const LAS u32x2*)(p + 32);
                ST16(((u32x4){lo[0], lo[1], hi[0], hi[1]}), WS_XN + ((row >> 4) * KB_X + kb) * 1024 + (g * 16 + (row & 15)) * 16); }
            CBAR;
            STB(0, 0, 0); STB(0, 1, 0); STE(0, 0);
            WAIT_V(5); BAR;
            if (wid == 0) { if (lane == 0) flag_st(FL(F_LN + bid), tag); flag_wait(FL(F_LN + rt * 32 + (lane & 31)), tag); }
            STB(1, 0, 1); STB(1, 1, 1); STE(1, 1);
            if (wid != 0) { TCH(2); TCH(3); }
            BAR; CBAR;
        }
        f32x4 acc[2][2][4][2], ace[2][4];
        {
            const int g = lane >> 4, q = (lane & 15) >> 2, p = lane & 3, swz = (2 * g + (q >> 1)) & 3;
            const int aoff = wr * 8192 + lane * 16;
            const int boff0 = 65536 + (wc >> 1) * 8192 + (4 * g + q) * 128 + (((2 * (wc & 1)) ^ swz) * 32) + p * 8, boff1 = boff0 ^ 32;
            int eoff = 131072 + (4 * g + q) * 128 + ((wc ^ swz) * 32) + p * 8; PIN(eoff);
#define LDA(dst, b, h) do { _Pragma("unroll") for (int m = 0; m < 4; ++m) _Pragma("unroll") for (int k = 0; k < 2; ++k) \
        dst[m][k] = *(const LAS bf16x8*)(lds + aoff + SA(b, h) + (m * 2 + k) * 1024); } while (0)
#define LDB(dst, b, h) do { _Pragma("unroll") for (int n = 0; n < 2; ++n) _Pragma("unroll") for (int k = 0; k < 2; ++k) { \
        const LAS char* b_ = lds + (n ? boff1 : boff0) + SB(b, h) + k * 4096; dst[n][k] = cat8(trd(b_), trd(b_ + 2048)); } } while (0)
#define LDE(dst, b) do { _Pragma("unroll") for (int k = 0; k < 2; ++k) { const LAS char* b_ = lds + eoff + (b) * 8192 + k * 4096; dst[k] = cat8(trd(b_), trd(b_ + 2048)); } } while (0)
#define MMA(ai) do { __builtin_amdgcn_s_setprio(1); \
        _Pragma("unroll") for (int k = 0; k < 2; ++k) _Pragma("unroll") for (int m = 0; m < 4; ++m) { \
            MFMA16_ACC(acc[ai][0][m][0], B0[0][k], At[m][k]); MFMA16_ACC(acc[ai][0][m][1], B0[1][k], At[m][k]); \
            MFMA16_ACC(acc[ai][1][m][0], B1[0][k], At[m][k]); MFMA16_ACC(acc[ai][1][m][1], B1[1][k], At[m][k]); \
            MFMA16_ACC(ace[ai][m], BE[k], At[m][k]); } \
        __builtin_amdgcn_s_setprio(0); } while (0)
            STA(0, 0, 0); STA(0, 1, 0); STA(1, 0, 1);
#pragma unroll
            for (int a = 0; a < 2; ++a)
#pragma unroll
                for (int m = 0; m < 4; ++m) { ace[a][m] = (f32x4){0.f, 0.f, 0.f, 0.f}; PIN(ace[a][m]);
#pragma unroll
                    for (int b = 0; b < 2; ++b)
#pragma unroll
                        for (int n = 0; n < 2; ++n) { acc[a][b][m][n] = (f32x4){0.f, 0.f, 0.f, 0.f}; PIN(acc[a][b][m][n]); } }
            bf16x8 At[4][2], B0[2][2], B1[2][2], BE[2];
            constexpr int nt = DM / 64;
            if (wr == 1) BAR;
            WAIT_V(4); BAR;
            WAIT_V(2); BAR;
            for (int t = 0; t < nt; t += 2) {
                const bool more = t + 2 < nt, far = t + 4 < nt;
                LDB(B0, 0, 0); LDB(B1, 0, 1); LDE(BE, 0); SCHED; LDA(At, 0, 0); STA(1, 1, t + 1);
                if (far) { TCH(t + 4); WAIT_V(11); } else { WAIT_V(9); }
                WAIT_L(0); BAR; MMA(0); BAR; SCHED;
                LDA(At, 0, 1);
                if (more) { STB(0, 0, t + 2); STB(0, 1, t + 2); STE(0, t + 2); STA(0, 0, t + 2); if (far) { WAIT_V(10); } else { WAIT_V(9); } } else { WAIT_V(2); }
                WAIT_L(0); BAR; MMA(1); BAR; SCHED;
                LDB(B0, 1, 0); LDB(B1, 1, 1); LDE(BE, 1); SCHED; LDA(At, 1, 0);
                if (more) { STA(0, 1, t + 2); if (far) { TCH(t + 5); WAIT_V(11); } else { WAIT_V(9); } } else { WAIT_V(0); }
                WAIT_L(0); BAR; MMA(0); BAR; SCHED;
                LDA(At, 1, 1);
                if (more) { STB(1, 0, t + 3); STB(1, 1, t + 3); STE(1, t + 3); STA(1, 0, t + 3); if (far) { WAIT_V(10); } else { WAIT_V(9); } }
                WAIT_L(0); BAR; MMA(1); if (more || wr == 0) BAR; SCHED;
            }
            asm volatile("s_nop 7"); SCHED;
#undef SA
#undef SB
#undef STA
#undef STB
#undef STE
#undef TCH
#undef LDA
#undef LDB
#undef LDE
#undef MMA
        }
        const int ln = opaque_tid() & 63, fr = ln & 15, fq = ln >> 4;
        if (qkv) {
            const int kind = ct >> 2;
            const float sc = kind == 0 ? QSCALE : 1.0f;
            const unsigned base = WS_Q + kind * (SEQ * DM * 2) + ((ct & 3) * 8 + wc) * 131072 + (rt * 256 + wr * 64) * 64;
            const unsigned lo0 = base + (kind == 2 ? fr * 64 + fq * 16 : fr * 16 + fq * 512), lo1 = lo0 + (kind == 2 ? 1024 : 256);
#pragma unroll
            for (int bj = 0; bj < 2; ++bj)
#pragma unroll
                for (int ai = 0; ai < 2; ++ai)
#pragma unroll
                    for (int m = 0; m < 4; ++m)
                        ST16(pack8(acc[ai][bj][m][0] * sc, acc[ai][bj][m][1] * sc), bj * 4 * 131072 + ai * 8192 + (m >> 1) * 2048 + ((m & 1) ? lo1 : lo0));
            WAIT_V(0); BAR;
            if (wid == 0 && ln == 0) flag_st(FL(F_QKV + rt * 32 + ct), tag);
        } else {
#pragma unroll
            for (int ai = 0; ai < 2; ++ai)
#pragma unroll
                for (int m = 0; m < 4; ++m) { f32x4 h0, h1;
#pragma unroll
                    for (int j = 0; j < 4; ++j) { h0[j] = silu_mul(acc[ai][0][m][0][j], acc[ai][1][m][0][j]); h1[j] = silu_mul(acc[ai][0][m][1][j], acc[ai][1][m][1][j]); }
                    hmain[ai * 4 + m] = pack8(h0, h1);
                }
        }
#pragma unroll
        for (int ai = 0; ai < 2; ++ai)
#pragma unroll
            for (int mp = 0; mp < 2; ++mp) { float h[4];
#pragma unroll
                for (int j = 0; j < 4; ++j) { auto r = __builtin_amdgcn_permlane32_swap(__float_as_uint(ace[ai][2 * mp][j]), __float_as_uint(ace[ai][2 * mp + 1][j]), false, false);
                    h[j] = silu_mul(__uint_as_float(r[0]), __uint_as_float(r[1])); }
                hext[ai * 2 + mp] = (u32x2){cvtpk(h[0], h[1]), cvtpk(h[2], h[3])};
            }
    }

    const int kh = xcd & 1, rt2 = ((xcd >> 1) & 1) * 8 + (idx & 7), ct2 = (xcd >> 2) * 4 + (idx >> 3);
    G128 g2; g2.kh = kh; g2.a = ws + WS_AH + rt2 * 8 * KB_AH * 1024; g2.w2 = (const char*)w2 + ct2 * 256; g2.wo = (const char*)wo + ct2 * 256;
#define G2_OFFS(lw_, l_) const unsigned vA = ((lw_) ^ kh) * 2 * KB_AH * 1024 + (l_) * 16, vB = ((lw_) * 16 + ((l_) >> 3)) * (DM * 2) + ((((l_) & 7) ^ (2 * (((l_) >> 4) & 3))) * 16)

    {
        constexpr int AT_BASE = 65536, AT_STAGE = 32768;
        const int lane = opaque_tid() & 63, hi = lane >> 5;
        const int head = xcd * 4 + (idx >> 3), qb = idx & 7;
        if (wid == 0) flag_wait(FL(F_QKV + (lane < 4 ? (lane & 1) * 32 + 4 + (lane >> 1) * 4 : qb * 32) + (head >> 3)), tag);
        BAR; CBAR;
        const char* Kg = ws + WS_K + head * 131072; const char* Vg = ws + WS_V + head * 131072;
        const unsigned soff = wid * 2048 + lane * 16;
        const unsigned sdst = lds0 + AT_BASE + wid * 2048;
#define AT_ISSUE(s, slot) do { glds_pair_sc1<0>(Kg + (s) * 16384, soff, sdst + (slot)); glds_pair_sc1<16384>(Vg + (s) * 16384, soff, sdst + (slot)); } while (0)
        const unsigned qoff = WS_Q + (head * 64 + qb * 8 + wid) * 2048 + lane * 16;
        const bf16x8 q0 = __builtin_bit_cast(bf16x8, LD16(qoff)), q1 = __builtin_bit_cast(bf16x8, LD16(qoff + 1024));
        AT_ISSUE(0, 0); AT_ISSUE(1, AT_STAGE);
        const GAS u64* kvflag = FL(F_QKV + (lane & 7) * 32 + 4 + ((lane >> 3) & 1) * 4 + (head >> 3));
        u64 seen = flag_ld(kvflag);
        bf16x8 sel; { const short one = ((lane & 15) == ((lane >> 4) & 1)) ? (short)0x3f80 : (short)0;
#pragma unroll
            for (int i = 0; i < 8; ++i) sel[i] = one; }
        const LAS char* kb0 = lds + AT_BASE + lane * 16;
        const LAS char* vb0 = lds + AT_BASE + 16384 + (4 * hi + ((lane & 15) >> 2)) * 64 + ((lane >> 4) & 1) * 32 + (lane & 3) * 8;
        f32x16 O, negm, SA, SB; f32x4 L = {0.f, 0.f, 0.f, 0.f};
        bf16x8 k0, k1, v0, v1;
#pragma unroll
        for (int i = 0; i < 16; ++i) { O[i] = 0.f; negm[i] = 0.f; }
        WAIT_V(5); BAR;
        {
            k0 = *(const LAS bf16x8*)(kb0); k1 = *(const LAS bf16x8*)(kb0 + 1024);
            SA = MFMA32(k0, q0, negm); SA = MFMA32(k1, q1, SA);
            k0 = *(const LAS bf16x8*)(kb0 + 2048); k1 = *(const LAS bf16x8*)(kb0 + 3072);
            v0 = cat8(trd(vb0), trd(vb0 + 512)); v1 = cat8(trd(vb0 + 1024), trd(vb0 + 1536));
            float m = SA[0];
#pragma unroll
            for (int i = 1; i < 16; ++i) m = fmaxf(m, SA[i]);
            m = fmaxf(m, __shfl_xor(m, 32));
#pragma unroll
            for (int i = 0; i < 16; ++i) { negm[i] = -m; SA[i] -= m; }
            PIN(negm);
        }
#define EX(S, i) S[i] = __builtin_amdgcn_exp2f(S[i])
#define BC8(x) __builtin_bit_cast(bf16x8, x)
#define AT_STEP(SC, SN, kp, v1p, v0p) do { \
        O = MFMA32(v1, BC8(p1), O); SCHED; v1 = cat8(trd(v1p), trd((v1p) + 512)); EX(SC, 0); EX(SC, 1); EX(SC, 2); PIN(SC); SCHED; \
        SN = MFMA32(k0, q0, negm); SCHED; k0 = *(const LAS bf16x8*)(kp); EX(SC, 3); EX(SC, 4); EX(SC, 5); PIN(SC); SCHED; \
        L = MFMA16(sel, BC8(p1), L); SCHED; EX(SC, 6); EX(SC, 7); \
        p0[0] = cvtpk(SC[0], SC[1]); p0[1] = cvtpk(SC[2], SC[3]); p0[2] = cvtpk(SC[4], SC[5]); p0[3] = cvtpk(SC[6], SC[7]); PIN(p0); SCHED; \
        O = MFMA32(v0, BC8(p0), O); SCHED; v0 = cat8(trd(v0p), trd((v0p) + 512)); EX(SC, 8); EX(SC, 9); EX(SC, 10); PIN(SC); SCHED; \
        SN = MFMA32(k1, q1, SN); SCHED; k1 = *(const LAS bf16x8*)((kp) + 1024); EX(SC, 11); EX(SC, 12); EX(SC, 13); PIN(SC); SCHED; \
        L = MFMA16(sel, BC8(p0), L); SCHED; EX(SC, 14); EX(SC, 15); \
        p1[0] = cvtpk(SC[8], SC[9]); p1[1] = cvtpk(SC[10], SC[11]); p1[2] = cvtpk(SC[12], SC[13]); p1[3] = cvtpk(SC[14], SC[15]); PIN(p1); SCHED; } while (0)
        u32x4 p0, p1 = {0u, 0u, 0u, 0u};
        int cur = 0, nxt = AT_STAGE, fre = 2 * AT_STAGE;
        const GAS u64* hflag = FL(F_H + (rt2 >> 1) * 32 + (lane & 31));
        const unsigned hb = WS_AH + ((rt * 16 + (wid >> 2) * 4) * KB_AH + KB_X + (ct - 12) * 4 + (wid & 3)) * 1024 + lane * 16;
        const int cq = 2 * (wid & 3) + ((lane >> 4) & 1);
        const unsigned eb = WS_AH + ((rt * 16 + (wid >> 2) * 4 + hi) * KB_AH + KB_X + 80 + ct) * 1024 + ((cq & 3) * 16 + (lane & 15)) * 16 + (cq >> 2) * 8;
#define H_MAIN(j) do { if (!qkv && s < 4) { if (s == 0) ST16(hmain[j], hb + (j) * KB_AH * 1024); else if (s == 1) ST16(hmain[2 + (j)], hb + (2 + (j)) * KB_AH * 1024); \
            else if (s == 2) ST16(hmain[4 + (j)], hb + (8 + (j)) * KB_AH * 1024); else ST16(hmain[6 + (j)], hb + (10 + (j)) * KB_AH * 1024); } SCHED; } while (0)
#define H_EXT() do { if (s < 4) { if (s == 0) ST8(hext[0], eb); else if (s == 1) ST8(hext[1], eb + 2 * KB_AH * 1024); \
            else if (s == 2) ST8(hext[2], eb + 8 * KB_AH * 1024); else ST8(hext[3], eb + 10 * KB_AH * 1024); } SCHED; } while (0)
        for (int s = 0; s < 8; ++s) {
            if (s >= 1 && s <= 4) { if (qkv) { WAIT_V(2); } else { WAIT_V(4); } } else { WAIT_V(0); }
            WAIT_L(0); BAR;
            if (s == 0) { if (!__all(seen == tag)) flag_wait(kvflag, tag); }
            if (s + 2 < 8) AT_ISSUE(s + 2, fre);
            if (s < 4) {
                const int r0 = (idx * 8 + wid) * 10 + (s < 3 ? s * 4 : 0), rl = s == 2 ? ((lane >> 4) & 1) : (lane >> 4);
                const char* src = (kh ? (const char*)w2 + 1024 * (DM * 2) : r0 >= 1536 ? (const char*)wo - 1536 * (DM * 2) : (const char*)w2) + (size_t)r0 * (DM * 2) + (xcd >> 2) * 1024;
                touch<-AT_BASE>(src, rl * (DM * 2) + (lane & 15) * 64, sdst);
            }
            if (s == 5) { if (wid == 0 && lane == 0) flag_st(FL(F_H + rt * 32 + ct), tag); }
            if (s == 6) seen = flag_ld(hflag);
            if (s == 7 && wid >= 4) {
                if (!__all(seen == tag)) flag_wait(hflag, tag);
                G2_OFFS(wid - 4, lane);
                g128_issue<false>(lds0, g2, 0, wid - 4, vA, vB); g128_issue<false>(lds0, g2, 1, wid - 4, vA, vB);
            }
            if (wid >= 4) __builtin_amdgcn_s_setprio(1);
            const LAS char* kc = kb0 + cur; const LAS char* vc = vb0 + cur; const LAS char* kn = kb0 + nxt; const LAS char* vn = vb0 + nxt;
            AT_STEP(SA, SB, kc + 2 * 2048, vc + 1024, vc + 1 * 2048);            AT_STEP(SB, SA, kc + 3 * 2048, vc + 1 * 2048 + 1024, vc + 2 * 2048);
            H_MAIN(0);
            AT_STEP(SA, SB, kc + 4 * 2048, vc + 2 * 2048 + 1024, vc + 3 * 2048); AT_STEP(SB, SA, kc + 5 * 2048, vc + 3 * 2048 + 1024, vc + 4 * 2048);
            H_EXT();
            AT_STEP(SA, SB, kc + 6 * 2048, vc + 4 * 2048 + 1024, vc + 5 * 2048); __builtin_amdgcn_s_setprio(0); AT_STEP(SB, SA, kc + 7 * 2048, vc + 5 * 2048 + 1024, vc + 6 * 2048);
            H_MAIN(1);
            AT_STEP(SA, SB, kn, vc + 6 * 2048 + 1024, vc + 7 * 2048);            AT_STEP(SB, SA, kn + 2048, vc + 7 * 2048 + 1024, vn);
            const int tmp = cur; cur = nxt; nxt = fre; fre = tmp;
        }
        O = MFMA32(v1, BC8(p1), O); L = MFMA16(sel, BC8(p1), L);
        float lrow; { const float a = __shfl(L[0], lane & 15), b = __shfl(L[1], lane & 15); lrow = (lane & 16) ? b : a; }
        float oabs = 0.f;
#pragma unroll
        for (int i = 0; i < 16; ++i) oabs += fabsf(O[i]);
        if (__any(!(lrow < 1.0e30f) || !(oabs < 3.0e38f))) {
            __builtin_amdgcn_fence(__ATOMIC_ACQUIRE, "agent");
            float m = -INFINITY, l = 0.f;
#pragma unroll
            for (int i = 0; i < 16; ++i) { O[i] = 0.f; negm[i] = 0.f; }
            const u16* Vs = (const u16*)Vg;
            for (int t = 0; t < 64; ++t) { const int key0 = t * 32;
                k0 = *(const bf16x8*)(Kg + key0 * 64 + lane * 16); k1 = *(const bf16x8*)(Kg + key0 * 64 + 1024 + lane * 16);
#pragma unroll
                for (int j = 0; j < 8; ++j) { const int kk = key0 + 8 * (j >> 2) + 4 * hi + (j & 3); v0[j] = (short)Vs[kk * 32 + (lane & 31)]; v1[j] = (short)Vs[(kk + 16) * 32 + (lane & 31)]; }
                f32x16 S = MFMA32(k0, q0, negm); S = MFMA32(k1, q1, S);
                float mt = S[0];
#pragma unroll
                for (int i = 1; i < 16; ++i) mt = fmaxf(mt, S[i]);
                mt = fmaxf(mt, __shfl_xor(mt, 32));
                const float mn = fmaxf(m, mt), al = __builtin_amdgcn_exp2f(m - mn); m = mn;
                float ps = 0.f;
#pragma unroll
                for (int i = 0; i < 16; ++i) { S[i] = rbf(__builtin_amdgcn_exp2f(S[i] - mn)); ps += S[i]; O[i] *= al; }
                l = l * al + ps + __shfl_xor(ps, 32);
#pragma unroll
                for (int i = 0; i < 4; ++i) { p0[i] = cvtpk(S[2 * i], S[2 * i + 1]); p1[i] = cvtpk(S[8 + 2 * i], S[9 + 2 * i]); }
                O = MFMA32(v0, BC8(p0), O); O = MFMA32(v1, BC8(p1), O);
            }
            lrow = l;
        }
        {
            const float inv = 1.0f / lrow;
            unsigned pk[8];
#pragma unroll
            for (int i = 0; i < 8; ++i) pk[i] = cvtpk(O[2 * i] * inv, O[2 * i + 1] * inv);
            const unsigned dst = WS_AH + ((qb * 16 + wid * 2 + ((lane >> 4) & 1)) * KB_AH + head) * 1024 + (lane & 15) * 16 + hi * 256;
#pragma unroll
            for (int c = 0; c < 2; ++c) {
                auto r0 = __builtin_amdgcn_permlane32_swap(pk[4 * c + 0], pk[4 * c + 2], false, false);
                auto r1 = __builtin_amdgcn_permlane32_swap(pk[4 * c + 1], pk[4 * c + 3], false, false);
                ST16(((u32x4){r0[0], r1[0], r0[1], r1[1]}), dst + c * 512);
            }
        }
        if (wid >= 4) { WAIT_V(2); }
        WAIT_L(0); BAR;
#undef AT_ISSUE
#undef AT_STEP
#undef H_MAIN
#undef H_EXT
#undef EX
#undef BC8
    }

    {
        const int tid = opaque_tid(), lane = tid & 63, tile = rt2 * 8 + ct2;
        GAS u64* myflag = FL(F_G2 + tile * 2 + kh); const GAS u64* pflag = FL(F_G2 + tile * 2 + (kh ^ 1));
        if (wid >= 4) {
            const int lw = wid - 4;
            G2_OFFS(lw, lane);
            const unsigned vH = ((lw >> 1) * 4 + 2 * kh + (lw & 1)) * KB_AH * 1024 + lane * 16;
            g128_issue<false>(lds0, g2, 2, lw, vA, vB); g128_issue<false>(lds0, g2, 3, lw, vA, vB);
            WAIT_V(16);
            for (int t = 0; t < 28; ++t) {
                BAR;
                if (t == 0 && wid == 4 && lane == 0) flag_st(FL(F_AT + (idx & 7) * 32 + xcd * 4 + (idx >> 3)), tag);
                if (t == 12 && kh == 0) flag_wait(FL(F_AT + (rt2 >> 1) * 32 + (lane & 31)), tag);
                g128_issue<false>(lds0, g2, t + 4, lw, vA, vB);
                WAIT_V(16);
            }
            BAR; g128_issue<true>(lds0, g2, 32, lw, vH, vB); WAIT_V(14);
            for (int t = 29; t < 35; ++t) {
                BAR;
                if (t == 34 && wid == 4 && lane == 0) flag_st(myflag, tag);
                g128_issue<true>(lds0, g2, t + 4, lw, vH, vB); WAIT_V(12);
            }
            BAR; g128_issue<true>(lds0, g2, 39, lw, vH, vB);
            const u64 seen = flag_ld(pflag);
            WAIT_V(13);
            BAR; WAIT_V(7);
            BAR; WAIT_V(0);
            const char* psrc = ws + WS_PART + tile * 32768 + (kh ^ 1) * 16384 + lw * 4096;
            const bool early = __all(seen == tag);
            if (early) { glds_pair_sc1<131072>(psrc, lane * 16, lds0 + lw * 4096); glds_pair_sc1<131072 + 2048>(psrc + 2048, lane * 16, lds0 + lw * 4096); }
            BAR;
            if (!early) { flag_wait(pflag, tag); glds_pair_sc1<131072>(psrc, lane * 16, lds0 + lw * 4096); glds_pair_sc1<131072 + 2048>(psrc + 2048, lane * 16, lds0 + lw * 4096); }
            WAIT_V(0); BAR;
            if (bid == 0 && wid == 4) {
#pragma unroll
                for (int i = 0; i < 4; ++i) flag_wait(FL(F_LN + i * 64 + lane), tag);
                if (lane == 0) flag_st(flags0, tag);
            }
        } else {
            const int wm = wid >> 1, wn = wid & 1, g = lane >> 4, q = (lane & 15) >> 2, p = lane & 3, swz = (2 * g + (q >> 1)) & 3;
            const int aoff = wm * 8192 + lane * 16;
            const int boff0 = 16384 + (4 * g + q) * 128 + (((2 * wn) ^ swz) * 32) + p * 8, boff1 = boff0 ^ 32;
            f32x4 acc[4][4]; bf16x8 AX[4], AY[4];
            s16x4 XL[4], XH[4], YL[4], YH[4];
            int pa[2] = {aoff, aoff + 65536}, pb0[2] = {boff0, boff0 + 65536}, pb1[2] = {boff1, boff1 + 65536};
            PIN(pa[1]); PIN(pb0[1]); PIN(pb1[1]);
#define G_BP(n, sl, kk) (lds + (((n) & 1) ? pb1[(sl) >> 1] : pb0[(sl) >> 1]) + ((sl) & 1) * 32768 + ((n) >> 1) * 8192 + (kk) * 4096)
#define G_BL(L_, n, sl, kk) do { L_[n] = trd(G_BP(n, sl, kk)); SCHED; } while (0)
#define G_BH(H_, n, sl, kk) do { H_[n] = trd(G_BP(n, sl, kk) + 2048); SCHED; } while (0)
#define G_A(A_, m, sl, kk) do { A_[m] = *(const LAS bf16x8*)(lds + pa[(sl) >> 1] + ((sl) & 1) * 32768 + ((m) * 2 + (kk)) * 1024); SCHED; } while (0)
#define G_M(A_, L_, H_, m, n) do { MFMA16_ACC(acc[m][n], cat8(L_[n], H_[n]), A_[m]); SCHED; } while (0)
#define G_REST(AC, CL, CH, AN, NL, NH, sl, kk) do { \
        G_BL(NL, 0, sl, kk); G_M(AC, CL, CH, 0, 2); G_BH(NH, 0, sl, kk); G_M(AC, CL, CH, 0, 3); G_A(AN, 0, sl, kk); G_M(AC, CL, CH, 1, 0); G_BL(NL, 1, sl, kk); G_M(AC, CL, CH, 1, 1); \
        G_BH(NH, 1, sl, kk); G_M(AC, CL, CH, 1, 2); G_BL(NL, 2, sl, kk); G_M(AC, CL, CH, 1, 3); G_BH(NH, 2, sl, kk); G_M(AC, CL, CH, 2, 0); G_BL(NL, 3, sl, kk); G_M(AC, CL, CH, 2, 1); \
        G_BH(NH, 3, sl, kk); G_M(AC, CL, CH, 2, 2); G_A(AN, 1, sl, kk); G_M(AC, CL, CH, 2, 3); G_A(AN, 2, sl, kk); G_M(AC, CL, CH, 3, 0); G_A(AN, 3, sl, kk); G_M(AC, CL, CH, 3, 1); \
        G_M(AC, CL, CH, 3, 2); G_M(AC, CL, CH, 3, 3); } while (0)
#define G_TILE(sl) do { G_M(AX, XL, XH, 0, 0); G_M(AX, XL, XH, 0, 1); G_REST(AX, XL, XH, AY, YL, YH, sl, 1); \
        G_M(AY, YL, YH, 0, 0); G_M(AY, YL, YH, 0, 1); \
        asm volatile("s_waitcnt vmcnt(0) lgkmcnt(0)" ::: "memory"); BAR; SCHED;        \
        G_REST(AY, YL, YH, AX, XL, XH, ((sl) + 1) & 3, 0); } while (0)
#pragma unroll
            for (int m = 0; m < 4; ++m)
#pragma unroll
                for (int n = 0; n < 4; ++n) { acc[m][n] = (f32x4){0.f, 0.f, 0.f, 0.f}; PIN(acc[m][n]); }
            SCHED;
#pragma unroll
            for (int i = 0; i < 4; ++i) { G_BL(XL, i, 0, 0); G_BH(XH, i, 0, 0); G_A(AX, i, 0, 0); }
            for (int t = 0; t < 32; t += 4) { G_TILE(0); G_TILE(1); G_TILE(2); G_TILE(3); }
            asm volatile("s_nop 7"); SCHED;
            const int fr = lane & 15, fq = lane >> 4;
            const size_t rowo = (size_t)(rt2 * 128 + wm * 64 + kh * 32 + fr) * DM + ct2 * 128 + wn * 32 + (fq & 1) * 16 + (fq >> 1) * 8;
            u32x4 xv[2][2];
#pragma unroll
            for (int m = 0; m < 2; ++m)
#pragma unroll
                for (int s = 0; s < 2; ++s) ST16(pack8(acc[2 + m][2 * s], acc[2 + m][2 * s + 1]), WS_PART + tile * 32768 + kh * 16384 + (m * 2 + s) * 4096 + tid * 16);
#pragma unroll
            for (int m = 0; m < 2; ++m)
#pragma unroll
                for (int s = 0; s < 2; ++s) xv[m][s] = *(const u32x4*)(x + rowo + (size_t)m * 16 * DM + s * 64);
            SCHED;
#define H_REST(AC, CL, CH, AN, NL, NH, sl, kk) do { \
        G_BL(NL, 0, sl, kk); G_BH(NH, 0, sl, kk); G_M(AC, CL, CH, 0, 2); G_A(AN, 0, sl, kk); G_BL(NL, 1, sl, kk); G_M(AC, CL, CH, 0, 3); G_BH(NH, 1, sl, kk); G_BL(NL, 2, sl, kk); G_M(AC, CL, CH, 1, 0); \
        G_BH(NH, 2, sl, kk); G_BL(NL, 3, sl, kk); G_M(AC, CL, CH, 1, 1); G_BH(NH, 3, sl, kk); G_M(AC, CL, CH, 1, 2); G_A(AN, 1, sl, kk); G_M(AC, CL, CH, 1, 3); } while (0)
#define H_TILE(sl, SYNC) do { G_M(AX, XL, XH, 0, 0); G_M(AX, XL, XH, 0, 1); H_REST(AX, XL, XH, AY, YL, YH, sl, 1); \
        G_M(AY, YL, YH, 0, 0); G_M(AY, YL, YH, 0, 1); SYNC; SCHED; H_REST(AY, YL, YH, AX, XL, XH, ((sl) + 1) & 3, 0); } while (0)
#define H_BAR do { WAIT_L(0); BAR; } while (0)
            H_TILE(0, H_BAR); H_TILE(1, H_BAR); H_TILE(2, WAIT_V(0); H_BAR); H_TILE(3, H_BAR);
            H_TILE(0, H_BAR); H_TILE(1, H_BAR); H_TILE(2, H_BAR); H_TILE(3, );
#undef G_BP
#undef G_BL
#undef G_BH
#undef G_A
#undef G_M
#undef G_REST
#undef G_TILE
#undef H_REST
#undef H_TILE
#undef H_BAR
            asm volatile("s_nop 7"); SCHED;
            BAR; CBAR;
            u32x4 pv[2][2];
#pragma unroll
            for (int m = 0; m < 2; ++m)
#pragma unroll
                for (int s = 0; s < 2; ++s) pv[m][s] = *(const LAS u32x4*)(lds + 131072 + (m * 2 + s) * 4096 + tid * 16);
#pragma unroll
            for (int m = 0; m < 2; ++m)
#pragma unroll
                for (int s = 0; s < 2; ++s) { float lo[4], hi4[4]; const u32x4 p_ = pv[m][s], x_ = xv[m][s];
                    const f32x4 a0 = acc[m][2 * s] + (f32x4){bf_lo(p_[0]), bf_hi(p_[0]), bf_lo(p_[1]), bf_hi(p_[1])}, a1 = acc[m][2 * s + 1] + (f32x4){bf_lo(p_[2]), bf_hi(p_[2]), bf_lo(p_[3]), bf_hi(p_[3])};
#pragma unroll
                    for (int i = 0; i < 4; ++i) { auto r = __builtin_amdgcn_permlane16_swap(__float_as_uint(a0[i]), __float_as_uint(a1[i]), false, false);
                        lo[i] = __uint_as_float(r[0]); hi4[i] = __uint_as_float(r[1]); }
                    u32x4 o; o[0] = cvtpk(lo[0] + bf_lo(x_[0]), lo[1] + bf_hi(x_[0])); o[1] = cvtpk(lo[2] + bf_lo(x_[1]), lo[3] + bf_hi(x_[1]));
                    o[2] = cvtpk(hi4[0] + bf_lo(x_[2]), hi4[1] + bf_hi(x_[2])); o[3] = cvtpk(hi4[2] + bf_lo(x_[3]), hi4[3] + bf_hi(x_[3]));
                    __builtin_nontemporal_store(o, (u32x4*)(out + rowo + (size_t)m * 16 * DM + s * 64));
                }
        }
    }
}

extern "C" void kernel_launch(void* const* d_in, const int* in_sizes, int n_in, void* d_out, int out_size, void* d_ws, size_t ws_size, hipStream_t stream) {
    (void)hipFuncSetAttribute((const void*)k_block, hipFuncAttributeMaxDynamicSharedMemorySize, LDS_BYTES);
    k_block<<<256, 512, LDS_BYTES, stream>>>((const u16*)d_in[0], (const u16*)d_in[1], (const u16*)d_in[2], (const u16*)d_in[4], (const u16*)d_in[5], (const u16*)d_in[6], (char*)d_ws, (u16*)d_out);
}
```

```cpp
#include <hip/hip_runtime.h>
#include <hip/hip_bf16.h>
#include <stdint.h>

#define LAS __attribute__((address_space(3)))
#define GAS __attribute__((address_space(1)))
#define DI __device__ __forceinline__
typedef unsigned short u16;
typedef unsigned long long u64;
typedef short bf16x8 __attribute__((ext_vector_type(8)));
typedef short s16x4 __attribute__((ext_vector_type(4)));
typedef float f32x2 __attribute__((ext_vector_type(2)));
typedef float f32x4 __attribute__((ext_vector_type(4)));
typedef float f32x16 __attribute__((ext_vector_type(16)));
typedef unsigned u32x2 __attribute__((ext_vector_type(2)));
typedef unsigned u32x4 __attribute__((ext_vector_type(4)));
typedef __bf16 bf16x2_t __attribute__((ext_vector_type(2)));

constexpr int SEQ = 2048, DM = 1024, FF = 3584;
constexpr int KB_X = DM / 32;
constexpr int KB_AH = (DM + FF) / 32;
constexpr unsigned WS_XN = 0;
constexpr unsigned WS_Q = WS_XN + SEQ * DM * 2;
constexpr unsigned WS_K = WS_Q + SEQ * DM * 2;
constexpr unsigned WS_V = WS_K + SEQ * DM * 2;
constexpr unsigned WS_AH = WS_V + SEQ * DM * 2;
constexpr unsigned WS_PART = WS_AH + SEQ * (DM + FF) * 2;
constexpr unsigned WS_FLAG = WS_PART + 128 * 32768;
constexpr unsigned WS_END = WS_FLAG + 2048 * 128;
constexpr int F_LN = 32, F_QKV = F_LN + 256, F_H = F_QKV + 256, F_AT = F_H + 256, F_G2 = F_AT + 256;
constexpr int LDS_BYTES = 163840;

DI unsigned cvtpk(float lo, float hi) { f32x2 v = {lo, hi}; bf16x2_t b = __builtin_convertvector(v, bf16x2_t); return __builtin_bit_cast(unsigned, b); }
DI float bf_lo(unsigned u) { return __uint_as_float(u << 16); }
DI float bf_hi(unsigned u) { return __uint_as_float(u & 0xffff0000u); }
DI float rbf(float f) { return __uint_as_float(cvtpk(f, f) << 16); }
DI s16x4 trd(const LAS char* p) { return __builtin_amdgcn_ds_read_tr16_b64_v4i16((LAS s16x4*)p); }
DI bf16x8 cat8(s16x4 a, s16x4 b) { return __builtin_shufflevector(a, b, 0, 1, 2, 3, 4, 5, 6, 7); }
template <int OFF, int D> DI void glds(const char* base, unsigned voff, unsigned dst) {
    asm volatile("s_add_u32 m0, %2, %4\n\ts_nop 0\n\tglobal_load_lds_dwordx4 %0, %1 offset:%3" :: "v"(voff), "s"(base), "s"(dst), "i"(OFF), "i"(D - OFF) : "memory", "scc"); }
template <int D> DI void glds_pair_sc1(const char* base, unsigned voff, unsigned dst) {
    asm volatile("s_add_u32 m0, %2, %3\n\ts_nop 0\n\tglobal_load_lds_dwordx4 %0, %1 sc1\n\tglobal_load_lds_dwordx4 %0, %1 offset:1024 sc1" :: "v"(voff), "s"(base), "s"(dst), "i"(D) : "memory", "scc"); }
template <int D> DI void touch(const char* base, unsigned voff, unsigned dst) {
    asm volatile("s_add_u32 m0, %2, %3\n\ts_nop 0\n\tglobal_load_lds_dword %0, %1" :: "v"(voff), "s"(base), "s"(dst), "i"(D) : "memory", "scc"); }
#define WAIT_V(n) asm volatile("s_waitcnt vmcnt(" #n ")" ::: "memory")
#define WAIT_L(n) asm volatile("s_waitcnt lgkmcnt(" #n ")" ::: "memory")
#define BAR __builtin_amdgcn_s_barrier()
#define CBAR asm volatile("" ::: "memory")
#define SCHED __builtin_amdgcn_sched_barrier(0)
#define PIN(x) asm volatile("" : "+v"(x))
#define MFMA16(a, b, c) __builtin_amdgcn_mfma_f32_16x16x32_bf16(a, b, c, 0, 0, 0)
#define MFMA16_ACC(c, a, b) asm volatile("v_mfma_f32_16x16x32_bf16 %0, %1, %2, %0" : "+v"(c) : "v"(a), "v"(b))
#define MFMA32(a, b, c) __builtin_amdgcn_mfma_f32_32x32x16_bf16(a, b, c, 0, 0, 0)
#define ST16(v, off) __builtin_amdgcn_raw_buffer_store_b128(v, rs, (int)(off), 0, 16)
#define LD16(off) __builtin_amdgcn_raw_buffer_load_b128(rs, (int)(off), 0, 16)
DI u64 flag_ld(const GAS u64* p) { return __hip_atomic_load(p, __ATOMIC_RELAXED, __HIP_MEMORY_SCOPE_AGENT); }
DI void flag_st(GAS u64* p, u64 v) { __hip_atomic_store(p, v, __ATOMIC_RELAXED, __HIP_MEMORY_SCOPE_AGENT); }
DI void flag_wait(const GAS u64* p, u64 tag) {
    for (unsigned spins = 0; spins < (1u << 18); ++spins) { if (__all(flag_ld(p) == tag)) break; __builtin_amdgcn_s_sleep(1); }
    CBAR;
}
template <int N> DI float ror_add(float v) { return v + __builtin_bit_cast(float, __builtin_amdgcn_update_dpp(0, __builtin_bit_cast(int, v), 0x120 + N  , 0xf, 0xf, false)); }
DI float sum64(float v) {
    v = ror_add<1>(ror_add<2>(ror_add<4>(ror_add<8>(v))));
    { auto r = __builtin_amdgcn_permlane16_swap(__float_as_uint(v), __float_as_uint(v), false, false); v = __uint_as_float(r[0]) + __uint_as_float(r[1]); }
    { auto r = __builtin_amdgcn_permlane32_swap(__float_as_uint(v), __float_as_uint(v), false, false); v = __uint_as_float(r[0]) + __uint_as_float(r[1]); }
    return v;
}
DI u32x4 widen(u32x2 a, u32x2 b) { auto r0 = __builtin_amdgcn_permlane16_swap(a[0], b[0], false, false); auto r1 = __builtin_amdgcn_permlane16_swap(a[1], b[1], false, false);
    return (u32x4){r0[0], r1[0], r0[1], r1[1]}; }
DI int opaque_tid() { int t = threadIdx.x; asm volatile("" : "+v"(t)); return t; }

DI float silu_mul(float g, float u) { return g * u * __builtin_amdgcn_rcpf(1.0f + __builtin_amdgcn_exp2f(g * -1.4426950408889634f)); }
DI u32x4 pack8(const f32x4& a, const f32x4& b) { u32x4 o; o[0] = cvtpk(a[0], a[1]); o[1] = cvtpk(a[2], a[3]); o[2] = cvtpk(b[0], b[1]); o[3] = cvtpk(b[2], b[3]); return o; }
constexpr float QSCALE = 0.17677669529663687f * 1.4426950408889634f;


struct G128 { const char* a; const char* w2; const char* wo; int kh; };
template <bool HALF> DI void g128_issue(unsigned lds0, const G128& g, int t, int lw, unsigned voffA, unsigned voffB) {
    const unsigned so = lds0 + (t & 3) * 32768, sb = so + lw * 2048;
    const bool att = !HALF && g.kh == 0 && t >= 16;
    const int kt = HALF ? t - 16 : att ? t - 16 : g.kh * 24 + t;
    const char* a = g.a + (size_t)(att ? kt : 16 + kt) * 2048; const char* b = (att ? g.wo : g.w2) + (size_t)kt * (64 * DM * 2);
    if (HALF) glds_pair_sc1<0>(a, voffA, so + (lw >> 1) * 8192 + (lw & 1) * 2048);
    else { glds_pair_sc1<0>(a, voffA, so + lw * 4096); glds_pair_sc1<2048>(a + KB_AH * 1024, voffA, so + lw * 4096); }
    glds<0, 16384>(b, voffB, sb); glds<128, 24576>(b, voffB, sb);
    glds<0, 16384 + 1024>(b + 8 * DM * 2, voffB, sb); glds<128, 24576 + 1024>(b + 8 * DM * 2, voffB, sb);
}

__global__ __launch_bounds__(512, 2) void k_block(const u16* __restrict__ x, const u16* __restrict__ wqkv, const u16* __restrict__ wo, const u16* __restrict__ w1,
                                                  const u16* __restrict__ w3, const u16* __restrict__ w2, char* __restrict__ ws, u16* __restrict__ out) {
    extern __shared__ __attribute__((aligned(16))) char smem[];
    LAS char* lds = (LAS char*)smem;
    const unsigned lds0 = (unsigned)(size_t)lds;
    const int wid = __builtin_amdgcn_readfirstlane(threadIdx.x >> 6);
    const int bid = blockIdx.x, xcd = bid & 7, idx = bid >> 3;
    GAS u64* flags0 = (GAS u64*)(ws + WS_FLAG);
#define FL(i) (flags0 + (i) * 16)
    const __amdgpu_buffer_rsrc_t rs = __builtin_amdgcn_make_buffer_rsrc((void*)ws, 0, WS_END, 0x00020000);
    {
        const char* pc; asm volatile("s_getpc_b64 %0" : "=s"(pc));
        if (wid == 7) touch<148480>(pc + (idx & 7) * 4096, (threadIdx.x & 63) * 64, lds0 + 7 * 1024);
    }
    u64 tag;
    u32x4 xa, xb; u32x2 ep;
    { const char* xp = (const char*)x + (size_t)(bid * 8 + wid) * (DM * 2);
      asm volatile("global_load_dwordx4 %0, %3, %4\n\tglobal_load_dwordx4 %1, %3, %4 offset:1024\n\tglobal_load_dwordx2 %2, %5, %6 sc1"
                   : "=&v"(xa), "=&v"(xb), "=&v"(ep) : "v"((threadIdx.x & 63) * 16), "s"(xp), "v"(0), "s"(ws + WS_FLAG), "s"(wqkv), "s"(w1), "s"(w3) : "memory"); }
    const int cj = idx >> 2, ct = cj < 3 ? (xcd >> 1) * 3 + cj : 12 + (xcd >> 1) * 5 + (cj - 3), rt = (xcd & 1) * 4 + (idx & 3);
    const bool qkv = ct < 12;
    u32x4 hmain[8], hext[2];

    {
        const int tid = opaque_tid(), lane = tid & 63, wr = wid >> 2, wc = wid & 3;
        const unsigned ldb2 = qkv ? 3 * DM * 2 : FF * 2;
        const char* cB0 = qkv ? (const char*)wqkv + ct * 512 : (const char*)w1 + (ct - 12) * 256;
        const char* cB1 = qkv ? cB0 + 256 : (const char*)w3 + (ct - 12) * 256;
        const char* cA = ws + WS_XN + rt * 16 * KB_X * 1024;
        const unsigned voffA = wid * (KB_X * 1024) + lane * 16;
        const unsigned voffB = (unsigned)(wid * 8 + (lane >> 3)) * ldb2 + (((lane & 7) ^ (2 * ((lane >> 4) & 3))) * 16);
        const char* pE = ((lane & 1) ? (const char*)w3 : (const char*)w1) + (size_t)(wid * 8 + (lane >> 3)) * (FF * 2) + (2560 + ct * 32) * 2 + ((((lane >> 1) & 3) ^ ((lane >> 4) & 3)) * 16);
        const size_t kstepB = (size_t)64 * ldb2;
        const unsigned ldsA = lds0 + wid * 2048, ldsB = lds0 + wid * 1024;
        const char* cT = ((idx & 2) ? cB1 : cB0) + (idx & 1) * 128;
#define SA(b, h) (((b) * 2 + (h)) * 16384)
#define SB(b, h) (((b) * 2 + (h)) * 16384)
#define STA(b, h, kt) glds_pair_sc1<SA(b, h)>(cA + (h) * 8 * KB_X * 1024 + (kt) * 2048, voffA, ldsA)
#define STB(b, h, kt) do { const char* s_ = ((h) ? cB1 : cB0) + (size_t)(kt) * kstepB; \
        glds<0, 65536 + SB(b, h)>(s_, voffB, ldsB); glds<128, 65536 + SB(b, h) + 8192>(s_, voffB, ldsB); } while (0)
#define TCH(kt) touch<148480>(cT + (size_t)(kt) * kstepB, voffB, ldsB)
#define STE(b, kt) do { const char* s_ = pE + (size_t)(kt) * (64 * FF * 2); \
        asm volatile("s_add_u32 m0, %1, %2\n\ts_nop 0\n\tglobal_load_lds_dwordx4 %0, off" :: "v"(s_), "s"(ldsB), "i"(131072 + (b) * 8192) : "memory", "scc"); } while (0)
        {
            TCH(0); TCH(1);
            { const char* s_ = pE + (size_t)(idx & 1) * (64 * FF * 2) + (ptrdiff_t)(((idx >> 1) & 1) - (wid >> 2)) * (32 * FF * 2);
              asm volatile("s_add_u32 m0, %1, 148480\n\ts_nop 0\n\tglobal_load_lds_dword %0, off" :: "v"(s_), "s"(ldsB) : "memory", "scc"); }
            asm volatile("s_waitcnt vmcnt(3)" : "+v"(xa), "+v"(xb), "+v"(ep) :: "memory");
            tag = (((u64)(unsigned)__builtin_amdgcn_readfirstlane(ep[1]) << 32) | (unsigned)__builtin_amdgcn_readfirstlane(ep[0])) + 1;
            float f[16];
#pragma unroll
            for (int i = 0; i < 4; ++i) { f[2 * i] = bf_lo(xa[i]); f[2 * i + 1] = bf_hi(xa[i]); f[8 + 2 * i] = bf_lo(xb[i]); f[9 + 2 * i] = bf_hi(xb[i]); }
            float s = 0.f;
#pragma unroll
            for (int i = 0; i < 16; ++i) s += f[i];
            const float mean = sum64(s) * (1.0f / DM);
            float q = 0.f;
#pragma unroll
            for (int i = 0; i < 16; ++i) { const float d = f[i] - mean; q += d * d; }
            const float var = sum64(q) * (1.0f / DM);
            const float mb = rbf(mean), sd = rbf(__builtin_amdgcn_sqrtf(rbf(rbf(var) + 1e-5f))), inv = __builtin_amdgcn_rcpf(sd);
#pragma unroll
            for (int c = 0; c < 2; ++c) { u32x4 o;
#pragma unroll
                for (int i = 0; i < 4; ++i) o[i] = cvtpk(rbf(f[c * 8 + 2 * i] - mb) * inv, rbf(f[c * 8 + 2 * i + 1] - mb) * inv);
                *(LAS u32x4*)(lds + wid * 2080 + c * 1024 + lane * 16) = o; }
            WAIT_L(0); BAR; CBAR;
            const int r = lane & 7, g = (lane >> 3) & 3, row = bid * 8 + r;
#pragma unroll
            for (int c = 0; c < 2; ++c) { const int kb = 4 * wid + 2 * c + (lane >> 5); const LAS char* p = lds + r * 2080 + kb * 64 + g * 8;
                const u32x2 lo = *(const LAS u32x2*)p, hi = *(const LAS u32x2*)(p + 32);
                ST16(((u32x4){lo[0], lo[1], hi[0], hi[1]}), WS_XN + ((row >> 4) * KB_X + kb) * 1024 + (g * 16 + (row & 15)) * 16); }
            CBAR;
            STB(0, 0, 0); STB(0, 1, 0); STE(0, 0);
            WAIT_V(5); BAR;
            if (wid == 0) { if (lane == 0) flag_st(FL(F_LN + bid), tag); flag_wait(FL(F_LN + rt * 32 + (lane & 31)), tag); }
            STB(1, 0, 1); STB(1, 1, 1); STE(1, 1);
            if (wid != 0) { TCH(2); TCH(3); }
            BAR; CBAR;
        }
        f32x4 acc[2][2][4][2], ace[2][4];
        {
            const int g = lane >> 4, q = (lane & 15) >> 2, p = lane & 3, swz = (2 * g + (q >> 1)) & 3;
            const int aoff = wr * 8192 + lane * 16;
            const int boff0 = 65536 + (wc >> 1) * 8192 + (4 * g + q) * 128 + (((2 * (wc & 1)) ^ swz) * 32) + p * 8, boff1 = boff0 ^ 32;
            int eoff = 131072 + (4 * g + q) * 128 + ((((wc >> 1) + 2 * (p & 1)) ^ swz) * 32) + (p >> 1) * 16 + (wc & 1) * 8; PIN(eoff);
#define LDA(dst, b, h) do { _Pragma("unroll") for (int m = 0; m < 4; ++m) _Pragma("unroll") for (int k = 0; k < 2; ++k) \
        dst[m][k] = *(const LAS bf16x8*)(lds + aoff + SA(b, h) + (m * 2 + k) * 1024); } while (0)
#define LDB(dst, b, h) do { _Pragma("unroll") for (int n = 0; n < 2; ++n) _Pragma("unroll") for (int k = 0; k < 2; ++k) { \
        const LAS char* b_ = lds + (n ? boff1 : boff0) + SB(b, h) + k * 4096; dst[n][k] = cat8(trd(b_), trd(b_ + 2048)); } } while (0)
#define LDE(dst, b) do { _Pragma("unroll") for (int k = 0; k < 2; ++k) { const LAS char* b_ = lds + eoff + (b) * 8192 + k * 4096; dst[k] = cat8(trd(b_), trd(b_ + 2048)); } } while (0)
#define MMA(ai) do { __builtin_amdgcn_s_setprio(1); \
        _Pragma("unroll") for (int k = 0; k < 2; ++k) _Pragma("unroll") for (int m = 0; m < 4; ++m) { \
            MFMA16_ACC(acc[ai][0][m][0], B0[0][k], At[m][k]); MFMA16_ACC(acc[ai][0][m][1], B0[1][k], At[m][k]); \
            MFMA16_ACC(acc[ai][1][m][0], B1[0][k], At[m][k]); MFMA16_ACC(acc[ai][1][m][1], B1[1][k], At[m][k]); \
            MFMA16_ACC(ace[ai][m], BE[k], At[m][k]); } \
        __builtin_amdgcn_s_setprio(0); } while (0)
            STA(0, 0, 0); STA(0, 1, 0); STA(1, 0, 1);
#pragma unroll
            for (int a = 0; a < 2; ++a)
#pragma unroll
                for (int m = 0; m < 4; ++m) { ace[a][m] = (f32x4){0.f, 0.f, 0.f, 0.f}; PIN(ace[a][m]);
#pragma unroll
                    for (int b = 0; b < 2; ++b)
#pragma unroll
                        for (int n = 0; n < 2; ++n) { acc[a][b][m][n] = (f32x4){0.f, 0.f, 0.f, 0.f}; PIN(acc[a][b][m][n]); } }
            bf16x8 At[4][2], B0[2][2], B1[2][2], BE[2];
            constexpr int nt = DM / 64;
            if (wr == 1) BAR;
            WAIT_V(4); BAR;
            WAIT_V(2); BAR;
            for (int t = 0; t < nt; t += 2) {
                const bool more = t + 2 < nt, far = t + 4 < nt;
                LDB(B0, 0, 0); LDB(B1, 0, 1); LDE(BE, 0); SCHED; LDA(At, 0, 0); STA(1, 1, t + 1);
                if (far) { TCH(t + 4); WAIT_V(11); } else { WAIT_V(9); }
                WAIT_L(0); BAR; MMA(0); BAR; SCHED;
                LDA(At, 0, 1);
                if (more) { STB(0, 0, t + 2); STB(0, 1, t + 2); STE(0, t + 2); STA(0, 0, t + 2); if (far) { WAIT_V(10); } else { WAIT_V(9); } } else { WAIT_V(2); }
                WAIT_L(0); BAR; MMA(1); BAR; SCHED;
                LDB(B0, 1, 0); LDB(B1, 1, 1); LDE(BE, 1); SCHED; LDA(At, 1, 0);
                if (more) { STA(0, 1, t + 2); if (far) { TCH(t + 5); WAIT_V(11); } else { WAIT_V(9); } } else { WAIT_V(0); }
                WAIT_L(0); BAR; MMA(0); BAR; SCHED;
                LDA(At, 1, 1);
                if (more) { STB(1, 0, t + 3); STB(1, 1, t + 3); STE(1, t + 3); STA(1, 0, t + 3); if (far) { WAIT_V(10); } else { WAIT_V(9); } }
                WAIT_L(0); BAR; MMA(1); if (more || wr == 0) BAR; SCHED;
            }
            asm volatile("s_nop 7"); SCHED;
#undef SA
#undef SB
#undef STA
#undef STB
#undef STE
#undef TCH
#undef LDA
#undef LDB
#undef LDE
#undef MMA
        }
        const int ln = opaque_tid() & 63, fr = ln & 15, fq = ln >> 4;
        if (qkv) {
            const int kind = ct >> 2;
            const float sc = kind == 0 ? QSCALE : 1.0f;
            const unsigned base = WS_Q + kind * (SEQ * DM * 2) + ((ct & 3) * 8 + wc) * 131072 + (rt * 256 + wr * 64) * 64;
            const unsigned lo0 = base + (kind == 2 ? fr * 64 + fq * 16 : fr * 16 + fq * 512), lo1 = lo0 + (kind == 2 ? 1024 : 256);
#pragma unroll
            for (int bj = 0; bj < 2; ++bj)
#pragma unroll
                for (int ai = 0; ai < 2; ++ai)
#pragma unroll
                    for (int m = 0; m < 4; ++m)
                        ST16(pack8(acc[ai][bj][m][0] * sc, acc[ai][bj][m][1] * sc), bj * 4 * 131072 + ai * 8192 + (m >> 1) * 2048 + ((m & 1) ? lo1 : lo0));
            WAIT_V(0); BAR;
            if (wid == 0 && ln == 0) flag_st(FL(F_QKV + rt * 32 + ct), tag);
        } else {
#pragma unroll
            for (int ai = 0; ai < 2; ++ai)
#pragma unroll
                for (int m = 0; m < 4; ++m) { f32x4 h0, h1;
#pragma unroll
                    for (int j = 0; j < 4; ++j) { h0[j] = silu_mul(acc[ai][0][m][0][j], acc[ai][1][m][0][j]); h1[j] = silu_mul(acc[ai][0][m][1][j], acc[ai][1][m][1][j]); }
                    hmain[ai * 4 + m] = pack8(h0, h1);
                }
        }
#pragma unroll
        for (int ai = 0; ai < 2; ++ai) { u32x2 e[2];
#pragma unroll
            for (int mp = 0; mp < 2; ++mp) { float h[4];
#pragma unroll
                for (int j = 0; j < 4; ++j) { auto r = __builtin_amdgcn_permlane32_swap(__float_as_uint(ace[ai][2 * mp][j]), __float_as_uint(ace[ai][2 * mp + 1][j]), false, false);
                    h[j] = silu_mul(__uint_as_float(r[0]), __uint_as_float(r[1])); }
                e[mp] = (u32x2){cvtpk(h[0], h[1]), cvtpk(h[2], h[3])}; }
            hext[ai] = widen(e[0], e[1]); }
    }

    const int kh = xcd & 1, rt2 = ((xcd >> 1) & 1) * 8 + (idx & 7), ct2 = (xcd >> 2) * 4 + (idx >> 3);
    G128 g2; g2.kh = kh; g2.a = ws + WS_AH + rt2 * 8 * KB_AH * 1024; g2.w2 = (const char*)w2 + ct2 * 256; g2.wo = (const char*)wo + ct2 * 256;
#define G2_OFFS(lw_, l_) const unsigned vA = ((lw_) ^ kh) * 2 * KB_AH * 1024 + (l_) * 16, vB = ((lw_) * 16 + ((l_) >> 3)) * (DM * 2) + ((((l_) & 7) ^ (2 * (((l_) >> 4) & 3))) * 16)

    {
        constexpr int AT_BASE = 65536, AT_STAGE = 32768;
        const int lane = opaque_tid() & 63, hi = lane >> 5;
        const int head = xcd * 4 + (idx >> 3), qb = idx & 7;
        if (wid == 0) flag_wait(FL(F_QKV + (lane < 4 ? (lane & 1) * 32 + 4 + (lane >> 1) * 4 : qb * 32) + (head >> 3)), tag);
        BAR; CBAR;
        const char* Kg = ws + WS_K + head * 131072; const char* Vg = ws + WS_V + head * 131072;
        const unsigned soff = wid * 2048 + lane * 16;
        const unsigned sdst = lds0 + AT_BASE + wid * 2048;
#define AT_ISSUE(s, slot) do { glds_pair_sc1<0>(Kg + (s) * 16384, soff, sdst + (slot)); glds_pair_sc1<16384>(Vg + (s) * 16384, soff, sdst + (slot)); } while (0)
        const unsigned qoff = WS_Q + (head * 64 + qb * 8 + wid) * 2048 + lane * 16;
        const bf16x8 q0 = __builtin_bit_cast(bf16x8, LD16(qoff)), q1 = __builtin_bit_cast(bf16x8, LD16(qoff + 1024));
        AT_ISSUE(0, 0); AT_ISSUE(1, AT_STAGE);
        const GAS u64* kvflag = FL(F_QKV + (lane & 7) * 32 + 4 + ((lane >> 3) & 1) * 4 + (head >> 3));
        u64 seen = flag_ld(kvflag);
        bf16x8 sel; { const short one = ((lane & 15) == ((lane >> 4) & 1)) ? (short)0x3f80 : (short)0;
#pragma unroll
            for (int i = 0; i < 8; ++i) sel[i] = one; }
        const LAS char* kb0 = lds + AT_BASE + lane * 16;
        const LAS char* vb0 = lds + AT_BASE + 16384 + (4 * hi + ((lane & 15) >> 2)) * 64 + ((lane >> 4) & 1) * 32 + (lane & 3) * 8;
        f32x16 O, negm, SA, SB; f32x4 L = {0.f, 0.f, 0.f, 0.f};
        bf16x8 k0, k1, v0, v1;
#pragma unroll
        for (int i = 0; i < 16; ++i) { O[i] = 0.f; negm[i] = 0.f; }
        WAIT_V(5); BAR;
        {
            k0 = *(const LAS bf16x8*)(kb0); k1 = *(const LAS bf16x8*)(kb0 + 1024);
            SA = MFMA32(k0, q0, negm); SA = MFMA32(k1, q1, SA);
            k0 = *(const LAS bf16x8*)(kb0 + 2048); k1 = *(const LAS bf16x8*)(kb0 + 3072);
            v0 = cat8(trd(vb0), trd(vb0 + 512)); v1 = cat8(trd(vb0 + 1024), trd(vb0 + 1536));
            float m = SA[0];
#pragma unroll
            for (int i = 1; i < 16; ++i) m = fmaxf(m, SA[i]);
            m = fmaxf(m, __shfl_xor(m, 32));
#pragma unroll
            for (int i = 0; i < 16; ++i) { negm[i] = -m; SA[i] -= m; }
            PIN(negm);
        }
#define EX(S, i) S[i] = __builtin_amdgcn_exp2f(S[i])
#define BC8(x) __builtin_bit_cast(bf16x8, x)
#define AT_STEP(SC, SN, kp, v1p, v0p) do { \
        O = MFMA32(v1, BC8(p1), O); SCHED; v1 = cat8(trd(v1p), trd((v1p) + 512)); EX(SC, 0); EX(SC, 1); EX(SC, 2); PIN(SC); SCHED; \
        SN = MFMA32(k0, q0, negm); SCHED; k0 = *(const LAS bf16x8*)(kp); EX(SC, 3); EX(SC, 4); EX(SC, 5); PIN(SC); SCHED; \
        L = MFMA16(sel, BC8(p1), L); SCHED; EX(SC, 6); EX(SC, 7); \
        p0[0] = cvtpk(SC[0], SC[1]); p0[1] = cvtpk(SC[2], SC[3]); p0[2] = cvtpk(SC[4], SC[5]); p0[3] = cvtpk(SC[6], SC[7]); PIN(p0); SCHED; \
        O = MFMA32(v0, BC8(p0), O); SCHED; v0 = cat8(trd(v0p), trd((v0p) + 512)); EX(SC, 8); EX(SC, 9); EX(SC, 10); PIN(SC); SCHED; \
        SN = MFMA32(k1, q1, SN); SCHED; k1 = *(const LAS bf16x8*)((kp) + 1024); EX(SC, 11); EX(SC, 12); EX(SC, 13); PIN(SC); SCHED; \
        L = MFMA16(sel, BC8(p0), L); SCHED; EX(SC, 14); EX(SC, 15); \
        p1[0] = cvtpk(SC[8], SC[9]); p1[1] = cvtpk(SC[10], SC[11]); p1[2] = cvtpk(SC[12], SC[13]); p1[3] = cvtpk(SC[14], SC[15]); PIN(p1); SCHED; } while (0)
        u32x4 p0, p1 = {0u, 0u, 0u, 0u};
        int cur = 0, nxt = AT_STAGE, fre = 2 * AT_STAGE;
        const GAS u64* hflag = FL(F_H + (rt2 >> 1) * 32 + (lane & 31));
        const unsigned hb = WS_AH + ((rt * 16 + (wid >> 2) * 4) * KB_AH + KB_X + (ct - 12) * 4 + (wid & 3)) * 1024 + lane * 16;
        const unsigned eb = WS_AH + ((rt * 16 + (wid >> 2) * 4 + 2 * ((lane >> 4) & 1) + hi) * KB_AH + KB_X + 80 + ct) * 1024 + ((wid & 3) * 16 + (lane & 15)) * 16;
#define HM(i) ST16(hmain[i], hb + (((i) >> 2) * 8 + ((i) & 3)) * KB_AH * 1024)
#define H_ST(j) do { if (s == 4) ST16(hext[j], eb + (j) * 8 * KB_AH * 1024); else if (!qkv && s < 4) { if (s == 0) HM(j); else if (s == 1) HM(2 + (j)); else if (s == 2) HM(4 + (j)); else HM(6 + (j)); } SCHED; } while (0)
        for (int s = 0; s < 8; ++s) {
            if (s >= 1 && s <= 4) { if (qkv) { WAIT_V(1); } else { WAIT_V(3); } } else if (s == 5) { WAIT_V(2); } else { WAIT_V(0); }
            WAIT_L(0); BAR;
            if (s == 0) { if (!__all(seen == tag)) flag_wait(kvflag, tag); }
            if (s + 2 < 8) AT_ISSUE(s + 2, fre);
            if (s < 4) {
                const int r0 = (idx * 8 + wid) * 10 + (s < 3 ? s * 4 : 0), rl = s == 2 ? ((lane >> 4) & 1) : (lane >> 4);
                const char* src = (kh ? (const char*)w2 + 1024 * (DM * 2) : r0 >= 1536 ? (const char*)wo - 1536 * (DM * 2) : (const char*)w2) + (size_t)r0 * (DM * 2) + (xcd >> 2) * 1024;
                touch<-AT_BASE>(src, rl * (DM * 2) + (lane & 15) * 64, sdst);
            }
            if (s == 6) { if (wid == 0 && lane == 0) flag_st(FL(F_H + rt * 32 + ct), tag); }
            if (s == 7) seen = flag_ld(hflag);
            if (wid >= 4) __builtin_amdgcn_s_setprio(1);
            const LAS char* kc = kb0 + cur; const LAS char* vc = vb0 + cur; const LAS char* kn = kb0 + nxt; const LAS char* vn = vb0 + nxt;
            AT_STEP(SA, SB, kc + 2 * 2048, vc + 1024, vc + 1 * 2048);            AT_STEP(SB, SA, kc + 3 * 2048, vc + 1 * 2048 + 1024, vc + 2 * 2048);
            H_ST(0);
            AT_STEP(SA, SB, kc + 4 * 2048, vc + 2 * 2048 + 1024, vc + 3 * 2048); AT_STEP(SB, SA, kc + 5 * 2048, vc + 3 * 2048 + 1024, vc + 4 * 2048);
            if (s == 7 && wid >= 4) {
                if (!__all(seen == tag)) flag_wait(hflag, tag);
                G2_OFFS(wid - 4, lane);
                g128_issue<false>(lds0, g2, 0, wid - 4, vA, vB); g128_issue<false>(lds0, g2, 1, wid - 4, vA, vB);
            }
            SCHED;
            AT_STEP(SA, SB, kc + 6 * 2048, vc + 4 * 2048 + 1024, vc + 5 * 2048); __builtin_amdgcn_s_setprio(0); AT_STEP(SB, SA, kc + 7 * 2048, vc + 5 * 2048 + 1024, vc + 6 * 2048);
            H_ST(1);
            AT_STEP(SA, SB, kn, vc + 6 * 2048 + 1024, vc + 7 * 2048);            AT_STEP(SB, SA, kn + 2048, vc + 7 * 2048 + 1024, vn);
            const int tmp = cur; cur = nxt; nxt = fre; fre = tmp;
        }
        O = MFMA32(v1, BC8(p1), O); L = MFMA16(sel, BC8(p1), L);
        float lrow; { const float a = __shfl(L[0], lane & 15), b = __shfl(L[1], lane & 15); lrow = (lane & 16) ? b : a; }
        float oabs = 0.f;
#pragma unroll
        for (int i = 0; i < 16; ++i) oabs += fabsf(O[i]);
        if (__any(!(lrow < 1.0e30f) || !(oabs < 3.0e38f))) {
            __builtin_amdgcn_fence(__ATOMIC_ACQUIRE, "agent");
            float m = -INFINITY, l = 0.f;
#pragma unroll
            for (int i = 0; i < 16; ++i) { O[i] = 0.f; negm[i] = 0.f; }
            const u16* Vs = (const u16*)Vg;
            for (int t = 0; t < 64; ++t) { const int key0 = t * 32;
                k0 = *(const bf16x8*)(Kg + key0 * 64 + lane * 16); k1 = *(const bf16x8*)(Kg + key0 * 64 + 1024 + lane * 16);
#pragma unroll
                for (int j = 0; j < 8; ++j) { const int kk = key0 + 8 * (j >> 2) + 4 * hi + (j & 3); v0[j] = (short)Vs[kk * 32 + (lane & 31)]; v1[j] = (short)Vs[(kk + 16) * 32 + (lane & 31)]; }
                f32x16 S = MFMA32(k0, q0, negm); S = MFMA32(k1, q1, S);
                float mt = S[0];
#pragma unroll
                for (int i = 1; i < 16; ++i) mt = fmaxf(mt, S[i]);
                mt = fmaxf(mt, __shfl_xor(mt, 32));
                const float mn = fmaxf(m, mt), al = __builtin_amdgcn_exp2f(m - mn); m = mn;
                float ps = 0.f;
#pragma unroll
                for (int i = 0; i < 16; ++i) { S[i] = rbf(__builtin_amdgcn_exp2f(S[i] - mn)); ps += S[i]; O[i] *= al; }
                l = l * al + ps + __shfl_xor(ps, 32);
#pragma unroll
                for (int i = 0; i < 4; ++i) { p0[i] = cvtpk(S[2 * i], S[2 * i + 1]); p1[i] = cvtpk(S[8 + 2 * i], S[9 + 2 * i]); }
                O = MFMA32(v0, BC8(p0), O); O = MFMA32(v1, BC8(p1), O);
            }
            lrow = l;
        }
        {
            const float inv = 1.0f / lrow;
            unsigned pk[8];
#pragma unroll
            for (int i = 0; i < 8; ++i) pk[i] = cvtpk(O[2 * i] * inv, O[2 * i + 1] * inv);
            const unsigned dst = WS_AH + ((qb * 16 + wid * 2 + ((lane >> 4) & 1)) * KB_AH + head) * 1024 + (lane & 15) * 16 + hi * 256;
#pragma unroll
            for (int c = 0; c < 2; ++c) {
                auto r0 = __builtin_amdgcn_permlane32_swap(pk[4 * c + 0], pk[4 * c + 2], false, false);
                auto r1 = __builtin_amdgcn_permlane32_swap(pk[4 * c + 1], pk[4 * c + 3], false, false);
                ST16(((u32x4){r0[0], r1[0], r0[1], r1[1]}), dst + c * 512);
            }
        }
        if (wid >= 4) { WAIT_V(2); }
        WAIT_L(0); BAR;
#undef AT_ISSUE
#undef AT_STEP
#undef HM
#undef H_ST
#undef EX
#undef BC8
    }

    {
        const int tid = opaque_tid(), lane = tid & 63, tile = rt2 * 8 + ct2;
        GAS u64* myflag = FL(F_G2 + tile * 2 + kh); const GAS u64* pflag = FL(F_G2 + tile * 2 + (kh ^ 1));
        if (wid >= 4) {
            const int lw = wid - 4;
            G2_OFFS(lw, lane);
            const unsigned vH = ((lw >> 1) * 4 + 2 * kh + (lw & 1)) * KB_AH * 1024 + lane * 16;
            g128_issue<false>(lds0, g2, 2, lw, vA, vB); g128_issue<false>(lds0, g2, 3, lw, vA, vB);
            WAIT_V(16);
            for (int t = 0; t < 28; ++t) {
                BAR;
                if (t == 0 && wid == 4 && lane == 0) flag_st(FL(F_AT + (idx & 7) * 32 + xcd * 4 + (idx >> 3)), tag);
                if (t == 12 && kh == 0) flag_wait(FL(F_AT + (rt2 >> 1) * 32 + (lane & 31)), tag);
                g128_issue<false>(lds0, g2, t + 4, lw, vA, vB);
                WAIT_V(16);
            }
            BAR; g128_issue<true>(lds0, g2, 32, lw, vH, vB); WAIT_V(14);
            for (int t = 29; t < 35; ++t) {
                BAR;
                if (t == 34 && wid == 4 && lane == 0) flag_st(myflag, tag);
                g128_issue<true>(lds0, g2, t + 4, lw, vH, vB); WAIT_V(12);
            }
            BAR; g128_issue<true>(lds0, g2, 39, lw, vH, vB);
            const u64 seen = flag_ld(pflag);
            WAIT_V(13);
            BAR; WAIT_V(7);
            BAR; WAIT_V(0);
            const char* psrc = ws + WS_PART + tile * 32768 + (kh ^ 1) * 16384 + lw * 4096;
            const bool early = __all(seen == tag);
            if (early) { glds_pair_sc1<131072>(psrc, lane * 16, lds0 + lw * 4096); glds_pair_sc1<131072 + 2048>(psrc + 2048, lane * 16, lds0 + lw * 4096); }
            BAR;
            if (!early) { flag_wait(pflag, tag); glds_pair_sc1<131072>(psrc, lane * 16, lds0 + lw * 4096); glds_pair_sc1<131072 + 2048>(psrc + 2048, lane * 16, lds0 + lw * 4096); }
            WAIT_V(0); BAR;
            if (bid == 0 && wid == 4) {
#pragma unroll
                for (int i = 0; i < 4; ++i) flag_wait(FL(F_LN + i * 64 + lane), tag);
                if (lane == 0) flag_st(flags0, tag);
            }
        } else {
            const int wm = wid >> 1, wn = wid & 1, g = lane >> 4, q = (lane & 15) >> 2, p = lane & 3, swz = (2 * g + (q >> 1)) & 3;
            const int aoff = wm * 8192 + lane * 16;
            const int boff0 = 16384 + (4 * g + q) * 128 + (((2 * wn) ^ swz) * 32) + p * 8, boff1 = boff0 ^ 32;
            f32x4 acc[4][4]; bf16x8 AX[4], AY[4];
            s16x4 XL[4], XH[4], YL[4], YH[4];
            int pa[2] = {aoff, aoff + 65536}, pb0[2] = {boff0, boff0 + 65536}, pb1[2] = {boff1, boff1 + 65536};
            PIN(pa[1]); PIN(pb0[1]); PIN(pb1[1]);
#define G_BP(n, sl, kk) (lds + (((n) & 1) ? pb1[(sl) >> 1] : pb0[(sl) >> 1]) + ((sl) & 1) * 32768 + ((n) >> 1) * 8192 + (kk) * 4096)
#define G_BL(L_, n, sl, kk) do { L_[n] = trd(G_BP(n, sl, kk)); SCHED; } while (0)
#define G_BH(H_, n, sl, kk) do { H_[n] = trd(G_BP(n, sl, kk) + 2048); SCHED; } while (0)
#define G_A(A_, m, sl, kk) do { A_[m] = *(const LAS bf16x8*)(lds + pa[(sl) >> 1] + ((sl) & 1) * 32768 + ((m) * 2 + (kk)) * 1024); SCHED; } while (0)
#define G_M(A_, L_, H_, m, n) do { MFMA16_ACC(acc[m][n], cat8(L_[n], H_[n]), A_[m]); SCHED; } while (0)
#define G_REST(AC, CL, CH, AN, NL, NH, sl, kk) do { \
        G_BL(NL, 0, sl, kk); G_M(AC, CL, CH, 0, 2); G_BH(NH, 0, sl, kk); G_M(AC, CL, CH, 0, 3); G_A(AN, 0, sl, kk); G_M(AC, CL, CH, 1, 0); G_BL(NL, 1, sl, kk); G_M(AC, CL, CH, 1, 1); \
        G_BH(NH, 1, sl, kk); G_M(AC, CL, CH, 1, 2); G_BL(NL, 2, sl, kk); G_M(AC, CL, CH, 1, 3); G_BH(NH, 2, sl, kk); G_M(AC, CL, CH, 2, 0); G_BL(NL, 3, sl, kk); G_M(AC, CL, CH, 2, 1); \
        G_BH(NH, 3, sl, kk); G_M(AC, CL, CH, 2, 2); G_A(AN, 1, sl, kk); G_M(AC, CL, CH, 2, 3); G_A(AN, 2, sl, kk); G_M(AC, CL, CH, 3, 0); G_A(AN, 3, sl, kk); G_M(AC, CL, CH, 3, 1); \
        G_M(AC, CL, CH, 3, 2); G_M(AC, CL, CH, 3, 3); } while (0)
#define G_TILE(sl) do { G_M(AX, XL, XH, 0, 0); G_M(AX, XL, XH, 0, 1); G_REST(AX, XL, XH, AY, YL, YH, sl, 1); \
        G_M(AY, YL, YH, 0, 0); G_M(AY, YL, YH, 0, 1); \
        asm volatile("s_waitcnt vmcnt(0) lgkmcnt(0)" ::: "memory"); BAR; SCHED;        \
        G_REST(AY, YL, YH, AX, XL, XH, ((sl) + 1) & 3, 0); } while (0)
#pragma unroll
            for (int m = 0; m < 4; ++m)
#pragma unroll
                for (int n = 0; n < 4; ++n) { acc[m][n] = (f32x4){0.f, 0.f, 0.f, 0.f}; PIN(acc[m][n]); }
            SCHED;
#pragma unroll
            for (int i = 0; i < 4; ++i) { G_BL(XL, i, 0, 0); G_BH(XH, i, 0, 0); G_A(AX, i, 0, 0); }
            for (int t = 0; t < 32; t += 4) { G_TILE(0); G_TILE(1); G_TILE(2); G_TILE(3); }
            asm volatile("s_nop 7"); SCHED;
            const int fr = lane & 15, fq = lane >> 4;
            const size_t rowo = (size_t)(rt2 * 128 + wm * 64 + kh * 32 + fr) * DM + ct2 * 128 + wn * 32 + (fq & 1) * 16 + (fq >> 1) * 8;
            u32x4 xv[2][2];
#pragma unroll
            for (int m = 0; m < 2; ++m)
#pragma unroll
                for (int s = 0; s < 2; ++s) ST16(pack8(acc[2 + m][2 * s], acc[2 + m][2 * s + 1]), WS_PART + tile * 32768 + kh * 16384 + (m * 2 + s) * 4096 + tid * 16);
#pragma unroll
            for (int m = 0; m < 2; ++m)
#pragma unroll
                for (int s = 0; s < 2; ++s) xv[m][s] = *(const u32x4*)(x + rowo + (size_t)m * 16 * DM + s * 64);
            SCHED;
#define H_REST(AC, CL, CH, AN, NL, NH, sl, kk) do { \
        G_BL(NL, 0, sl, kk); G_BH(NH, 0, sl, kk); G_M(AC, CL, CH, 0, 2); G_A(AN, 0, sl, kk); G_BL(NL, 1, sl, kk); G_M(AC, CL, CH, 0, 3); G_BH(NH, 1, sl, kk); G_BL(NL, 2, sl, kk); G_M(AC, CL, CH, 1, 0); \
        G_BH(NH, 2, sl, kk); G_BL(NL, 3, sl, kk); G_M(AC, CL, CH, 1, 1); G_BH(NH, 3, sl, kk); G_M(AC, CL, CH, 1, 2); G_A(AN, 1, sl, kk); G_M(AC, CL, CH, 1, 3); } while (0)
#define H_TILE(sl, SYNC) do { G_M(AX, XL, XH, 0, 0); G_M(AX, XL, XH, 0, 1); H_REST(AX, XL, XH, AY, YL, YH, sl, 1); \
        G_M(AY, YL, YH, 0, 0); G_M(AY, YL, YH, 0, 1); SYNC; SCHED; H_REST(AY, YL, YH, AX, XL, XH, ((sl) + 1) & 3, 0); } while (0)
#define H_BAR do { WAIT_L(0); BAR; } while (0)
            H_TILE(0, H_BAR); H_TILE(1, H_BAR); H_TILE(2, WAIT_V(0); H_BAR); H_TILE(3, H_BAR);
            H_TILE(0, H_BAR); H_TILE(1, H_BAR); H_TILE(2, H_BAR); H_TILE(3, );
#undef G_BP
#undef G_BL
#undef G_BH
#undef G_A
#undef G_M
#undef G_REST
#undef G_TILE
#undef H_REST
#undef H_TILE
#undef H_BAR
            asm volatile("s_nop 7"); SCHED;
            BAR; CBAR;
            u32x4 pv[2][2];
#pragma unroll
            for (int m = 0; m < 2; ++m)
#pragma unroll
                for (int s = 0; s < 2; ++s) pv[m][s] = *(const LAS u32x4*)(lds + 131072 + (m * 2 + s) * 4096 + tid * 16);
#pragma unroll
            for (int m = 0; m < 2; ++m)
#pragma unroll
                for (int s = 0; s < 2; ++s) { float lo[4], hi4[4]; const u32x4 p_ = pv[m][s], x_ = xv[m][s];
                    const f32x4 a0 = acc[m][2 * s] + (f32x4){bf_lo(p_[0]), bf_hi(p_[0]), bf_lo(p_[1]), bf_hi(p_[1])}, a1 = acc[m][2 * s + 1] + (f32x4){bf_lo(p_[2]), bf_hi(p_[2]), bf_lo(p_[3]), bf_hi(p_[3])};
#pragma unroll
                    for (int i = 0; i < 4; ++i) { auto r = __builtin_amdgcn_permlane16_swap(__float_as_uint(a0[i]), __float_as_uint(a1[i]), false, false);
                        lo[i] = __uint_as_float(r[0]); hi4[i] = __uint_as_float(r[1]); }
                    u32x4 o; o[0] = cvtpk(lo[0] + bf_lo(x_[0]), lo[1] + bf_hi(x_[0])); o[1] = cvtpk(lo[2] + bf_lo(x_[1]), lo[3] + bf_hi(x_[1]));
                    o[2] = cvtpk(hi4[0] + bf_lo(x_[2]), hi4[1] + bf_hi(x_[2])); o[3] = cvtpk(hi4[2] + bf_lo(x_[3]), hi4[3] + bf_hi(x_[3]));
                    __builtin_nontemporal_store(o, (u32x4*)(out + rowo + (size_t)m * 16 * DM + s * 64));
                }
        }
    }
}

extern "C" void kernel_launch(void* const* d_in, const int* in_sizes, int n_in, void* d_out, int out_size, void* d_ws, size_t ws_size, hipStream_t stream) {
    (void)hipFuncSetAttribute((const void*)k_block, hipFuncAttributeMaxDynamicSharedMemorySize, LDS_BYTES);
    k_block<<<256, 512, LDS_BYTES, stream>>>((const u16*)d_in[0], (const u16*)d_in[1], (const u16*)d_in[2], (const u16*)d_in[4], (const u16*)d_in[5], (const u16*)d_in[6], (char*)d_ws, (u16*)d_out);
}
```

```cpp
#include <hip/hip_runtime.h>
#include <hip/hip_bf16.h>
#include <stdint.h>

#define LAS __attribute__((address_space(3)))
#define GAS __attribute__((address_space(1)))
#define DI __device__ __forceinline__
typedef unsigned short u16;
typedef unsigned long long u64;
typedef short bf16x8 __attribute__((ext_vector_type(8)));
typedef short s16x4 __attribute__((ext_vector_type(4)));
typedef float f32x2 __attribute__((ext_vector_type(2)));
typedef float f32x4 __attribute__((ext_vector_type(4)));
typedef float f32x16 __attribute__((ext_vector_type(16)));
typedef unsigned u32x2 __attribute__((ext_vector_type(2)));
typedef unsigned u32x4 __attribute__((ext_vector_type(4)));
typedef __bf16 bf16x2_t __attribute__((ext_vector_type(2)));

constexpr int SEQ = 2048, DM = 1024, FF = 3584;
constexpr int KB_X = DM / 32;
constexpr int KB_AH = (DM + FF) / 32;
constexpr unsigned WS_XN = 0;
constexpr unsigned WS_Q = WS_XN + SEQ * DM * 2;
constexpr unsigned WS_K = WS_Q + SEQ * DM * 2;
constexpr unsigned WS_V = WS_K + SEQ * DM * 2;
constexpr unsigned WS_AH = WS_V + SEQ * DM * 2;
constexpr unsigned WS_PART = WS_AH + SEQ * (DM + FF) * 2;
constexpr unsigned WS_FLAG = WS_PART + 128 * 32768;
constexpr unsigned WS_END = WS_FLAG + 2048 * 128;
constexpr int F_LN = 32, F_QKV = F_LN + 256, F_H = F_QKV + 256, F_AT = F_H + 256, F_G2 = F_AT + 256;
constexpr int LDS_BYTES = 163840;

DI unsigned cvtpk(float lo, float hi) { f32x2 v = {lo, hi}; bf16x2_t b = __builtin_convertvector(v, bf16x2_t); return __builtin_bit_cast(unsigned, b); }
DI float bf_lo(unsigned u) { return __uint_as_float(u << 16); }
DI float bf_hi(unsigned u) { return __uint_as_float(u & 0xffff0000u); }
DI float rbf(float f) { return __uint_as_float(cvtpk(f, f) << 16); }
DI s16x4 trd(const LAS char* p) { return __builtin_amdgcn_ds_read_tr16_b64_v4i16((LAS s16x4*)p); }
DI bf16x8 cat8(s16x4 a, s16x4 b) { return __builtin_shufflevector(a, b, 0, 1, 2, 3, 4, 5, 6, 7); }
template <int OFF, int D> DI void glds(const char* base, unsigned voff, unsigned dst) {
    asm volatile("s_add_u32 m0, %2, %4\n\ts_nop 0\n\tglobal_load_lds_dwordx4 %0, %1 offset:%3" :: "v"(voff), "s"(base), "s"(dst), "i"(OFF), "i"(D - OFF) : "memory", "scc"); }
template <int D> DI void glds_pair_sc1(const char* base, unsigned voff, unsigned dst) {
    asm volatile("s_add_u32 m0, %2, %3\n\ts_nop 0\n\tglobal_load_lds_dwordx4 %0, %1 sc1\n\tglobal_load_lds_dwordx4 %0, %1 offset:1024 sc1" :: "v"(voff), "s"(base), "s"(dst), "i"(D) : "memory", "scc"); }
template <int D> DI void touch(const char* base, unsigned voff, unsigned dst) {
    asm volatile("s_add_u32 m0, %2, %3\n\ts_nop 0\n\tglobal_load_lds_dword %0, %1" :: "v"(voff), "s"(base), "s"(dst), "i"(D) : "memory", "scc"); }
#define WAIT_V(n) asm volatile("s_waitcnt vmcnt(" #n ")" ::: "memory")
#define WAIT_L(n) asm volatile("s_waitcnt lgkmcnt(" #n ")" ::: "memory")
#define BAR __builtin_amdgcn_s_barrier()
#define CBAR asm volatile("" ::: "memory")
#define SCHED __builtin_amdgcn_sched_barrier(0)
#define PIN(x) asm volatile("" : "+v"(x))
#define MFMA16(a, b, c) __builtin_amdgcn_mfma_f32_16x16x32_bf16(a, b, c, 0, 0, 0)
#define MFMA16_ACC(c, a, b) asm volatile("v_mfma_f32_16x16x32_bf16 %0, %1, %2, %0" : "+v"(c) : "v"(a), "v"(b))
#define MFMA32(a, b, c) __builtin_amdgcn_mfma_f32_32x32x16_bf16(a, b, c, 0, 0, 0)
#define ST16(v, off) __builtin_amdgcn_raw_buffer_store_b128(v, rs, (int)(off), 0, 16)
#define LD16(off) __builtin_amdgcn_raw_buffer_load_b128(rs, (int)(off), 0, 16)
DI u64 flag_ld(const GAS u64* p) { return __hip_atomic_load(p, __ATOMIC_RELAXED, __HIP_MEMORY_SCOPE_AGENT); }
DI void flag_st(GAS u64* p, u64 v) { __hip_atomic_store(p, v, __ATOMIC_RELAXED, __HIP_MEMORY_SCOPE_AGENT); }
DI void flag_wait(const GAS u64* p, u64 tag) {
    for (unsigned spins = 0; spins < (1u << 18); ++spins) { if (__all(flag_ld(p) == tag)) break; __builtin_amdgcn_s_sleep(1); }
    CBAR;
}
template <int N> DI float ror_add(float v) { return v + __builtin_bit_cast(float, __builtin_amdgcn_update_dpp(0, __builtin_bit_cast(int, v), 0x120 + N  , 0xf, 0xf, false)); }
DI float sum64(float v) {
    v = ror_add<1>(ror_add<2>(ror_add<4>(ror_add<8>(v))));
    { auto r = __builtin_amdgcn_permlane16_swap(__float_as_uint(v), __float_as_uint(v), false, false); v = __uint_as_float(r[0]) + __uint_as_float(r[1]); }
    { auto r = __builtin_amdgcn_permlane32_swap(__float_as_uint(v), __float_as_uint(v), false, false); v = __uint_as_float(r[0]) + __uint_as_float(r[1]); }
    return v;
}
DI u32x4 widen(u32x2 a, u32x2 b) { auto r0 = __builtin_amdgcn_permlane16_swap(a[0], b[0], false, false); auto r1 = __builtin_amdgcn_permlane16_swap(a[1], b[1], false, false);
    return (u32x4){r0[0], r1[0], r0[1], r1[1]}; }
DI int opaque_tid() { int t = threadIdx.x; asm volatile("" : "+v"(t)); return t; }

DI float silu_mul(float g, float u) { return g * u * __builtin_amdgcn_rcpf(1.0f + __builtin_amdgcn_exp2f(g * -1.4426950408889634f)); }
DI u32x4 pack8(const f32x4& a, const f32x4& b) { u32x4 o; o[0] = cvtpk(a[0], a[1]); o[1] = cvtpk(a[2], a[3]); o[2] = cvtpk(b[0], b[1]); o[3] = cvtpk(b[2], b[3]); return o; }
constexpr float QSCALE = 0.17677669529663687f * 1.4426950408889634f;


struct G128 { const char* a; const char* w2; const char* wo; int kh; };
template <bool HALF> DI void g128_issue(unsigned lds0, const G128& g, int t, int lw, unsigned voffA, unsigned voffB) {
    const unsigned so = lds0 + (t & 3) * 32768, sb = so + lw * 2048;
    const bool att = !HALF && g.kh == 0 && t >= 16;
    const int kt = HALF ? t - 16 : att ? t - 16 : g.kh * 24 + t;
    const char* a = g.a + (size_t)(att ? kt : 16 + kt) * 2048; const char* b = (att ? g.wo : g.w2) + (size_t)kt * (64 * DM * 2);
    if (HALF) glds_pair_sc1<0>(a, voffA, so + (lw >> 1) * 8192 + (lw & 1) * 2048);
    else { glds_pair_sc1<0>(a, voffA, so + lw * 4096); glds_pair_sc1<2048>(a + KB_AH * 1024, voffA, so + lw * 4096); }
    glds<0, 16384>(b, voffB, sb); glds<128, 24576>(b, voffB, sb);
    glds<0, 16384 + 1024>(b + 8 * DM * 2, voffB, sb); glds<128, 24576 + 1024>(b + 8 * DM * 2, voffB, sb);
}

__global__ __launch_bounds__(512, 2) void k_block(const u16* __restrict__ x, const u16* __restrict__ wqkv, const u16* __restrict__ wo, const u16* __restrict__ w1,
                                                  const u16* __restrict__ w3, const u16* __restrict__ w2, char* __restrict__ ws, u16* __restrict__ out) {
    extern __shared__ __attribute__((aligned(16))) char smem[];
    LAS char* lds = (LAS char*)smem;
    const unsigned lds0 = (unsigned)(size_t)lds;
    const int wid = __builtin_amdgcn_readfirstlane(threadIdx.x >> 6);
    const int bid = blockIdx.x, xcd = bid & 7, idx = bid >> 3;
    GAS u64* flags0 = (GAS u64*)(ws + WS_FLAG);
#define FL(i) (flags0 + (i) * 16)
    const __amdgpu_buffer_rsrc_t rs = __builtin_amdgcn_make_buffer_rsrc((void*)ws, 0, WS_END, 0x00020000);
    {
        const char* pc; asm volatile("s_getpc_b64 %0" : "=s"(pc));
        if (wid == 7) touch<148480>(pc + (idx & 7) * 4096, (threadIdx.x & 63) * 64, lds0 + 7 * 1024);
    }
    u64 tag;
    u32x4 xa, xb; u32x2 ep;
    { const char* xp = (const char*)x + (size_t)(bid * 8 + wid) * (DM * 2);
      asm volatile("global_load_dwordx4 %0, %3, %4\n\tglobal_load_dwordx4 %1, %3, %4 offset:1024\n\tglobal_load_dwordx2 %2, %5, %6 sc1"
                   : "=&v"(xa), "=&v"(xb), "=&v"(ep) : "v"((threadIdx.x & 63) * 16), "s"(xp), "v"(0), "s"(ws + WS_FLAG), "s"(wqkv), "s"(w1), "s"(w3) : "memory"); }
    const int cj = idx >> 2, ct = cj < 3 ? (xcd >> 1) * 3 + cj : 12 + (xcd >> 1) * 5 + (cj - 3), rt = (xcd & 1) * 4 + (idx & 3);
    const bool qkv = ct < 12;
    u32x4 hmain[8], hext[2];

    {
        const int tid = opaque_tid(), lane = tid & 63, wr = wid >> 2, wc = wid & 3;
        const unsigned ldb2 = qkv ? 3 * DM * 2 : FF * 2;
        const char* cB0 = qkv ? (const char*)wqkv + ct * 512 : (const char*)w1 + (ct - 12) * 256;
        const char* cB1 = qkv ? cB0 + 256 : (const char*)w3 + (ct - 12) * 256;
        const char* cA = ws + WS_XN + rt * 16 * KB_X * 1024;
        const unsigned voffA = wid * (KB_X * 1024) + lane * 16;
        const unsigned voffB = (unsigned)(wid * 8 + (lane >> 3)) * ldb2 + (((lane & 7) ^ (2 * ((lane >> 4) & 3))) * 16);
        const char* pE = ((lane & 1) ? (const char*)w3 : (const char*)w1) + (size_t)(wid * 8 + (lane >> 3)) * (FF * 2) + (2560 + ct * 32) * 2 + ((((lane >> 1) & 3) ^ ((lane >> 4) & 3)) * 16);
        const size_t kstepB = (size_t)64 * ldb2;
        const unsigned ldsA = lds0 + wid * 2048, ldsB = lds0 + wid * 1024;
        const char* cT = ((idx & 2) ? cB1 : cB0) + (idx & 1) * 128;
#define SA(b, h) (((b) * 2 + (h)) * 16384)
#define SB(b, h) (((b) * 2 + (h)) * 16384)
#define STA(b, h, kt) glds_pair_sc1<SA(b, h)>(cA + (h) * 8 * KB_X * 1024 + (kt) * 2048, voffA, ldsA)
#define STB(b, h, kt) do { const char* s_ = ((h) ? cB1 : cB0) + (size_t)(kt) * kstepB; \
        glds<0, 65536 + SB(b, h)>(s_, voffB, ldsB); glds<128, 65536 + SB(b, h) + 8192>(s_, voffB, ldsB); } while (0)
#define TCH(kt) touch<148480>(cT + (size_t)(kt) * kstepB, voffB, ldsB)
#define STE(b, kt) do { const char* s_ = pE + (size_t)(kt) * (64 * FF * 2); \
        asm volatile("s_add_u32 m0, %1, %2\n\ts_nop 0\n\tglobal_load_lds_dwordx4 %0, off" :: "v"(s_), "s"(ldsB), "i"(131072 + (b) * 8192) : "memory", "scc"); } while (0)
        {
            TCH(0); TCH(1);
            { const char* s_ = pE + (size_t)(idx & 1) * (64 * FF * 2) + (ptrdiff_t)(((idx >> 1) & 1) - (wid >> 2)) * (32 * FF * 2);
              asm volatile("s_add_u32 m0, %1, 148480\n\ts_nop 0\n\tglobal_load_lds_dword %0, off" :: "v"(s_), "s"(ldsB) : "memory", "scc"); }
            asm volatile("s_waitcnt vmcnt(3)" : "+v"(xa), "+v"(xb), "+v"(ep) :: "memory");
            tag = (((u64)(unsigned)__builtin_amdgcn_readfirstlane(ep[1]) << 32) | (unsigned)__builtin_amdgcn_readfirstlane(ep[0])) + 1;
            float f[16];
#pragma unroll
            for (int i = 0; i < 4; ++i) { f[2 * i] = bf_lo(xa[i]); f[2 * i + 1] = bf_hi(xa[i]); f[8 + 2 * i] = bf_lo(xb[i]); f[9 + 2 * i] = bf_hi(xb[i]); }
            float s = 0.f;
#pragma unroll
            for (int i = 0; i < 16; ++i) s += f[i];
            const float mean = sum64(s) * (1.0f / DM);
            float q = 0.f;
#pragma unroll
            for (int i = 0; i < 16; ++i) { const float d = f[i] - mean; q += d * d; }
            const float var = sum64(q) * (1.0f / DM);
            const float mb = rbf(mean), sd = rbf(__builtin_amdgcn_sqrtf(rbf(rbf(var) + 1e-5f))), inv = __builtin_amdgcn_rcpf(sd);
#pragma unroll
            for (int c = 0; c < 2; ++c) { u32x4 o;
#pragma unroll
                for (int i = 0; i < 4; ++i) o[i] = cvtpk(rbf(f[c * 8 + 2 * i] - mb) * inv, rbf(f[c * 8 + 2 * i + 1] - mb) * inv);
                *(LAS u32x4*)(lds + wid * 2080 + c * 1024 + lane * 16) = o; }
            WAIT_L(0); BAR; CBAR;
            const int r = lane & 7, g = (lane >> 3) & 3, row = bid * 8 + r;
#pragma unroll
            for (int c = 0; c < 2; ++c) { const int kb = 4 * wid + 2 * c + (lane >> 5); const LAS char* p = lds + r * 2080 + kb * 64 + g * 8;
                const u32x2 lo = *(const LAS u32x2*)p, hi = *(const LAS u32x2*)(p + 32);
                ST16(((u32x4){lo[0], lo[1], hi[0], hi[1]}), WS_XN + ((row >> 4) * KB_X + kb) * 1024 + (g * 16 + (row & 15)) * 16); }
            CBAR;
            STB(0, 0, 0); STB(0, 1, 0); STE(0, 0);
            WAIT_V(5); BAR;
            if (wid == 0) { if (lane == 0) flag_st(FL(F_LN + bid), tag); flag_wait(FL(F_LN + rt * 32 + (lane & 31)), tag); }
            STB(1, 0, 1); STB(1, 1, 1); STE(1, 1);
            if (wid != 0) { TCH(2); TCH(3); }
            BAR; CBAR;
        }
        f32x4 acc[2][2][4][2], ace[2][4];
        {
            const int g = lane >> 4, q = (lane & 15) >> 2, p = lane & 3, swz = (2 * g + (q >> 1)) & 3;
            const int aoff = wr * 8192 + lane * 16;
            const int boff0 = 65536 + (wc >> 1) * 8192 + (4 * g + q) * 128 + (((2 * (wc & 1)) ^ swz) * 32) + p * 8, boff1 = boff0 ^ 32;
            int eoff = 131072 + (4 * g + q) * 128 + ((((wc >> 1) + 2 * (p & 1)) ^ swz) * 32) + (p >> 1) * 16 + (wc & 1) * 8; PIN(eoff);
#define LDA(dst, b, h) do { _Pragma("unroll") for (int m = 0; m < 4; ++m) _Pragma("unroll") for (int k = 0; k < 2; ++k) \
        dst[m][k] = *(const LAS bf16x8*)(lds + aoff + SA(b, h) + (m * 2 + k) * 1024); } while (0)
#define LDB(dst, b, h) do { _Pragma("unroll") for (int n = 0; n < 2; ++n) _Pragma("unroll") for (int k = 0; k < 2; ++k) { \
        const LAS char* b_ = lds + (n ? boff1 : boff0) + SB(b, h) + k * 4096; dst[n][k] = cat8(trd(b_), trd(b_ + 2048)); } } while (0)
#define LDE(dst, b) do { _Pragma("unroll") for (int k = 0; k < 2; ++k) { const LAS char* b_ = lds + eoff + (b) * 8192 + k * 4096; dst[k] = cat8(trd(b_), trd(b_ + 2048)); } } while (0)
#define MMA(ai) do { __builtin_amdgcn_s_setprio(1); \
        _Pragma("unroll") for (int k = 0; k < 2; ++k) _Pragma("unroll") for (int m = 0; m < 4; ++m) { \
            MFMA16_ACC(acc[ai][0][m][0], B0[0][k], At[m][k]); MFMA16_ACC(acc[ai][0][m][1], B0[1][k], At[m][k]); \
            MFMA16_ACC(acc[ai][1][m][0], B1[0][k], At[m][k]); MFMA16_ACC(acc[ai][1][m][1], B1[1][k], At[m][k]); \
            MFMA16_ACC(ace[ai][m], BE[k], At[m][k]); } \
        __builtin_amdgcn_s_setprio(0); } while (0)
            STA(0, 0, 0); STA(0, 1, 0); STA(1, 0, 1);
#pragma unroll
            for (int a = 0; a < 2; ++a)
#pragma unroll
                for (int m = 0; m < 4; ++m) { ace[a][m] = (f32x4){0.f, 0.f, 0.f, 0.f}; PIN(ace[a][m]);
#pragma unroll
                    for (int b = 0; b < 2; ++b)
#pragma unroll
                        for (int n = 0; n < 2; ++n) { acc[a][b][m][n] = (f32x4){0.f, 0.f, 0.f, 0.f}; PIN(acc[a][b][m][n]); } }
            bf16x8 At[4][2], B0[2][2], B1[2][2], BE[2];
            constexpr int nt = DM / 64;
            if (wr == 1) BAR;
            WAIT_V(4); BAR;
            WAIT_V(2); BAR;
            for (int t = 0; t < nt; t += 2) {
                const bool more = t + 2 < nt, far = t + 4 < nt;
                LDB(B0, 0, 0); LDB(B1, 0, 1); LDE(BE, 0); SCHED; LDA(At, 0, 0); STA(1, 1, t + 1);
                if (far) { TCH(t + 4); WAIT_V(11); } else { WAIT_V(9); }
                WAIT_L(0); BAR; MMA(0); BAR; SCHED;
                LDA(At, 0, 1);
                if (more) { STB(0, 0, t + 2); STB(0, 1, t + 2); STE(0, t + 2); STA(0, 0, t + 2); if (far) { WAIT_V(10); } else { WAIT_V(9); } } else { WAIT_V(2); }
                WAIT_L(0); BAR; MMA(1); BAR; SCHED;
                LDB(B0, 1, 0); LDB(B1, 1, 1); LDE(BE, 1); SCHED; LDA(At, 1, 0);
                if (more) { STA(0, 1, t + 2); if (far) { TCH(t + 5); WAIT_V(11); } else { WAIT_V(9); } } else { WAIT_V(0); }
                WAIT_L(0); BAR; MMA(0); BAR; SCHED;
                LDA(At, 1, 1);
                if (more) { STB(1, 0, t + 3); STB(1, 1, t + 3); STE(1, t + 3); STA(1, 0, t + 3); if (far) { WAIT_V(10); } else { WAIT_V(9); } }
                WAIT_L(0); BAR; MMA(1); if (more || wr == 0) BAR; SCHED;
            }
            asm volatile("s_nop 7"); SCHED;
#undef SA
#undef SB
#undef STA
#undef STB
#undef STE
#undef TCH
#undef LDA
#undef LDB
#undef LDE
#undef MMA
        }
        const int ln = opaque_tid() & 63, fr = ln & 15, fq = ln >> 4;
        if (qkv) {
            const int kind = ct >> 2;
            const float sc = kind == 0 ? QSCALE : 1.0f;
            const unsigned base = WS_Q + kind * (SEQ * DM * 2) + ((ct & 3) * 8 + wc) * 131072 + (rt * 256 + wr * 64) * 64;
            const unsigned lo0 = base + (kind == 2 ? fr * 64 + fq * 16 : fr * 16 + fq * 512), lo1 = lo0 + (kind == 2 ? 1024 : 256);
#pragma unroll
            for (int bj = 0; bj < 2; ++bj)
#pragma unroll
                for (int ai = 0; ai < 2; ++ai)
#pragma unroll
                    for (int m = 0; m < 4; ++m)
                        ST16(pack8(acc[ai][bj][m][0] * sc, acc[ai][bj][m][1] * sc), bj * 4 * 131072 + ai * 8192 + (m >> 1) * 2048 + ((m & 1) ? lo1 : lo0));
            WAIT_V(0); BAR;
            if (wid == 0 && ln == 0) flag_st(FL(F_QKV + rt * 32 + ct), tag);
        } else {
#pragma unroll
            for (int ai = 0; ai < 2; ++ai)
#pragma unroll
                for (int m = 0; m < 4; ++m) { f32x4 h0, h1;
#pragma unroll
                    for (int j = 0; j < 4; ++j) { h0[j] = silu_mul(acc[ai][0][m][0][j], acc[ai][1][m][0][j]); h1[j] = silu_mul(acc[ai][0][m][1][j], acc[ai][1][m][1][j]); }
                    hmain[ai * 4 + m] = pack8(h0, h1);
                }
        }
#pragma unroll
        for (int ai = 0; ai < 2; ++ai) { u32x2 e[2];
#pragma unroll
            for (int mp = 0; mp < 2; ++mp) { float h[4];
#pragma unroll
                for (int j = 0; j < 4; ++j) { auto r = __builtin_amdgcn_permlane32_swap(__float_as_uint(ace[ai][2 * mp][j]), __float_as_uint(ace[ai][2 * mp + 1][j]), false, false);
                    h[j] = silu_mul(__uint_as_float(r[0]), __uint_as_float(r[1])); }
                e[mp] = (u32x2){cvtpk(h[0], h[1]), cvtpk(h[2], h[3])}; }
            hext[ai] = widen(e[0], e[1]); }
    }

    const int kh = xcd & 1, rt2 = ((xcd >> 1) & 1) * 8 + (idx & 7), ct2 = (xcd >> 2) * 4 + (idx >> 3);
    G128 g2; g2.kh = kh; g2.a = ws + WS_AH + rt2 * 8 * KB_AH * 1024; g2.w2 = (const char*)w2 + ct2 * 256; g2.wo = (const char*)wo + ct2 * 256;
#define G2_OFFS(lw_, l_) const unsigned vA = ((lw_) ^ kh) * 2 * KB_AH * 1024 + (l_) * 16, vB = ((lw_) * 16 + ((l_) >> 3)) * (DM * 2) + ((((l_) & 7) ^ (2 * (((l_) >> 4) & 3))) * 16)

    {
        constexpr int AT_BASE = 65536, AT_STAGE = 32768;
        const int lane = opaque_tid() & 63, hi = lane >> 5;
        const int head = xcd * 4 + (idx >> 3), qb = idx & 7;
        if (wid == 0) flag_wait(FL(F_QKV + (lane < 4 ? (lane & 1) * 32 + 4 + (lane >> 1) * 4 : qb * 32) + (head >> 3)), tag);
        BAR; CBAR;
        const char* Kg = ws + WS_K + head * 131072; const char* Vg = ws + WS_V + head * 131072;
        const unsigned soff = wid * 2048 + lane * 16;
        const unsigned sdst = lds0 + AT_BASE + wid * 2048;
#define AT_ISSUE(s, slot) do { glds_pair_sc1<0>(Kg + (s) * 16384, soff, sdst + (slot)); glds_pair_sc1<16384>(Vg + (s) * 16384, soff, sdst + (slot)); } while (0)
        const unsigned qoff = WS_Q + (head * 64 + qb * 8 + wid) * 2048 + lane * 16;
        const bf16x8 q0 = __builtin_bit_cast(bf16x8, LD16(qoff)), q1 = __builtin_bit_cast(bf16x8, LD16(qoff + 1024));
        AT_ISSUE(0, 0); AT_ISSUE(1, AT_STAGE);
        const GAS u64* kvflag = FL(F_QKV + (lane & 7) * 32 + 4 + ((lane >> 3) & 1) * 4 + (head >> 3));
        u64 seen = flag_ld(kvflag);
        bf16x8 sel; { const short one = ((lane & 15) == ((lane >> 4) & 1)) ? (short)0x3f80 : (short)0;
#pragma unroll
            for (int i = 0; i < 8; ++i) sel[i] = one; }
        const LAS char* kb0 = lds + AT_BASE + lane * 16;
        const LAS char* vb0 = lds + AT_BASE + 16384 + (4 * hi + ((lane & 15) >> 2)) * 64 + ((lane >> 4) & 1) * 32 + (lane & 3) * 8;
        f32x16 O, negm, SA, SB; f32x4 L = {0.f, 0.f, 0.f, 0.f};
        bf16x8 k0, k1, v0, v1;
#pragma unroll
        for (int i = 0; i < 16; ++i) { O[i] = 0.f; negm[i] = 0.f; }
        WAIT_V(5); BAR;
        {
            k0 = *(const LAS bf16x8*)(kb0); k1 = *(const LAS bf16x8*)(kb0 + 1024);
            SA = MFMA32(k0, q0, negm); SA = MFMA32(k1, q1, SA);
            k0 = *(const LAS bf16x8*)(kb0 + 2048); k1 = *(const LAS bf16x8*)(kb0 + 3072);
            v0 = cat8(trd(vb0), trd(vb0 + 512)); v1 = cat8(trd(vb0 + 1024), trd(vb0 + 1536));
            float m = SA[0];
#pragma unroll
            for (int i = 1; i < 16; ++i) m = fmaxf(m, SA[i]);
            m = fmaxf(m, __shfl_xor(m, 32));
#pragma unroll
            for (int i = 0; i < 16; ++i) { negm[i] = -m; SA[i] -= m; }
            PIN(negm);
        }
#define EX(S, i) S[i] = __builtin_amdgcn_exp2f(S[i])
#define BC8(x) __builtin_bit_cast(bf16x8, x)
#define AT_STEP(SC, SN, kp, v1p, v0p) do { \
        O = MFMA32(v1, BC8(p1), O); SCHED; v1 = cat8(trd(v1p), trd((v1p) + 512)); EX(SC, 0); EX(SC, 1); EX(SC, 2); PIN(SC); SCHED; \
        SN = MFMA32(k0, q0, negm); SCHED; k0 = *(const LAS bf16x8*)(kp); EX(SC, 3); EX(SC, 4); EX(SC, 5); PIN(SC); SCHED; \
        L = MFMA16(sel, BC8(p1), L); SCHED; EX(SC, 6); EX(SC, 7); \
        p0[0] = cvtpk(SC[0], SC[1]); p0[1] = cvtpk(SC[2], SC[3]); p0[2] = cvtpk(SC[4], SC[5]); p0[3] = cvtpk(SC[6], SC[7]); PIN(p0); SCHED; \
        O = MFMA32(v0, BC8(p0), O); SCHED; v0 = cat8(trd(v0p), trd((v0p) + 512)); EX(SC, 8); EX(SC, 9); EX(SC, 10); PIN(SC); SCHED; \
        SN = MFMA32(k1, q1, SN); SCHED; k1 = *(const LAS bf16x8*)((kp) + 1024); EX(SC, 11); EX(SC, 12); EX(SC, 13); PIN(SC); SCHED; \
        L = MFMA16(sel, BC8(p0), L); SCHED; EX(SC, 14); EX(SC, 15); \
        p1[0] = cvtpk(SC[8], SC[9]); p1[1] = cvtpk(SC[10], SC[11]); p1[2] = cvtpk(SC[12], SC[13]); p1[3] = cvtpk(SC[14], SC[15]); PIN(p1); SCHED; } while (0)
        u32x4 p0, p1 = {0u, 0u, 0u, 0u};
        int cur = 0, nxt = AT_STAGE, fre = 2 * AT_STAGE;
        const GAS u64* hflag = FL(F_H + (rt2 >> 1) * 32 + (lane & 31));
        const unsigned hb = WS_AH + ((rt * 16 + (wid >> 2) * 4) * KB_AH + KB_X + (ct - 12) * 4 + (wid & 3)) * 1024 + lane * 16;
        const unsigned eb = WS_AH + ((rt * 16 + (wid >> 2) * 4 + 2 * ((lane >> 4) & 1) + hi) * KB_AH + KB_X + 80 + ct) * 1024 + ((wid & 3) * 16 + (lane & 15)) * 16;
#define HM(i) ST16(hmain[i], hb + (((i) >> 2) * 8 + ((i) & 3)) * KB_AH * 1024)
#define H_ST(j) do { if (s == 4) ST16(hext[j], eb + (j) * 8 * KB_AH * 1024); else if (!qkv && s < 4) { if (s == 0) HM(j); else if (s == 1) HM(2 + (j)); else if (s == 2) HM(4 + (j)); else HM(6 + (j)); } SCHED; } while (0)
        for (int s = 0; s < 8; ++s) {
            if (s == 1) { if (qkv) { WAIT_V(0); } else { WAIT_V(2); } } else if (s >= 2 && s <= 4) { if (qkv) { WAIT_V(1); } else { WAIT_V(3); } } else if (s == 5) { WAIT_V(3); } else { WAIT_V(0); }
            WAIT_L(0); BAR;
            if (s == 0) { if (!__all(seen == tag)) flag_wait(kvflag, tag); }
            if (s + 2 < 8) AT_ISSUE(s + 2, fre);
            if (s >= 1 && s < 6) {
                const int r0 = ((s - 1) * 256 + idx * 8 + wid) * 2;
                const char* src = (kh ? (const char*)w2 + 1024 * (DM * 2) : r0 >= 1536 ? (const char*)wo - 1536 * (DM * 2) : (const char*)w2) + (size_t)r0 * (DM * 2) + (xcd >> 2) * 1024;
                touch<-AT_BASE>(src, ((lane >> 3) & 1) * (DM * 2) + (lane & 7) * 128, sdst);
            }
            if (s == 6) { if (wid == 0 && lane == 0) flag_st(FL(F_H + rt * 32 + ct), tag); }
            if (s == 7) seen = flag_ld(hflag);
            if (wid >= 4) __builtin_amdgcn_s_setprio(1);
            const LAS char* kc = kb0 + cur; const LAS char* vc = vb0 + cur; const LAS char* kn = kb0 + nxt; const LAS char* vn = vb0 + nxt;
            AT_STEP(SA, SB, kc + 2 * 2048, vc + 1024, vc + 1 * 2048);            AT_STEP(SB, SA, kc + 3 * 2048, vc + 1 * 2048 + 1024, vc + 2 * 2048);
            H_ST(0);
            AT_STEP(SA, SB, kc + 4 * 2048, vc + 2 * 2048 + 1024, vc + 3 * 2048); AT_STEP(SB, SA, kc + 5 * 2048, vc + 3 * 2048 + 1024, vc + 4 * 2048);
            if (s == 7 && wid >= 4) {
                if (!__all(seen == tag)) flag_wait(hflag, tag);
                G2_OFFS(wid - 4, lane);
                g128_issue<false>(lds0, g2, 0, wid - 4, vA, vB); g128_issue<false>(lds0, g2, 1, wid - 4, vA, vB);
            }
            SCHED;
            AT_STEP(SA, SB, kc + 6 * 2048, vc + 4 * 2048 + 1024, vc + 5 * 2048); __builtin_amdgcn_s_setprio(0); AT_STEP(SB, SA, kc + 7 * 2048, vc + 5 * 2048 + 1024, vc + 6 * 2048);
            H_ST(1);
            AT_STEP(SA, SB, kn, vc + 6 * 2048 + 1024, vc + 7 * 2048);            AT_STEP(SB, SA, kn + 2048, vc + 7 * 2048 + 1024, vn);
            const int tmp = cur; cur = nxt; nxt = fre; fre = tmp;
        }
        O = MFMA32(v1, BC8(p1), O); L = MFMA16(sel, BC8(p1), L);
        float lrow; { const float a = __shfl(L[0], lane & 15), b = __shfl(L[1], lane & 15); lrow = (lane & 16) ? b : a; }
        float oabs = 0.f;
#pragma unroll
        for (int i = 0; i < 16; ++i) oabs += fabsf(O[i]);
        if (__any(!(lrow < 1.0e30f) || !(oabs < 3.0e38f))) {
            __builtin_amdgcn_fence(__ATOMIC_ACQUIRE, "agent");
            float m = -INFINITY, l = 0.f;
#pragma unroll
            for (int i = 0; i < 16; ++i) { O[i] = 0.f; negm[i] = 0.f; }
            const u16* Vs = (const u16*)Vg;
            for (int t = 0; t < 64; ++t) { const int key0 = t * 32;
                k0 = *(const bf16x8*)(Kg + key0 * 64 + lane * 16); k1 = *(const bf16x8*)(Kg + key0 * 64 + 1024 + lane * 16);
#pragma unroll
                for (int j = 0; j < 8; ++j) { const int kk = key0 + 8 * (j >> 2) + 4 * hi + (j & 3); v0[j] = (short)Vs[kk * 32 + (lane & 31)]; v1[j] = (short)Vs[(kk + 16) * 32 + (lane & 31)]; }
                f32x16 S = MFMA32(k0, q0, negm); S = MFMA32(k1, q1, S);
                float mt = S[0];
#pragma unroll
                for (int i = 1; i < 16; ++i) mt = fmaxf(mt, S[i]);
                mt = fmaxf(mt, __shfl_xor(mt, 32));
                const float mn = fmaxf(m, mt), al = __builtin_amdgcn_exp2f(m - mn); m = mn;
                float ps = 0.f;
#pragma unroll
                for (int i = 0; i < 16; ++i) { S[i] = rbf(__builtin_amdgcn_exp2f(S[i] - mn)); ps += S[i]; O[i] *= al; }
                l = l * al + ps + __shfl_xor(ps, 32);
#pragma unroll
                for (int i = 0; i < 4; ++i) { p0[i] = cvtpk(S[2 * i], S[2 * i + 1]); p1[i] = cvtpk(S[8 + 2 * i], S[9 + 2 * i]); }
                O = MFMA32(v0, BC8(p0), O); O = MFMA32(v1, BC8(p1), O);
            }
            lrow = l;
        }
        {
            const float inv = 1.0f / lrow;
            unsigned pk[8];
#pragma unroll
            for (int i = 0; i < 8; ++i) pk[i] = cvtpk(O[2 * i] * inv, O[2 * i + 1] * inv);
            const unsigned dst = WS_AH + ((qb * 16 + wid * 2 + ((lane >> 4) & 1)) * KB_AH + head) * 1024 + (lane & 15) * 16 + hi * 256;
#pragma unroll
            for (int c = 0; c < 2; ++c) {
                auto r0 = __builtin_amdgcn_permlane32_swap(pk[4 * c + 0], pk[4 * c + 2], false, false);
                auto r1 = __builtin_amdgcn_permlane32_swap(pk[4 * c + 1], pk[4 * c + 3], false, false);
                ST16(((u32x4){r0[0], r1[0], r0[1], r1[1]}), dst + c * 512);
            }
        }
        if (wid >= 4) { WAIT_V(2); }
        WAIT_L(0); BAR;
#undef AT_ISSUE
#undef AT_STEP
#undef HM
#undef H_ST
#undef EX
#undef BC8
    }

    {
        const int tid = opaque_tid(), lane = tid & 63, tile = rt2 * 8 + ct2;
        GAS u64* myflag = FL(F_G2 + tile * 2 + kh); const GAS u64* pflag = FL(F_G2 + tile * 2 + (kh ^ 1));
        if (wid >= 4) {
            const int lw = wid - 4;
            G2_OFFS(lw, lane);
            const unsigned vH = ((lw >> 1) * 4 + 2 * kh + (lw & 1)) * KB_AH * 1024 + lane * 16;
            g128_issue<false>(lds0, g2, 2, lw, vA, vB); g128_issue<false>(lds0, g2, 3, lw, vA, vB);
            WAIT_V(16);
            for (int t = 0; t < 28; ++t) {
                BAR;
                if (t == 0 && wid == 4 && lane == 0) flag_st(FL(F_AT + (idx & 7) * 32 + xcd * 4 + (idx >> 3)), tag);
                if (t == 12 && kh == 0) flag_wait(FL(F_AT + (rt2 >> 1) * 32 + (lane & 31)), tag);
                g128_issue<false>(lds0, g2, t + 4, lw, vA, vB);
                WAIT_V(16);
            }
            BAR; g128_issue<true>(lds0, g2, 32, lw, vH, vB); WAIT_V(14);
            for (int t = 29; t < 35; ++t) {
                BAR;
                if (t == 34 && wid == 4 && lane == 0) flag_st(myflag, tag);
                g128_issue<true>(lds0, g2, t + 4, lw, vH, vB); WAIT_V(12);
            }
            BAR; g128_issue<true>(lds0, g2, 39, lw, vH, vB);
            const u64 seen = flag_ld(pflag);
            WAIT_V(13);
            BAR; WAIT_V(7);
            BAR; WAIT_V(0);
            const char* psrc = ws + WS_PART + tile * 32768 + (kh ^ 1) * 16384 + lw * 4096;
            const bool early = __all(seen == tag);
            if (early) { glds_pair_sc1<131072>(psrc, lane * 16, lds0 + lw * 4096); glds_pair_sc1<131072 + 2048>(psrc + 2048, lane * 16, lds0 + lw * 4096); }
            BAR;
            if (!early) { flag_wait(pflag, tag); glds_pair_sc1<131072>(psrc, lane * 16, lds0 + lw * 4096); glds_pair_sc1<131072 + 2048>(psrc + 2048, lane * 16, lds0 + lw * 4096); }
            WAIT_V(0); BAR;
            if (bid == 0 && wid == 4) {
#pragma unroll
                for (int i = 0; i < 4; ++i) flag_wait(FL(F_LN + i * 64 + lane), tag);
                if (lane == 0) flag_st(flags0, tag);
            }
        } else {
            const int wm = wid >> 1, wn = wid & 1, g = lane >> 4, q = (lane & 15) >> 2, p = lane & 3, swz = (2 * g + (q >> 1)) & 3;
            const int aoff = wm * 8192 + lane * 16;
            const int boff0 = 16384 + (4 * g + q) * 128 + (((2 * wn) ^ swz) * 32) + p * 8, boff1 = boff0 ^ 32;
            f32x4 acc[4][4]; bf16x8 AX[4], AY[4];
            s16x4 XL[4], XH[4], YL[4], YH[4];
            int pa[2] = {aoff, aoff + 65536}, pb0[2] = {boff0, boff0 + 65536}, pb1[2] = {boff1, boff1 + 65536};
            PIN(pa[1]); PIN(pb0[1]); PIN(pb1[1]);
#define G_BP(n, sl, kk) (lds + (((n) & 1) ? pb1[(sl) >> 1] : pb0[(sl) >> 1]) + ((sl) & 1) * 32768 + ((n) >> 1) * 8192 + (kk) * 4096)
#define G_BL(L_, n, sl, kk) do { L_[n] = trd(G_BP(n, sl, kk)); SCHED; } while (0)
#define G_BH(H_, n, sl, kk) do { H_[n] = trd(G_BP(n, sl, kk) + 2048); SCHED; } while (0)
#define G_A(A_, m, sl, kk) do { A_[m] = *(const LAS bf16x8*)(lds + pa[(sl) >> 1] + ((sl) & 1) * 32768 + ((m) * 2 + (kk)) * 1024); SCHED; } while (0)
#define G_M(A_, L_, H_, m, n) do { MFMA16_ACC(acc[m][n], cat8(L_[n], H_[n]), A_[m]); SCHED; } while (0)
#define G_REST(AC, CL, CH, AN, NL, NH, sl, kk) do { \
        G_BL(NL, 0, sl, kk); G_M(AC, CL, CH, 0, 2); G_BH(NH, 0, sl, kk); G_M(AC, CL, CH, 0, 3); G_A(AN, 0, sl, kk); G_M(AC, CL, CH, 1, 0); G_BL(NL, 1, sl, kk); G_M(AC, CL, CH, 1, 1); \
        G_BH(NH, 1, sl, kk); G_M(AC, CL, CH, 1, 2); G_BL(NL, 2, sl, kk); G_M(AC, CL, CH, 1, 3); G_BH(NH, 2, sl, kk); G_M(AC, CL, CH, 2, 0); G_BL(NL, 3, sl, kk); G_M(AC, CL, CH, 2, 1); \
        G_BH(NH, 3, sl, kk); G_M(AC, CL, CH, 2, 2); G_A(AN, 1, sl, kk); G_M(AC, CL, CH, 2, 3); G_A(AN, 2, sl, kk); G_M(AC, CL, CH, 3, 0); G_A(AN, 3, sl, kk); G_M(AC, CL, CH, 3, 1); \
        G_M(AC, CL, CH, 3, 2); G_M(AC, CL, CH, 3, 3); } while (0)
#define G_TILE(sl) do { G_M(AX, XL, XH, 0, 0); G_M(AX, XL, XH, 0, 1); G_REST(AX, XL, XH, AY, YL, YH, sl, 1); \
        G_M(AY, YL, YH, 0, 0); G_M(AY, YL, YH, 0, 1); \
        asm volatile("s_waitcnt vmcnt(0) lgkmcnt(0)" ::: "memory"); BAR; SCHED;        \
        G_REST(AY, YL, YH, AX, XL, XH, ((sl) + 1) & 3, 0); } while (0)
#pragma unroll
            for (int m = 0; m < 4; ++m)
#pragma unroll
                for (int n = 0; n < 4; ++n) { acc[m][n] = (f32x4){0.f, 0.f, 0.f, 0.f}; PIN(acc[m][n]); }
            SCHED;
#pragma unroll
            for (int i = 0; i < 4; ++i) { G_BL(XL, i, 0, 0); G_BH(XH, i, 0, 0); G_A(AX, i, 0, 0); }
            for (int t = 0; t < 32; t += 4) { G_TILE(0); G_TILE(1); G_TILE(2); G_TILE(3); }
            asm volatile("s_nop 7"); SCHED;
            const int fr = lane & 15, fq = lane >> 4;
            const size_t rowo = (size_t)(rt2 * 128 + wm * 64 + kh * 32 + fr) * DM + ct2 * 128 + wn * 32 + (fq & 1) * 16 + (fq >> 1) * 8;
            u32x4 xv[2][2];
#pragma unroll
            for (int m = 0; m < 2; ++m)
#pragma unroll
                for (int s = 0; s < 2; ++s) ST16(pack8(acc[2 + m][2 * s], acc[2 + m][2 * s + 1]), WS_PART + tile * 32768 + kh * 16384 + (m * 2 + s) * 4096 + tid * 16);
#pragma unroll
            for (int m = 0; m < 2; ++m)
#pragma unroll
                for (int s = 0; s < 2; ++s) xv[m][s] = *(const u32x4*)(x + rowo + (size_t)m * 16 * DM + s * 64);
            SCHED;
#define H_REST(AC, CL, CH, AN, NL, NH, sl, kk) do { \
        G_BL(NL, 0, sl, kk); G_BH(NH, 0, sl, kk); G_M(AC, CL, CH, 0, 2); G_A(AN, 0, sl, kk); G_BL(NL, 1, sl, kk); G_M(AC, CL, CH, 0, 3); G_BH(NH, 1, sl, kk); G_BL(NL, 2, sl, kk); G_M(AC, CL, CH, 1, 0); \
        G_BH(NH, 2, sl, kk); G_BL(NL, 3, sl, kk); G_M(AC, CL, CH, 1, 1); G_BH(NH, 3, sl, kk); G_M(AC, CL, CH, 1, 2); G_A(AN, 1, sl, kk); G_M(AC, CL, CH, 1, 3); } while (0)
#define H_TILE(sl, SYNC) do { G_M(AX, XL, XH, 0, 0); G_M(AX, XL, XH, 0, 1); H_REST(AX, XL, XH, AY, YL, YH, sl, 1); \
        G_M(AY, YL, YH, 0, 0); G_M(AY, YL, YH, 0, 1); SYNC; SCHED; H_REST(AY, YL, YH, AX, XL, XH, ((sl) + 1) & 3, 0); } while (0)
#define H_BAR do { WAIT_L(0); BAR; } while (0)
            H_TILE(0, H_BAR); H_TILE(1, H_BAR); H_TILE(2, WAIT_V(0); H_BAR); H_TILE(3, H_BAR);
            H_TILE(0, H_BAR); H_TILE(1, H_BAR); H_TILE(2, H_BAR); H_TILE(3, );
#undef G_BP
#undef G_BL
#undef G_BH
#undef G_A
#undef G_M
#undef G_REST
#undef G_TILE
#undef H_REST
#undef H_TILE
#undef H_BAR
            asm volatile("s_nop 7"); SCHED;
            BAR; CBAR;
            u32x4 pv[2][2];
#pragma unroll
            for (int m = 0; m < 2; ++m)
#pragma unroll
                for (int s = 0; s < 2; ++s) pv[m][s] = *(const LAS u32x4*)(lds + 131072 + (m * 2 + s) * 4096 + tid * 16);
#pragma unroll
            for (int m = 0; m < 2; ++m)
#pragma unroll
                for (int s = 0; s < 2; ++s) { float lo[4], hi4[4]; const u32x4 p_ = pv[m][s], x_ = xv[m][s];
                    const f32x4 a0 = acc[m][2 * s] + (f32x4){bf_lo(p_[0]), bf_hi(p_[0]), bf_lo(p_[1]), bf_hi(p_[1])}, a1 = acc[m][2 * s + 1] + (f32x4){bf_lo(p_[2]), bf_hi(p_[2]), bf_lo(p_[3]), bf_hi(p_[3])};
#pragma unroll
                    for (int i = 0; i < 4; ++i) { auto r = __builtin_amdgcn_permlane16_swap(__float_as_uint(a0[i]), __float_as_uint(a1[i]), false, false);
                        lo[i] = __uint_as_float(r[0]); hi4[i] = __uint_as_float(r[1]); }
                    u32x4 o; o[0] = cvtpk(lo[0] + bf_lo(x_[0]), lo[1] + bf_hi(x_[0])); o[1] = cvtpk(lo[2] + bf_lo(x_[1]), lo[3] + bf_hi(x_[1]));
                    o[2] = cvtpk(hi4[0] + bf_lo(x_[2]), hi4[1] + bf_hi(x_[2])); o[3] = cvtpk(hi4[2] + bf_lo(x_[3]), hi4[3] + bf_hi(x_[3]));
                    __builtin_nontemporal_store(o, (u32x4*)(out + rowo + (size_t)m * 16 * DM + s * 64));
                }
        }
    }
}

extern "C" void kernel_launch(void* const* d_in, const int* in_sizes, int n_in, void* d_out, int out_size, void* d_ws, size_t ws_size, hipStream_t stream) {
    (void)hipFuncSetAttribute((const void*)k_block, hipFuncAttributeMaxDynamicSharedMemorySize, LDS_BYTES);
    k_block<<<256, 512, LDS_BYTES, stream>>>((const u16*)d_in[0], (const u16*)d_in[1], (const u16*)d_in[2], (const u16*)d_in[4], (const u16*)d_in[5], (const u16*)d_in[6], (char*)d_ws, (u16*)d_out);
}
```

```cpp
#include <hip/hip_runtime.h>
#include <hip/hip_bf16.h>
#include <stdint.h>

#define LAS __attribute__((address_space(3)))
#define GAS __attribute__((address_space(1)))
#define DI __device__ __forceinline__
typedef unsigned short u16;
typedef unsigned long long u64;
typedef short bf16x8 __attribute__((ext_vector_type(8)));
typedef short s16x4 __attribute__((ext_vector_type(4)));
typedef float f32x2 __attribute__((ext_vector_type(2)));
typedef float f32x4 __attribute__((ext_vector_type(4)));
typedef float f32x16 __attribute__((ext_vector_type(16)));
typedef unsigned u32x2 __attribute__((ext_vector_type(2)));
typedef unsigned u32x4 __attribute__((ext_vector_type(4)));
typedef __bf16 bf16x2_t __attribute__((ext_vector_type(2)));

constexpr int SEQ = 2048, DM = 1024, FF = 3584;
constexpr int KB_X = DM / 32;
constexpr int KB_AH = (DM + FF) / 32;
constexpr unsigned WS_XN = 0;
constexpr unsigned WS_Q = WS_XN + SEQ * DM * 2;
constexpr unsigned WS_K = WS_Q + SEQ * DM * 2;
constexpr unsigned WS_V = WS_K + SEQ * DM * 2;
constexpr unsigned WS_AH = WS_V + SEQ * DM * 2;
constexpr unsigned WS_PART = WS_AH + SEQ * (DM + FF) * 2;
constexpr unsigned WS_FLAG = WS_PART + 128 * 32768;
constexpr unsigned WS_END = WS_FLAG + 2048 * 128;
constexpr int F_LN = 32, F_QKV = F_LN + 256, F_H = F_QKV + 256, F_AT = F_H + 256, F_G2 = F_AT + 256;
constexpr int LDS_BYTES = 163840;

DI unsigned cvtpk(float lo, float hi) { f32x2 v = {lo, hi}; bf16x2_t b = __builtin_convertvector(v, bf16x2_t); return __builtin_bit_cast(unsigned, b); }
DI float bf_lo(unsigned u) { return __uint_as_float(u << 16); }
DI float bf_hi(unsigned u) { return __uint_as_float(u & 0xffff0000u); }
DI float rbf(float f) { return __uint_as_float(cvtpk(f, f) << 16); }
DI s16x4 trd(const LAS char* p) { return __builtin_amdgcn_ds_read_tr16_b64_v4i16((LAS s16x4*)p); }
DI bf16x8 cat8(s16x4 a, s16x4 b) { return __builtin_shufflevector(a, b, 0, 1, 2, 3, 4, 5, 6, 7); }
template <int OFF, int D> DI void glds(const char* base, unsigned voff, unsigned dst) {
    asm volatile("s_add_u32 m0, %2, %4\n\ts_nop 0\n\tglobal_load_lds_dwordx4 %0, %1 offset:%3" :: "v"(voff), "s"(base), "s"(dst), "i"(OFF), "i"(D - OFF) : "memory", "scc"); }
template <int D> DI void glds_pair_sc1(const char* base, unsigned voff, unsigned dst) {
    asm volatile("s_add_u32 m0, %2, %3\n\ts_nop 0\n\tglobal_load_lds_dwordx4 %0, %1 sc1\n\tglobal_load_lds_dwordx4 %0, %1 offset:1024 sc1" :: "v"(voff), "s"(base), "s"(dst), "i"(D) : "memory", "scc"); }
template <int D> DI void touch(const char* base, unsigned voff, unsigned dst) {
    asm volatile("s_add_u32 m0, %2, %3\n\ts_nop 0\n\tglobal_load_lds_dword %0, %1" :: "v"(voff), "s"(base), "s"(dst), "i"(D) : "memory", "scc"); }
#define WAIT_V(n) asm volatile("s_waitcnt vmcnt(" #n ")" ::: "memory")
#define WAIT_L(n) asm volatile("s_waitcnt lgkmcnt(" #n ")" ::: "memory")
#define BAR __builtin_amdgcn_s_barrier()
#define CBAR asm volatile("" ::: "memory")
#define SCHED __builtin_amdgcn_sched_barrier(0)
#define PIN(x) asm volatile("" : "+v"(x))
#define MFMA16(a, b, c) __builtin_amdgcn_mfma_f32_16x16x32_bf16(a, b, c, 0, 0, 0)
#define MFMA16_ACC(c, a, b) asm volatile("v_mfma_f32_16x16x32_bf16 %0, %1, %2, %0" : "+v"(c) : "v"(a), "v"(b))
#define MFMA32(a, b, c) __builtin_amdgcn_mfma_f32_32x32x16_bf16(a, b, c, 0, 0, 0)
#define ST16(v, off) __builtin_amdgcn_raw_buffer_store_b128(v, rs, (int)(off), 0, 16)
#define LD16(off) __builtin_amdgcn_raw_buffer_load_b128(rs, (int)(off), 0, 16)
DI u64 flag_ld(const GAS u64* p) { return __hip_atomic_load(p, __ATOMIC_RELAXED, __HIP_MEMORY_SCOPE_AGENT); }
DI void flag_st(GAS u64* p, u64 v) { __hip_atomic_store(p, v, __ATOMIC_RELAXED, __HIP_MEMORY_SCOPE_AGENT); }
DI void flag_wait(const GAS u64* p, u64 tag) {
    for (unsigned spins = 0; spins < (1u << 18); ++spins) { if (__all(flag_ld(p) == tag)) break; __builtin_amdgcn_s_sleep(1); }
    CBAR;
}
template <int N> DI float ror_add(float v) { return v + __builtin_bit_cast(float, __builtin_amdgcn_update_dpp(0, __builtin_bit_cast(int, v), 0x120 + N  , 0xf, 0xf, false)); }
DI float sum64(float v) {
    v = ror_add<1>(ror_add<2>(ror_add<4>(ror_add<8>(v))));
    { auto r = __builtin_amdgcn_permlane16_swap(__float_as_uint(v), __float_as_uint(v), false, false); v = __uint_as_float(r[0]) + __uint_as_float(r[1]); }
    { auto r = __builtin_amdgcn_permlane32_swap(__float_as_uint(v), __float_as_uint(v), false, false); v = __uint_as_float(r[0]) + __uint_as_float(r[1]); }
    return v;
}
DI u32x4 widen(u32x2 a, u32x2 b) { auto r0 = __builtin_amdgcn_permlane16_swap(a[0], b[0], false, false); auto r1 = __builtin_amdgcn_permlane16_swap(a[1], b[1], false, false);
    return (u32x4){r0[0], r1[0], r0[1], r1[1]}; }
DI int opaque_tid() { int t = threadIdx.x; asm volatile("" : "+v"(t)); return t; }

DI float silu_mul(float g, float u) { return g * u * __builtin_amdgcn_rcpf(1.0f + __builtin_amdgcn_exp2f(g * -1.4426950408889634f)); }
DI u32x4 pack8(const f32x4& a, const f32x4& b) { u32x4 o; o[0] = cvtpk(a[0], a[1]); o[1] = cvtpk(a[2], a[3]); o[2] = cvtpk(b[0], b[1]); o[3] = cvtpk(b[2], b[3]); return o; }
constexpr float QSCALE = 0.17677669529663687f * 1.4426950408889634f;


struct G128 { const char* a; const char* w2; const char* wo; int kh; };
template <bool HALF> DI void g128_issue(unsigned lds0, const G128& g, int t, int lw, unsigned voffA, unsigned voffB) {
    const unsigned so = lds0 + (t & 3) * 32768, sb = so + lw * 2048;
    const bool att = !HALF && g.kh == 0 && t >= 16;
    const int kt = HALF ? t - 16 : att ? t - 16 : g.kh * 24 + t;
    const char* a = g.a + (size_t)(att ? kt : 16 + kt) * 2048; const char* b = (att ? g.wo : g.w2) + (size_t)kt * (64 * DM * 2);
    if (HALF) glds_pair_sc1<0>(a, voffA, so + (lw >> 1) * 8192 + (lw & 1) * 2048);
    else { glds_pair_sc1<0>(a, voffA, so + lw * 4096); glds_pair_sc1<2048>(a + KB_AH * 1024, voffA, so + lw * 4096); }
    glds<0, 16384>(b, voffB, sb); glds<128, 24576>(b, voffB, sb);
    glds<0, 16384 + 1024>(b + 8 * DM * 2, voffB, sb); glds<128, 24576 + 1024>(b + 8 * DM * 2, voffB, sb);
}

__global__ __launch_bounds__(512, 2) void k_block(const u16* __restrict__ x, const u16* __restrict__ wqkv, const u16* __restrict__ wo, const u16* __restrict__ w1,
                                                  const u16* __restrict__ w3, const u16* __restrict__ w2, char* __restrict__ ws, u16* __restrict__ out) {
    extern __shared__ __attribute__((aligned(16))) char smem[];
    LAS char* lds = (LAS char*)smem;
    const unsigned lds0 = (unsigned)(size_t)lds;
    const int wid = __builtin_amdgcn_readfirstlane(threadIdx.x >> 6);
    const int bid = blockIdx.x, xcd = bid & 7, idx = bid >> 3;
    GAS u64* flags0 = (GAS u64*)(ws + WS_FLAG);
#define FL(i) (flags0 + (i) * 16)
    const __amdgpu_buffer_rsrc_t rs = __builtin_amdgcn_make_buffer_rsrc((void*)ws, 0, WS_END, 0x00020000);
    {
        const char* pc; asm volatile("s_getpc_b64 %0" : "=s"(pc));
        if (wid == 7) touch<148480>(pc + (idx & 7) * 4096, (threadIdx.x & 63) * 64, lds0 + 7 * 1024);
    }
    u64 tag;
    u32x4 xa, xb; u32x2 ep;
    { const char* xp = (const char*)x + (size_t)(bid * 8 + wid) * (DM * 2);
      asm volatile("global_load_dwordx4 %0, %3, %4\n\tglobal_load_dwordx4 %1, %3, %4 offset:1024\n\tglobal_load_dwordx2 %2, %5, %6 sc1"
                   : "=&v"(xa), "=&v"(xb), "=&v"(ep) : "v"((threadIdx.x & 63) * 16), "s"(xp), "v"(0), "s"(ws + WS_FLAG), "s"(wqkv), "s"(w1), "s"(w3) : "memory"); }
    const int cj = idx >> 2, ct = cj < 3 ? (xcd >> 1) * 3 + cj : 12 + (xcd >> 1) * 5 + (cj - 3), rt = (xcd & 1) * 4 + (idx & 3);
    const bool qkv = ct < 12;
    u32x4 hmain[8], hext[2];

    {
        const int tid = opaque_tid(), lane = tid & 63, wr = wid >> 2, wc = wid & 3;
        const unsigned ldb2 = qkv ? 3 * DM * 2 : FF * 2;
        const char* cB0 = qkv ? (const char*)wqkv + ct * 512 : (const char*)w1 + (ct - 12) * 256;
        const char* cB1 = qkv ? cB0 + 256 : (const char*)w3 + (ct - 12) * 256;
        const char* cA = ws + WS_XN + rt * 16 * KB_X * 1024;
        const unsigned voffA = wid * (KB_X * 1024) + lane * 16;
        const unsigned voffB = (unsigned)(wid * 8 + (lane >> 3)) * ldb2 + (((lane & 7) ^ (2 * ((lane >> 4) & 3))) * 16);
        const char* pE = ((lane & 1) ? (const char*)w3 : (const char*)w1) + (size_t)(wid * 8 + (lane >> 3)) * (FF * 2) + (2560 + ct * 32) * 2 + ((((lane >> 1) & 3) ^ ((lane >> 4) & 3)) * 16);
        const size_t kstepB = (size_t)64 * ldb2;
        const unsigned ldsA = lds0 + wid * 2048, ldsB = lds0 + wid * 1024;
        const char* cT = ((idx & 2) ? cB1 : cB0) + (idx & 1) * 128;
#define SA(b, h) (((b) * 2 + (h)) * 16384)
#define SB(b, h) (((b) * 2 + (h)) * 16384)
#define STA(b, h, kt) glds_pair_sc1<SA(b, h)>(cA + (h) * 8 * KB_X * 1024 + (kt) * 2048, voffA, ldsA)
#define STB(b, h, kt) do { const char* s_ = ((h) ? cB1 : cB0) + (size_t)(kt) * kstepB; \
        glds<0, 65536 + SB(b, h)>(s_, voffB, ldsB); glds<128, 65536 + SB(b, h) + 8192>(s_, voffB, ldsB); } while (0)
#define TCH(kt) touch<148480>(cT + (size_t)(kt) * kstepB, voffB, ldsB)
#define STE(b, kt) do { const char* s_ = pE + (size_t)(kt) * (64 * FF * 2); \
        asm volatile("s_add_u32 m0, %1, %2\n\ts_nop 0\n\tglobal_load_lds_dwordx4 %0, off" :: "v"(s_), "s"(ldsB), "i"(131072 + (b) * 8192) : "memory", "scc"); } while (0)
        {
            TCH(0); TCH(1);
            { const char* s_ = pE + (size_t)(idx & 1) * (64 * FF * 2) + (ptrdiff_t)(((idx >> 1) & 1) - (wid >> 2)) * (32 * FF * 2);
              asm volatile("s_add_u32 m0, %1, 148480\n\ts_nop 0\n\tglobal_load_lds_dword %0, off" :: "v"(s_), "s"(ldsB) : "memory", "scc"); }
            asm volatile("s_waitcnt vmcnt(3)" : "+v"(xa), "+v"(xb), "+v"(ep) :: "memory");
            tag = (((u64)(unsigned)__builtin_amdgcn_readfirstlane(ep[1]) << 32) | (unsigned)__builtin_amdgcn_readfirstlane(ep[0])) + 1;
            float f[16];
#pragma unroll
            for (int i = 0; i < 4; ++i) { f[2 * i] = bf_lo(xa[i]); f[2 * i + 1] = bf_hi(xa[i]); f[8 + 2 * i] = bf_lo(xb[i]); f[9 + 2 * i] = bf_hi(xb[i]); }
            float s = 0.f;
#pragma unroll
            for (int i = 0; i < 16; ++i) s += f[i];
            const float mean = sum64(s) * (1.0f / DM);
            float q = 0.f;
#pragma unroll
            for (int i = 0; i < 16; ++i) { const float d = f[i] - mean; q += d * d; }
            const float var = sum64(q) * (1.0f / DM);
            const float mb = rbf(mean), sd = rbf(__builtin_amdgcn_sqrtf(rbf(rbf(var) + 1e-5f))), inv = __builtin_amdgcn_rcpf(sd);
#pragma unroll
            for (int c = 0; c < 2; ++c) { u32x4 o;
#pragma unroll
                for (int i = 0; i < 4; ++i) o[i] = cvtpk(rbf(f[c * 8 + 2 * i] - mb) * inv, rbf(f[c * 8 + 2 * i + 1] - mb) * inv);
                *(LAS u32x4*)(lds + wid * 2080 + c * 1024 + lane * 16) = o; }
            WAIT_L(0); BAR; CBAR;
            const int r = lane & 7, g = (lane >> 3) & 3, row = bid * 8 + r;
#pragma unroll
            for (int c = 0; c < 2; ++c) { const int kb = 4 * wid + 2 * c + (lane >> 5); const LAS char* p = lds + r * 2080 + kb * 64 + g * 8;
                const u32x2 lo = *(const LAS u32x2*)p, hi = *(const LAS u32x2*)(p + 32);
                ST16(((u32x4){lo[0], lo[1], hi[0], hi[1]}), WS_XN + ((row >> 4) * KB_X + kb) * 1024 + (g * 16 + (row & 15)) * 16); }
            CBAR;
            STB(0, 0, 0); STB(0, 1, 0); STE(0, 0);
            WAIT_V(5); BAR;
            if (wid == 0) { if (lane == 0) flag_st(FL(F_LN + bid), tag); flag_wait(FL(F_LN + rt * 32 + (lane & 31)), tag); }
            STB(1, 0, 1); STB(1, 1, 1); STE(1, 1);
            if (wid != 0) { TCH(2); TCH(3); }
            BAR; CBAR;
        }
        f32x4 acc[2][2][4][2], ace[2][4];
        {
            const int g = lane >> 4, q = (lane & 15) >> 2, p = lane & 3, swz = (2 * g + (q >> 1)) & 3;
            const int aoff = wr * 8192 + lane * 16;
            const int boff0 = 65536 + (wc >> 1) * 8192 + (4 * g + q) * 128 + (((2 * (wc & 1)) ^ swz) * 32) + p * 8, boff1 = boff0 ^ 32;
            int eoff = 131072 + (4 * g + q) * 128 + ((((wc >> 1) + 2 * (p & 1)) ^ swz) * 32) + (p >> 1) * 16 + (wc & 1) * 8; PIN(eoff);
#define LDA(dst, b, h) do { _Pragma("unroll") for (int m = 0; m < 4; ++m) _Pragma("unroll") for (int k = 0; k < 2; ++k) \
        dst[m][k] = *(const LAS bf16x8*)(lds + aoff + SA(b, h) + (m * 2 + k) * 1024); } while (0)
#define LDB(dst, b, h) do { _Pragma("unroll") for (int n = 0; n < 2; ++n) _Pragma("unroll") for (int k = 0; k < 2; ++k) { \
        const LAS char* b_ = lds + (n ? boff1 : boff0) + SB(b, h) + k * 4096; dst[n][k] = cat8(trd(b_), trd(b_ + 2048)); } } while (0)
#define LDE(dst, b) do { _Pragma("unroll") for (int k = 0; k < 2; ++k) { const LAS char* b_ = lds + eoff + (b) * 8192 + k * 4096; dst[k] = cat8(trd(b_), trd(b_ + 2048)); } } while (0)
#define MMA(ai) do { __builtin_amdgcn_s_setprio(1); \
        _Pragma("unroll") for (int k = 0; k < 2; ++k) _Pragma("unroll") for (int m = 0; m < 4; ++m) { \
            MFMA16_ACC(acc[ai][0][m][0], B0[0][k], At[m][k]); MFMA16_ACC(acc[ai][0][m][1], B0[1][k], At[m][k]); \
            MFMA16_ACC(acc[ai][1][m][0], B1[0][k], At[m][k]); MFMA16_ACC(acc[ai][1][m][1], B1[1][k], At[m][k]); \
            MFMA16_ACC(ace[ai][m], BE[k], At[m][k]); } \
        __builtin_amdgcn_s_setprio(0); } while (0)
            STA(0, 0, 0); STA(0, 1, 0); STA(1, 0, 1);
#pragma unroll
            for (int a = 0; a < 2; ++a)
#pragma unroll
                for (int m = 0; m < 4; ++m) { ace[a][m] = (f32x4){0.f, 0.f, 0.f, 0.f}; PIN(ace[a][m]);
#pragma unroll
                    for (int b = 0; b < 2; ++b)
#pragma unroll
                        for (int n = 0; n < 2; ++n) { acc[a][b][m][n] = (f32x4){0.f, 0.f, 0.f, 0.f}; PIN(acc[a][b][m][n]); } }
            bf16x8 At[4][2], B0[2][2], B1[2][2], BE[2];
            constexpr int nt = DM / 64;
            if (wr == 1) BAR;
            WAIT_V(4); BAR;
            WAIT_V(2); BAR;
            for (int t = 0; t < nt; t += 2) {
                const bool more = t + 2 < nt, far = t + 4 < nt;
                LDB(B0, 0, 0); LDB(B1, 0, 1); LDE(BE, 0); SCHED; LDA(At, 0, 0); STA(1, 1, t + 1);
                if (far) { TCH(t + 4); WAIT_V(11); } else { WAIT_V(9); }
                WAIT_L(0); BAR; MMA(0); BAR; SCHED;
                LDA(At, 0, 1);
                if (more) { STB(0, 0, t + 2); STB(0, 1, t + 2); STE(0, t + 2); STA(0, 0, t + 2); if (far) { WAIT_V(10); } else { WAIT_V(9); } } else { WAIT_V(2); }
                WAIT_L(0); BAR; MMA(1); BAR; SCHED;
                LDB(B0, 1, 0); LDB(B1, 1, 1); LDE(BE, 1); SCHED; LDA(At, 1, 0);
                if (more) { STA(0, 1, t + 2); if (far) { TCH(t + 5); WAIT_V(11); } else { WAIT_V(9); } } else { WAIT_V(0); }
                WAIT_L(0); BAR; MMA(0); BAR; SCHED;
                LDA(At, 1, 1);
                if (more) { STB(1, 0, t + 3); STB(1, 1, t + 3); STE(1, t + 3); STA(1, 0, t + 3); if (far) { WAIT_V(10); } else { WAIT_V(9); } }
                WAIT_L(0); BAR; MMA(1); if (more || wr == 0) BAR; SCHED;
            }
            asm volatile("s_nop 7"); SCHED;
#undef SA
#undef SB
#undef STA
#undef STB
#undef STE
#undef TCH
#undef LDA
#undef LDB
#undef LDE
#undef MMA
        }
        const int ln = opaque_tid() & 63, fr = ln & 15, fq = ln >> 4;
        if (qkv) {
            const int kind = ct >> 2;
            const float sc = kind == 0 ? QSCALE : 1.0f;
            const unsigned base = WS_Q + kind * (SEQ * DM * 2) + ((ct & 3) * 8 + wc) * 131072 + (rt * 256 + wr * 64) * 64;
            const unsigned lo0 = base + (kind == 2 ? fr * 64 + fq * 16 : fr * 16 + fq * 512), lo1 = lo0 + (kind == 2 ? 1024 : 256);
#pragma unroll
            for (int bj = 0; bj < 2; ++bj)
#pragma unroll
                for (int ai = 0; ai < 2; ++ai)
#pragma unroll
                    for (int m = 0; m < 4; ++m)
                        ST16(pack8(acc[ai][bj][m][0] * sc, acc[ai][bj][m][1] * sc), bj * 4 * 131072 + ai * 8192 + (m >> 1) * 2048 + ((m & 1) ? lo1 : lo0));
            WAIT_V(0); BAR;
            if (wid == 0 && ln == 0) flag_st(FL(F_QKV + rt * 32 + ct), tag);
        } else {
#pragma unroll
            for (int ai = 0; ai < 2; ++ai)
#pragma unroll
                for (int m = 0; m < 4; ++m) { f32x4 h0, h1;
#pragma unroll
                    for (int j = 0; j < 4; ++j) { h0[j] = silu_mul(acc[ai][0][m][0][j], acc[ai][1][m][0][j]); h1[j] = silu_mul(acc[ai][0][m][1][j], acc[ai][1][m][1][j]); }
                    hmain[ai * 4 + m] = pack8(h0, h1);
                }
        }
#pragma unroll
        for (int ai = 0; ai < 2; ++ai) { u32x2 e[2];
#pragma unroll
            for (int mp = 0; mp < 2; ++mp) { float h[4];
#pragma unroll
                for (int j = 0; j < 4; ++j) { auto r = __builtin_amdgcn_permlane32_swap(__float_as_uint(ace[ai][2 * mp][j]), __float_as_uint(ace[ai][2 * mp + 1][j]), false, false);
                    h[j] = silu_mul(__uint_as_float(r[0]), __uint_as_float(r[1])); }
                e[mp] = (u32x2){cvtpk(h[0], h[1]), cvtpk(h[2], h[3])}; }
            hext[ai] = widen(e[0], e[1]); }
    }

    const int kh = xcd & 1, rt2 = ((xcd >> 1) & 1) * 8 + (idx & 7), ct2 = (xcd >> 2) * 4 + (idx >> 3);
    G128 g2; g2.kh = kh; g2.a = ws + WS_AH + rt2 * 8 * KB_AH * 1024; g2.w2 = (const char*)w2 + ct2 * 256; g2.wo = (const char*)wo + ct2 * 256;
#define G2_OFFS(lw_, l_) const unsigned vA = ((lw_) ^ kh) * 2 * KB_AH * 1024 + (l_) * 16, vB = ((lw_) * 16 + ((l_) >> 3)) * (DM * 2) + ((((l_) & 7) ^ (2 * (((l_) >> 4) & 3))) * 16)

    {
        constexpr int AT_BASE = 65536, AT_STAGE = 32768;
        const int lane = opaque_tid() & 63, hi = lane >> 5;
        const int head = xcd * 4 + (idx >> 3), qb = idx & 7;
        if (wid == 0) flag_wait(FL(F_QKV + (lane < 4 ? (lane & 1) * 32 + 4 + (lane >> 1) * 4 : qb * 32) + (head >> 3)), tag);
        BAR; CBAR;
        const char* Kg = ws + WS_K + head * 131072; const char* Vg = ws + WS_V + head * 131072;
        const unsigned soff = wid * 2048 + lane * 16;
        const unsigned sdst = lds0 + AT_BASE + wid * 2048;
#define AT_ISSUE(s, slot) do { glds_pair_sc1<0>(Kg + (s) * 16384, soff, sdst + (slot)); glds_pair_sc1<16384>(Vg + (s) * 16384, soff, sdst + (slot)); } while (0)
        const unsigned qoff = WS_Q + (head * 64 + qb * 8 + wid) * 2048 + lane * 16;
        const bf16x8 q0 = __builtin_bit_cast(bf16x8, LD16(qoff)), q1 = __builtin_bit_cast(bf16x8, LD16(qoff + 1024));
        AT_ISSUE(0, 0); AT_ISSUE(1, AT_STAGE);
        const GAS u64* kvflag = FL(F_QKV + (lane & 7) * 32 + 4 + ((lane >> 3) & 1) * 4 + (head >> 3));
        u64 seen = flag_ld(kvflag);
        bf16x8 sel; { const short one = ((lane & 15) == ((lane >> 4) & 1)) ? (short)0x3f80 : (short)0;
#pragma unroll
            for (int i = 0; i < 8; ++i) sel[i] = one; }
        const LAS char* kb0 = lds + AT_BASE + lane * 16;
        const LAS char* vb0 = lds + AT_BASE + 16384 + (4 * hi + ((lane & 15) >> 2)) * 64 + ((lane >> 4) & 1) * 32 + (lane & 3) * 8;
        f32x16 O, negm, SA, SB; f32x4 L = {0.f, 0.f, 0.f, 0.f};
        bf16x8 k0, k1, v0, v1;
#pragma unroll
        for (int i = 0; i < 16; ++i) { O[i] = 0.f; negm[i] = 0.f; }
        WAIT_V(5); BAR;
        {
            k0 = *(const LAS bf16x8*)(kb0); k1 = *(const LAS bf16x8*)(kb0 + 1024);
            SA = MFMA32(k0, q0, negm); SA = MFMA32(k1, q1, SA);
            k0 = *(const LAS bf16x8*)(kb0 + 2048); k1 = *(const LAS bf16x8*)(kb0 + 3072);
            v0 = cat8(trd(vb0), trd(vb0 + 512)); v1 = cat8(trd(vb0 + 1024), trd(vb0 + 1536));
            float m = SA[0];
#pragma unroll
            for (int i = 1; i < 16; ++i) m = fmaxf(m, SA[i]);
            m = fmaxf(m, __shfl_xor(m, 32));
#pragma unroll
            for (int i = 0; i < 16; ++i) { negm[i] = -m; SA[i] -= m; }
            PIN(negm);
        }
#define EX(S, i) S[i] = __builtin_amdgcn_exp2f(S[i])
#define BC8(x) __builtin_bit_cast(bf16x8, x)
#define AT_STEP(SC, SN, kp, v1p, v0p) do { \
        O = MFMA32(v1, BC8(p1), O); SCHED; v1 = cat8(trd(v1p), trd((v1p) + 512)); EX(SC, 0); EX(SC, 1); EX(SC, 2); PIN(SC); SCHED; \
        SN = MFMA32(k0, q0, negm); SCHED; k0 = *(const LAS bf16x8*)(kp); EX(SC, 3); EX(SC, 4); EX(SC, 5); PIN(SC); SCHED; \
        L = MFMA16(sel, BC8(p1), L); SCHED; EX(SC, 6); EX(SC, 7); \
        p0[0] = cvtpk(SC[0], SC[1]); p0[1] = cvtpk(SC[2], SC[3]); p0[2] = cvtpk(SC[4], SC[5]); p0[3] = cvtpk(SC[6], SC[7]); PIN(p0); SCHED; \
        O = MFMA32(v0, BC8(p0), O); SCHED; v0 = cat8(trd(v0p), trd((v0p) + 512)); EX(SC, 8); EX(SC, 9); EX(SC, 10); PIN(SC); SCHED; \
        SN = MFMA32(k1, q1, SN); SCHED; k1 = *(const LAS bf16x8*)((kp) + 1024); EX(SC, 11); EX(SC, 12); EX(SC, 13); PIN(SC); SCHED; \
        L = MFMA16(sel, BC8(p0), L); SCHED; EX(SC, 14); EX(SC, 15); \
        p1[0] = cvtpk(SC[8], SC[9]); p1[1] = cvtpk(SC[10], SC[11]); p1[2] = cvtpk(SC[12], SC[13]); p1[3] = cvtpk(SC[14], SC[15]); PIN(p1); SCHED; } while (0)
        u32x4 p0, p1 = {0u, 0u, 0u, 0u};
        int cur = 0, nxt = AT_STAGE, fre = 2 * AT_STAGE;
        const GAS u64* hflag = FL(F_H + (rt2 >> 1) * 32 + (lane & 31));
        const unsigned hb = WS_AH + ((rt * 16 + (wid >> 2) * 4) * KB_AH + KB_X + (ct - 12) * 4 + (wid & 3)) * 1024 + lane * 16;
        const unsigned eb = WS_AH + ((rt * 16 + (wid >> 2) * 4 + 2 * ((lane >> 4) & 1) + hi) * KB_AH + KB_X + 80 + ct) * 1024 + ((wid & 3) * 16 + (lane & 15)) * 16;
#define HM(i) ST16(hmain[i], hb + (((i) >> 2) * 8 + ((i) & 3)) * KB_AH * 1024)
#define H_ST(j) do { if (s == 4) ST16(hext[j], eb + (j) * 8 * KB_AH * 1024); else if (!qkv && s < 4) { if (s == 0) HM(j); else if (s == 1) HM(2 + (j)); else if (s == 2) HM(4 + (j)); else HM(6 + (j)); } SCHED; } while (0)
        for (int s = 0; s < 8; ++s) {
            if (s == 1) { if (qkv) { WAIT_V(0); } else { WAIT_V(2); } } else if (s >= 2 && s <= 4) { if (qkv) { WAIT_V(1); } else { WAIT_V(3); } } else if (s == 5) { WAIT_V(3); } else { WAIT_V(0); }
            WAIT_L(0); BAR;
            if (s == 0) { if (!__all(seen == tag)) flag_wait(kvflag, tag); }
            if (s + 2 < 8) glds_pair_sc1<0>(Kg + (s + 2) * 16384, soff, sdst + fre);
            if (s == 6) { if (wid == 0 && lane == 0) flag_st(FL(F_H + rt * 32 + ct), tag); }
            if (s == 7) seen = flag_ld(hflag);
            if (wid >= 4) __builtin_amdgcn_s_setprio(1);
            const LAS char* kc = kb0 + cur; const LAS char* vc = vb0 + cur; const LAS char* kn = kb0 + nxt; const LAS char* vn = vb0 + nxt;
            AT_STEP(SA, SB, kc + 2 * 2048, vc + 1024, vc + 1 * 2048);
            if (s + 2 < 8) glds_pair_sc1<16384>(Vg + (s + 2) * 16384, soff, sdst + fre);
            SCHED;
            AT_STEP(SB, SA, kc + 3 * 2048, vc + 1 * 2048 + 1024, vc + 2 * 2048);
            H_ST(0);
            AT_STEP(SA, SB, kc + 4 * 2048, vc + 2 * 2048 + 1024, vc + 3 * 2048); AT_STEP(SB, SA, kc + 5 * 2048, vc + 3 * 2048 + 1024, vc + 4 * 2048);
            if (s >= 1 && s < 6) {
                const int r0 = ((s - 1) * 256 + idx * 8 + wid) * 2;
                const char* src = (kh ? (const char*)w2 + 1024 * (DM * 2) : r0 >= 1536 ? (const char*)wo - 1536 * (DM * 2) : (const char*)w2) + (size_t)r0 * (DM * 2) + (xcd >> 2) * 1024;
                touch<-AT_BASE>(src, ((lane >> 3) & 1) * (DM * 2) + (lane & 7) * 128, sdst);
            }
            if (s == 7 && wid >= 4) {
                if (!__all(seen == tag)) flag_wait(hflag, tag);
                G2_OFFS(wid - 4, lane);
                g128_issue<false>(lds0, g2, 0, wid - 4, vA, vB); g128_issue<false>(lds0, g2, 1, wid - 4, vA, vB);
            }
            SCHED;
            AT_STEP(SA, SB, kc + 6 * 2048, vc + 4 * 2048 + 1024, vc + 5 * 2048); __builtin_amdgcn_s_setprio(0); AT_STEP(SB, SA, kc + 7 * 2048, vc + 5 * 2048 + 1024, vc + 6 * 2048);
            H_ST(1);
            AT_STEP(SA, SB, kn, vc + 6 * 2048 + 1024, vc + 7 * 2048);            AT_STEP(SB, SA, kn + 2048, vc + 7 * 2048 + 1024, vn);
            const int tmp = cur; cur = nxt; nxt = fre; fre = tmp;
        }
        O = MFMA32(v1, BC8(p1), O); L = MFMA16(sel, BC8(p1), L);
        float lrow; { const float a = __shfl(L[0], lane & 15), b = __shfl(L[1], lane & 15); lrow = (lane & 16) ? b : a; }
        float oabs = 0.f;
#pragma unroll
        for (int i = 0; i < 16; ++i) oabs += fabsf(O[i]);
        if (__any(!(lrow < 1.0e30f) || !(oabs < 3.0e38f))) {
            __builtin_amdgcn_fence(__ATOMIC_ACQUIRE, "agent");
            float m = -INFINITY, l = 0.f;
#pragma unroll
            for (int i = 0; i < 16; ++i) { O[i] = 0.f; negm[i] = 0.f; }
            const u16* Vs = (const u16*)Vg;
            for (int t = 0; t < 64; ++t) { const int key0 = t * 32;
                k0 = *(const bf16x8*)(Kg + key0 * 64 + lane * 16); k1 = *(const bf16x8*)(Kg + key0 * 64 + 1024 + lane * 16);
#pragma unroll
                for (int j = 0; j < 8; ++j) { const int kk = key0 + 8 * (j >> 2) + 4 * hi + (j & 3); v0[j] = (short)Vs[kk * 32 + (lane & 31)]; v1[j] = (short)Vs[(kk + 16) * 32 + (lane & 31)]; }
                f32x16 S = MFMA32(k0, q0, negm); S = MFMA32(k1, q1, S);
                float mt = S[0];
#pragma unroll
                for (int i = 1; i < 16; ++i) mt = fmaxf(mt, S[i]);
                mt = fmaxf(mt, __shfl_xor(mt, 32));
                const float mn = fmaxf(m, mt), al = __builtin_amdgcn_exp2f(m - mn); m = mn;
                float ps = 0.f;
#pragma unroll
                for (int i = 0; i < 16; ++i) { S[i] = rbf(__builtin_amdgcn_exp2f(S[i] - mn)); ps += S[i]; O[i] *= al; }
                l = l * al + ps + __shfl_xor(ps, 32);
#pragma unroll
                for (int i = 0; i < 4; ++i) { p0[i] = cvtpk(S[2 * i], S[2 * i + 1]); p1[i] = cvtpk(S[8 + 2 * i], S[9 + 2 * i]); }
                O = MFMA32(v0, BC8(p0), O); O = MFMA32(v1, BC8(p1), O);
            }
            lrow = l;
        }
        {
            const float inv = 1.0f / lrow;
            unsigned pk[8];
#pragma unroll
            for (int i = 0; i < 8; ++i) pk[i] = cvtpk(O[2 * i] * inv, O[2 * i + 1] * inv);
            const unsigned dst = WS_AH + ((qb * 16 + wid * 2 + ((lane >> 4) & 1)) * KB_AH + head) * 1024 + (lane & 15) * 16 + hi * 256;
#pragma unroll
            for (int c = 0; c < 2; ++c) {
                auto r0 = __builtin_amdgcn_permlane32_swap(pk[4 * c + 0], pk[4 * c + 2], false, false);
                auto r1 = __builtin_amdgcn_permlane32_swap(pk[4 * c + 1], pk[4 * c + 3], false, false);
                ST16(((u32x4){r0[0], r1[0], r0[1], r1[1]}), dst + c * 512);
            }
        }
        if (wid >= 4) { WAIT_V(2); }
        WAIT_L(0); BAR;
#undef AT_ISSUE
#undef AT_STEP
#undef HM
#undef H_ST
#undef EX
#undef BC8
    }

    {
        const int tid = opaque_tid(), lane = tid & 63, tile = rt2 * 8 + ct2;
        GAS u64* myflag = FL(F_G2 + tile * 2 + kh); const GAS u64* pflag = FL(F_G2 + tile * 2 + (kh ^ 1));
        if (wid >= 4) {
            const int lw = wid - 4;
            G2_OFFS(lw, lane);
            const unsigned vH = ((lw >> 1) * 4 + 2 * kh + (lw & 1)) * KB_AH * 1024 + lane * 16;
            g128_issue<false>(lds0, g2, 2, lw, vA, vB); g128_issue<false>(lds0, g2, 3, lw, vA, vB);
            WAIT_V(16);
            for (int t = 0; t < 28; ++t) {
                BAR;
                if (t == 0 && wid == 4 && lane == 0) flag_st(FL(F_AT + (idx & 7) * 32 + xcd * 4 + (idx >> 3)), tag);
                if (t == 12 && kh == 0) flag_wait(FL(F_AT + (rt2 >> 1) * 32 + (lane & 31)), tag);
                g128_issue<false>(lds0, g2, t + 4, lw, vA, vB);
                WAIT_V(16);
            }
            BAR; g128_issue<true>(lds0, g2, 32, lw, vH, vB); WAIT_V(14);
            for (int t = 29; t < 35; ++t) {
                BAR;
                if (t == 34 && wid == 4 && lane == 0) flag_st(myflag, tag);
                g128_issue<true>(lds0, g2, t + 4, lw, vH, vB); WAIT_V(12);
            }
            BAR; g128_issue<true>(lds0, g2, 39, lw, vH, vB);
            const u64 seen = flag_ld(pflag);
            WAIT_V(13);
            BAR; WAIT_V(7);
            BAR; WAIT_V(0);
            const char* psrc = ws + WS_PART + tile * 32768 + (kh ^ 1) * 16384 + lw * 4096;
            const bool early = __all(seen == tag);
            if (early) { glds_pair_sc1<131072>(psrc, lane * 16, lds0 + lw * 4096); glds_pair_sc1<131072 + 2048>(psrc + 2048, lane * 16, lds0 + lw * 4096); }
            BAR;
            if (!early) { flag_wait(pflag, tag); glds_pair_sc1<131072>(psrc, lane * 16, lds0 + lw * 4096); glds_pair_sc1<131072 + 2048>(psrc + 2048, lane * 16, lds0 + lw * 4096); }
            WAIT_V(0); BAR;
            if (bid == 0 && wid == 4) {
#pragma unroll
                for (int i = 0; i < 4; ++i) flag_wait(FL(F_LN + i * 64 + lane), tag);
                if (lane == 0) flag_st(flags0, tag);
            }
        } else {
            const int wm = wid >> 1, wn = wid & 1, g = lane >> 4, q = (lane & 15) >> 2, p = lane & 3, swz = (2 * g + (q >> 1)) & 3;
            const int aoff = wm * 8192 + lane * 16;
            const int boff0 = 16384 + (4 * g + q) * 128 + (((2 * wn) ^ swz) * 32) + p * 8, boff1 = boff0 ^ 32;
            f32x4 acc[4][4]; bf16x8 AX[4], AY[4];
            s16x4 XL[4], XH[4], YL[4], YH[4];
            int pa[2] = {aoff, aoff + 65536}, pb0[2] = {boff0, boff0 + 65536}, pb1[2] = {boff1, boff1 + 65536};
            PIN(pa[1]); PIN(pb0[1]); PIN(pb1[1]);
#define G_BP(n, sl, kk) (lds + (((n) & 1) ? pb1[(sl) >> 1] : pb0[(sl) >> 1]) + ((sl) & 1) * 32768 + ((n) >> 1) * 8192 + (kk) * 4096)
#define G_BL(L_, n, sl, kk) do { L_[n] = trd(G_BP(n, sl, kk)); SCHED; } while (0)
#define G_BH(H_, n, sl, kk) do { H_[n] = trd(G_BP(n, sl, kk) + 2048); SCHED; } while (0)
#define G_A(A_, m, sl, kk) do { A_[m] = *(const LAS bf16x8*)(lds + pa[(sl) >> 1] + ((sl) & 1) * 32768 + ((m) * 2 + (kk)) * 1024); SCHED; } while (0)
#define G_M(A_, L_, H_, m, n) do { MFMA16_ACC(acc[m][n], cat8(L_[n], H_[n]), A_[m]); SCHED; } while (0)
#define G_REST(AC, CL, CH, AN, NL, NH, sl, kk) do { \
        G_BL(NL, 0, sl, kk); G_M(AC, CL, CH, 0, 2); G_BH(NH, 0, sl, kk); G_M(AC, CL, CH, 0, 3); G_A(AN, 0, sl, kk); G_M(AC, CL, CH, 1, 0); G_BL(NL, 1, sl, kk); G_M(AC, CL, CH, 1, 1); \
        G_BH(NH, 1, sl, kk); G_M(AC, CL, CH, 1, 2); G_BL(NL, 2, sl, kk); G_M(AC, CL, CH, 1, 3); G_BH(NH, 2, sl, kk); G_M(AC, CL, CH, 2, 0); G_BL(NL, 3, sl, kk); G_M(AC, CL, CH, 2, 1); \
        G_BH(NH, 3, sl, kk); G_M(AC, CL, CH, 2, 2); G_A(AN, 1, sl, kk); G_M(AC, CL, CH, 2, 3); G_A(AN, 2, sl, kk); G_M(AC, CL, CH, 3, 0); G_A(AN, 3, sl, kk); G_M(AC, CL, CH, 3, 1); \
        G_M(AC, CL, CH, 3, 2); G_M(AC, CL, CH, 3, 3); } while (0)
#define G_TILE(sl) do { G_M(AX, XL, XH, 0, 0); G_M(AX, XL, XH, 0, 1); G_REST(AX, XL, XH, AY, YL, YH, sl, 1); \
        G_M(AY, YL, YH, 0, 0); G_M(AY, YL, YH, 0, 1); \
        asm volatile("s_waitcnt vmcnt(0) lgkmcnt(0)" ::: "memory"); BAR; SCHED;        \
        G_REST(AY, YL, YH, AX, XL, XH, ((sl) + 1) & 3, 0); } while (0)
#pragma unroll
            for (int m = 0; m < 4; ++m)
#pragma unroll
                for (int n = 0; n < 4; ++n) { acc[m][n] = (f32x4){0.f, 0.f, 0.f, 0.f}; PIN(acc[m][n]); }
            SCHED;
#pragma unroll
            for (int i = 0; i < 4; ++i) { G_BL(XL, i, 0, 0); G_BH(XH, i, 0, 0); G_A(AX, i, 0, 0); }
            for (int t = 0; t < 32; t += 4) { G_TILE(0); G_TILE(1); G_TILE(2); G_TILE(3); }
            asm volatile("s_nop 7"); SCHED;
            const int fr = lane & 15, fq = lane >> 4;
            const size_t rowo = (size_t)(rt2 * 128 + wm * 64 + kh * 32 + fr) * DM + ct2 * 128 + wn * 32 + (fq & 1) * 16 + (fq >> 1) * 8;
            u32x4 xv[2][2];
#pragma unroll
            for (int m = 0; m < 2; ++m)
#pragma unroll
                for (int s = 0; s < 2; ++s) ST16(pack8(acc[2 + m][2 * s], acc[2 + m][2 * s + 1]), WS_PART + tile * 32768 + kh * 16384 + (m * 2 + s) * 4096 + tid * 16);
#pragma unroll
            for (int m = 0; m < 2; ++m)
#pragma unroll
                for (int s = 0; s < 2; ++s) xv[m][s] = *(const u32x4*)(x + rowo + (size_t)m * 16 * DM + s * 64);
            SCHED;
#define H_REST(AC, CL, CH, AN, NL, NH, sl, kk) do { \
        G_BL(NL, 0, sl, kk); G_BH(NH, 0, sl, kk); G_M(AC, CL, CH, 0, 2); G_A(AN, 0, sl, kk); G_BL(NL, 1, sl, kk); G_M(AC, CL, CH, 0, 3); G_BH(NH, 1, sl, kk); G_BL(NL, 2, sl, kk); G_M(AC, CL, CH, 1, 0); \
        G_BH(NH, 2, sl, kk); G_BL(NL, 3, sl, kk); G_M(AC, CL, CH, 1, 1); G_BH(NH, 3, sl, kk); G_M(AC, CL, CH, 1, 2); G_A(AN, 1, sl, kk); G_M(AC, CL, CH, 1, 3); } while (0)
#define H_TILE(sl, SYNC) do { G_M(AX, XL, XH, 0, 0); G_M(AX, XL, XH, 0, 1); H_REST(AX, XL, XH, AY, YL, YH, sl, 1); \
        G_M(AY, YL, YH, 0, 0); G_M(AY, YL, YH, 0, 1); SYNC; SCHED; H_REST(AY, YL, YH, AX, XL, XH, ((sl) + 1) & 3, 0); } while (0)
#define H_BAR do { WAIT_L(0); BAR; } while (0)
            H_TILE(0, H_BAR); H_TILE(1, H_BAR); H_TILE(2, WAIT_V(0); H_BAR); H_TILE(3, H_BAR);
            H_TILE(0, H_BAR); H_TILE(1, H_BAR); H_TILE(2, H_BAR); H_TILE(3, );
#undef G_BP
#undef G_BL
#undef G_BH
#undef G_A
#undef G_M
#undef G_REST
#undef G_TILE
#undef H_REST
#undef H_TILE
#undef H_BAR
            asm volatile("s_nop 7"); SCHED;
            BAR; CBAR;
            u32x4 pv[2][2];
#pragma unroll
            for (int m = 0; m < 2; ++m)
#pragma unroll
                for (int s = 0; s < 2; ++s) pv[m][s] = *(const LAS u32x4*)(lds + 131072 + (m * 2 + s) * 4096 + tid * 16);
#pragma unroll
            for (int m = 0; m < 2; ++m)
#pragma unroll
                for (int s = 0; s < 2; ++s) { float lo[4], hi4[4]; const u32x4 p_ = pv[m][s], x_ = xv[m][s];
                    const f32x4 a0 = acc[m][2 * s] + (f32x4){bf_lo(p_[0]), bf_hi(p_[0]), bf_lo(p_[1]), bf_hi(p_[1])}, a1 = acc[m][2 * s + 1] + (f32x4){bf_lo(p_[2]), bf_hi(p_[2]), bf_lo(p_[3]), bf_hi(p_[3])};
#pragma unroll
                    for (int i = 0; i < 4; ++i) { auto r = __builtin_amdgcn_permlane16_swap(__float_as_uint(a0[i]), __float_as_uint(a1[i]), false, false);
                        lo[i] = __uint_as_float(r[0]); hi4[i] = __uint_as_float(r[1]); }
                    u32x4 o; o[0] = cvtpk(lo[0] + bf_lo(x_[0]), lo[1] + bf_hi(x_[0])); o[1] = cvtpk(lo[2] + bf_lo(x_[1]), lo[3] + bf_hi(x_[1]));
                    o[2] = cvtpk(hi4[0] + bf_lo(x_[2]), hi4[1] + bf_hi(x_[2])); o[3] = cvtpk(hi4[2] + bf_lo(x_[3]), hi4[3] + bf_hi(x_[3]));
                    __builtin_nontemporal_store(o, (u32x4*)(out + rowo + (size_t)m * 16 * DM + s * 64));
                }
        }
    }
}

extern "C" void kernel_launch(void* const* d_in, const int* in_sizes, int n_in, void* d_out, int out_size, void* d_ws, size_t ws_size, hipStream_t stream) {
    (void)hipFuncSetAttribute((const void*)k_block, hipFuncAttributeMaxDynamicSharedMemorySize, LDS_BYTES);
    k_block<<<256, 512, LDS_BYTES, stream>>>((const u16*)d_in[0], (const u16*)d_in[1], (const u16*)d_in[2], (const u16*)d_in[4], (const u16*)d_in[5], (const u16*)d_in[6], (char*)d_ws, (u16*)d_out);
}
```

```cpp
#include <hip/hip_runtime.h>
#include <hip/hip_bf16.h>
#include <stdint.h>

#define LAS __attribute__((address_space(3)))
#define GAS __attribute__((address_space(1)))
#define DI __device__ __forceinline__
typedef unsigned short u16;
typedef unsigned long long u64;
typedef short bf16x8 __attribute__((ext_vector_type(8)));
typedef short s16x4 __attribute__((ext_vector_type(4)));
typedef float f32x2 __attribute__((ext_vector_type(2)));
typedef float f32x4 __attribute__((ext_vector_type(4)));
typedef float f32x16 __attribute__((ext_vector_type(16)));
typedef unsigned u32x2 __attribute__((ext_vector_type(2)));
typedef unsigned u32x4 __attribute__((ext_vector_type(4)));
typedef __bf16 bf16x2_t __attribute__((ext_vector_type(2)));

constexpr int SEQ = 2048, DM = 1024, FF = 3584;
constexpr int KB_X = DM / 32;
constexpr int KB_AH = (DM + FF) / 32;
constexpr unsigned WS_XN = 0;
constexpr unsigned WS_Q = WS_XN + SEQ * DM * 2;
constexpr unsigned WS_K = WS_Q + SEQ * DM * 2;
constexpr unsigned WS_V = WS_K + SEQ * DM * 2;
constexpr unsigned WS_AH = WS_V + SEQ * DM * 2;
constexpr unsigned WS_PART = WS_AH + SEQ * (DM + FF) * 2;
constexpr unsigned WS_FLAG = WS_PART + 128 * 32768;
constexpr unsigned WS_END = WS_FLAG + 2048 * 128;
constexpr int F_LN = 32, F_QKV = F_LN + 256, F_H = F_QKV + 256, F_AT = F_H + 256, F_G2 = F_AT + 256;
constexpr int LDS_BYTES = 163840;

DI unsigned cvtpk(float lo, float hi) { f32x2 v = {lo, hi}; bf16x2_t b = __builtin_convertvector(v, bf16x2_t); return __builtin_bit_cast(unsigned, b); }
DI float bf_lo(unsigned u) { return __uint_as_float(u << 16); }
DI float bf_hi(unsigned u) { return __uint_as_float(u & 0xffff0000u); }
DI float rbf(float f) { return __uint_as_float(cvtpk(f, f) << 16); }
DI s16x4 trd(const LAS char* p) { return __builtin_amdgcn_ds_read_tr16_b64_v4i16((LAS s16x4*)p); }
DI bf16x8 cat8(s16x4 a, s16x4 b) { return __builtin_shufflevector(a, b, 0, 1, 2, 3, 4, 5, 6, 7); }
template <int OFF, int D> DI void glds(const char* base, unsigned voff, unsigned dst) {
    asm volatile("s_add_u32 m0, %2, %4\n\ts_nop 0\n\tglobal_load_lds_dwordx4 %0, %1 offset:%3" :: "v"(voff), "s"(base), "s"(dst), "i"(OFF), "i"(D - OFF) : "memory", "scc"); }
template <int D> DI void glds_pair_sc1(const char* base, unsigned voff, unsigned dst) {
    asm volatile("s_add_u32 m0, %2, %3\n\ts_nop 0\n\tglobal_load_lds_dwordx4 %0, %1 sc1\n\tglobal_load_lds_dwordx4 %0, %1 offset:1024 sc1" :: "v"(voff), "s"(base), "s"(dst), "i"(D) : "memory", "scc"); }
template <int D> DI void touch(const char* base, unsigned voff, unsigned dst) {
    asm volatile("s_add_u32 m0, %2, %3\n\ts_nop 0\n\tglobal_load_lds_dword %0, %1" :: "v"(voff), "s"(base), "s"(dst), "i"(D) : "memory", "scc"); }
#define WAIT_V(n) asm volatile("s_waitcnt vmcnt(" #n ")" ::: "memory")
#define WAIT_L(n) asm volatile("s_waitcnt lgkmcnt(" #n ")" ::: "memory")
#define BAR __builtin_amdgcn_s_barrier()
#define CBAR asm volatile("" ::: "memory")
#define SCHED __builtin_amdgcn_sched_barrier(0)
#define PIN(x) asm volatile("" : "+v"(x))
#define MFMA16(a, b, c) __builtin_amdgcn_mfma_f32_16x16x32_bf16(a, b, c, 0, 0, 0)
#define MFMA16_ACC(c, a, b) asm volatile("v_mfma_f32_16x16x32_bf16 %0, %1, %2, %0" : "+v"(c) : "v"(a), "v"(b))
#define MFMA32(a, b, c) __builtin_amdgcn_mfma_f32_32x32x16_bf16(a, b, c, 0, 0, 0)
#define ST16(v, off) __builtin_amdgcn_raw_buffer_store_b128(v, rs, (int)(off), 0, 16)
#define LD16(off) __builtin_amdgcn_raw_buffer_load_b128(rs, (int)(off), 0, 16)
DI u64 flag_ld(const GAS u64* p) { return __hip_atomic_load(p, __ATOMIC_RELAXED, __HIP_MEMORY_SCOPE_AGENT); }
DI void flag_st(GAS u64* p, u64 v) { __hip_atomic_store(p, v, __ATOMIC_RELAXED, __HIP_MEMORY_SCOPE_AGENT); }
DI void flag_wait(const GAS u64* p, u64 tag) {
    for (unsigned spins = 0; spins < (1u << 18); ++spins) { if (__all(flag_ld(p) == tag)) break; __builtin_amdgcn_s_sleep(1); }
    CBAR;
}
template <int N> DI float ror_add(float v) { return v + __builtin_bit_cast(float, __builtin_amdgcn_update_dpp(0, __builtin_bit_cast(int, v), 0x120 + N  , 0xf, 0xf, false)); }
DI float sum64(float v) {
    v = ror_add<1>(ror_add<2>(ror_add<4>(ror_add<8>(v))));
    { auto r = __builtin_amdgcn_permlane16_swap(__float_as_uint(v), __float_as_uint(v), false, false); v = __uint_as_float(r[0]) + __uint_as_float(r[1]); }
    { auto r = __builtin_amdgcn_permlane32_swap(__float_as_uint(v), __float_as_uint(v), false, false); v = __uint_as_float(r[0]) + __uint_as_float(r[1]); }
    return v;
}
DI u32x4 widen(u32x2 a, u32x2 b) { auto r0 = __builtin_amdgcn_permlane16_swap(a[0], b[0], false, false); auto r1 = __builtin_amdgcn_permlane16_swap(a[1], b[1], false, false);
    return (u32x4){r0[0], r1[0], r0[1], r1[1]}; }
DI int opaque_tid() { int t = threadIdx.x; asm volatile("" : "+v"(t)); return t; }

DI float silu_mul(float g, float u) { return g * u * __builtin_amdgcn_rcpf(1.0f + __builtin_amdgcn_exp2f(g * -1.4426950408889634f)); }
DI u32x4 pack8(const f32x4& a, const f32x4& b) { u32x4 o; o[0] = cvtpk(a[0], a[1]); o[1] = cvtpk(a[2], a[3]); o[2] = cvtpk(b[0], b[1]); o[3] = cvtpk(b[2], b[3]); return o; }
constexpr float QSCALE = 0.17677669529663687f * 1.4426950408889634f;


struct G128 { const char* a; const char* w2; const char* wo; int kh; };
template <bool HALF> DI void g128_issue(unsigned lds0, const G128& g, int t, int lw, unsigned voffA, unsigned voffB) {
    const unsigned so = lds0 + (t & 3) * 32768, sb = so + lw * 2048;
    const bool att = !HALF && g.kh == 0 && t >= 16;
    const int kt = HALF ? t - 16 : att ? t - 16 : g.kh * 24 + t;
    const char* a = g.a + (size_t)(att ? kt : 16 + kt) * 2048; const char* b = (att ? g.wo : g.w2) + (size_t)kt * (64 * DM * 2);
    if (HALF) glds_pair_sc1<0>(a, voffA, so + (lw >> 1) * 8192 + (lw & 1) * 2048);
    else { glds_pair_sc1<0>(a, voffA, so + lw * 4096); glds_pair_sc1<2048>(a + KB_AH * 1024, voffA, so + lw * 4096); }
    glds<0, 16384>(b, voffB, sb); glds<128, 24576>(b, voffB, sb);
    glds<0, 16384 + 1024>(b + 8 * DM * 2, voffB, sb); glds<128, 24576 + 1024>(b + 8 * DM * 2, voffB, sb);
}

__global__ __launch_bounds__(512, 2) void k_block(const u16* __restrict__ x, const u16* __restrict__ wqkv, const u16* __restrict__ wo, const u16* __restrict__ w1,
                                                  const u16* __restrict__ w3, const u16* __restrict__ w2, char* __restrict__ ws, u16* __restrict__ out) {
    extern __shared__ __attribute__((aligned(16))) char smem[];
    LAS char* lds = (LAS char*)smem;
    const unsigned lds0 = (unsigned)(size_t)lds;
    const int wid = __builtin_amdgcn_readfirstlane(threadIdx.x >> 6);
    unsigned ra_ = 0, rb_ = 0;
    const int bid = blockIdx.x, xcd = bid & 7, idx = bid >> 3;
    GAS u64* flags0 = (GAS u64*)(ws + WS_FLAG);
#define FL(i) (flags0 + (i) * 16)
    const __amdgpu_buffer_rsrc_t rs = __builtin_amdgcn_make_buffer_rsrc((void*)ws, 0, WS_END, 0x00020000);
    {
        const char* pc; asm volatile("s_getpc_b64 %0" : "=s"(pc));
        if (wid == 7) touch<148480>(pc + (idx & 7) * 4096, (threadIdx.x & 63) * 64, lds0 + 7 * 1024);
    }
    ra_ = (unsigned)__builtin_amdgcn_s_memrealtime(); asm volatile("" : "+s"(ra_));
    u64 tag;
    u32x4 xa, xb; u32x2 ep;
    { const char* xp = (const char*)x + (size_t)(bid * 8 + wid) * (DM * 2);
      asm volatile("global_load_dwordx4 %0, %3, %4\n\tglobal_load_dwordx4 %1, %3, %4 offset:1024\n\tglobal_load_dwordx2 %2, %5, %6 sc1"
                   : "=&v"(xa), "=&v"(xb), "=&v"(ep) : "v"((threadIdx.x & 63) * 16), "s"(xp), "v"(0), "s"(ws + WS_FLAG), "s"(wqkv), "s"(w1), "s"(w3) : "memory"); }
    const int cj = idx >> 2, ct = cj < 3 ? (xcd >> 1) * 3 + cj : 12 + (xcd >> 1) * 5 + (cj - 3), rt = (xcd & 1) * 4 + (idx & 3);
    const bool qkv = ct < 12;
    u32x4 hmain[8], hext[2];

    {
        const int tid = opaque_tid(), lane = tid & 63, wr = wid >> 2, wc = wid & 3;
        const unsigned ldb2 = qkv ? 3 * DM * 2 : FF * 2;
        const char* cB0 = qkv ? (const char*)wqkv + ct * 512 : (const char*)w1 + (ct - 12) * 256;
        const char* cB1 = qkv ? cB0 + 256 : (const char*)w3 + (ct - 12) * 256;
        const char* cA = ws + WS_XN + rt * 16 * KB_X * 1024;
        const unsigned voffA = wid * (KB_X * 1024) + lane * 16;
        const unsigned voffB = (unsigned)(wid * 8 + (lane >> 3)) * ldb2 + (((lane & 7) ^ (2 * ((lane >> 4) & 3))) * 16);
        const char* pE = ((lane & 1) ? (const char*)w3 : (const char*)w1) + (size_t)(wid * 8 + (lane >> 3)) * (FF * 2) + (2560 + ct * 32) * 2 + ((((lane >> 1) & 3) ^ ((lane >> 4) & 3)) * 16);
        const size_t kstepB = (size_t)64 * ldb2;
        const unsigned ldsA = lds0 + wid * 2048, ldsB = lds0 + wid * 1024;
        const char* cT = ((idx & 2) ? cB1 : cB0) + (idx & 1) * 128;
#define SA(b, h) (((b) * 2 + (h)) * 16384)
#define SB(b, h) (((b) * 2 + (h)) * 16384)
#define STA(b, h, kt) glds_pair_sc1<SA(b, h)>(cA + (h) * 8 * KB_X * 1024 + (kt) * 2048, voffA, ldsA)
#define STB(b, h, kt) do { const char* s_ = ((h) ? cB1 : cB0) + (size_t)(kt) * kstepB; \
        glds<0, 65536 + SB(b, h)>(s_, voffB, ldsB); glds<128, 65536 + SB(b, h) + 8192>(s_, voffB, ldsB); } while (0)
#define TCH(kt) touch<148480>(cT + (size_t)(kt) * kstepB, voffB, ldsB)
#define STE(b, kt) do { const char* s_ = pE + (size_t)(kt) * (64 * FF * 2); \
        asm volatile("s_add_u32 m0, %1, %2\n\ts_nop 0\n\tglobal_load_lds_dwordx4 %0, off" :: "v"(s_), "s"(ldsB), "i"(131072 + (b) * 8192) : "memory", "scc"); } while (0)
        {
            TCH(0); TCH(1);
            { const char* s_ = pE + (size_t)(idx & 1) * (64 * FF * 2) + (ptrdiff_t)(((idx >> 1) & 1) - (wid >> 2)) * (32 * FF * 2);
              asm volatile("s_add_u32 m0, %1, 148480\n\ts_nop 0\n\tglobal_load_lds_dword %0, off" :: "v"(s_), "s"(ldsB) : "memory", "scc"); }
            asm volatile("s_waitcnt vmcnt(3)" : "+v"(xa), "+v"(xb), "+v"(ep) :: "memory");
            tag = (((u64)(unsigned)__builtin_amdgcn_readfirstlane(ep[1]) << 32) | (unsigned)__builtin_amdgcn_readfirstlane(ep[0])) + 1;
            float f[16];
#pragma unroll
            for (int i = 0; i < 4; ++i) { f[2 * i] = bf_lo(xa[i]); f[2 * i + 1] = bf_hi(xa[i]); f[8 + 2 * i] = bf_lo(xb[i]); f[9 + 2 * i] = bf_hi(xb[i]); }
            float s = 0.f;
#pragma unroll
            for (int i = 0; i < 16; ++i) s += f[i];
            const float mean = sum64(s) * (1.0f / DM);
            float q = 0.f;
#pragma unroll
            for (int i = 0; i < 16; ++i) { const float d = f[i] - mean; q += d * d; }
            const float var = sum64(q) * (1.0f / DM);
            const float mb = rbf(mean), sd = rbf(__builtin_amdgcn_sqrtf(rbf(rbf(var) + 1e-5f))), inv = __builtin_amdgcn_rcpf(sd);
#pragma unroll
            for (int c = 0; c < 2; ++c) { u32x4 o;
#pragma unroll
                for (int i = 0; i < 4; ++i) o[i] = cvtpk(rbf(f[c * 8 + 2 * i] - mb) * inv, rbf(f[c * 8 + 2 * i + 1] - mb) * inv);
                *(LAS u32x4*)(lds + wid * 2080 + c * 1024 + lane * 16) = o; }
            WAIT_L(0); BAR; CBAR;
            const int r = lane & 7, g = (lane >> 3) & 3, row = bid * 8 + r;
#pragma unroll
            for (int c = 0; c < 2; ++c) { const int kb = 4 * wid + 2 * c + (lane >> 5); const LAS char* p = lds + r * 2080 + kb * 64 + g * 8;
                const u32x2 lo = *(const LAS u32x2*)p, hi = *(const LAS u32x2*)(p + 32);
                ST16(((u32x4){lo[0], lo[1], hi[0], hi[1]}), WS_XN + ((row >> 4) * KB_X + kb) * 1024 + (g * 16 + (row & 15)) * 16); }
            CBAR;
            STB(0, 0, 0); STB(0, 1, 0); STE(0, 0);
            WAIT_V(5); BAR;
            if (wid == 0) { if (lane == 0) flag_st(FL(F_LN + bid), tag); flag_wait(FL(F_LN + rt * 32 + (lane & 31)), tag); }
            STB(1, 0, 1); STB(1, 1, 1); STE(1, 1);
            if (wid != 0) { TCH(2); TCH(3); }
            BAR; CBAR;
        }
        f32x4 acc[2][2][4][2], ace[2][4];
        {
            const int g = lane >> 4, q = (lane & 15) >> 2, p = lane & 3, swz = (2 * g + (q >> 1)) & 3;
            const int aoff = wr * 8192 + lane * 16;
            const int boff0 = 65536 + (wc >> 1) * 8192 + (4 * g + q) * 128 + (((2 * (wc & 1)) ^ swz) * 32) + p * 8, boff1 = boff0 ^ 32;
            int eoff = 131072 + (4 * g + q) * 128 + ((((wc >> 1) + 2 * (p & 1)) ^ swz) * 32) + (p >> 1) * 16 + (wc & 1) * 8; PIN(eoff);
#define LDA(dst, b, h) do { _Pragma("unroll") for (int m = 0; m < 4; ++m) _Pragma("unroll") for (int k = 0; k < 2; ++k) \
        dst[m][k] = *(const LAS bf16x8*)(lds + aoff + SA(b, h) + (m * 2 + k) * 1024); } while (0)
#define LDB(dst, b, h) do { _Pragma("unroll") for (int n = 0; n < 2; ++n) _Pragma("unroll") for (int k = 0; k < 2; ++k) { \
        const LAS char* b_ = lds + (n ? boff1 : boff0) + SB(b, h) + k * 4096; dst[n][k] = cat8(trd(b_), trd(b_ + 2048)); } } while (0)
#define LDE(dst, b) do { _Pragma("unroll") for (int k = 0; k < 2; ++k) { const LAS char* b_ = lds + eoff + (b) * 8192 + k * 4096; dst[k] = cat8(trd(b_), trd(b_ + 2048)); } } while (0)
#define MMA(ai) do { __builtin_amdgcn_s_setprio(1); \
        _Pragma("unroll") for (int k = 0; k < 2; ++k) _Pragma("unroll") for (int m = 0; m < 4; ++m) { \
            MFMA16_ACC(acc[ai][0][m][0], B0[0][k], At[m][k]); MFMA16_ACC(acc[ai][0][m][1], B0[1][k], At[m][k]); \
            MFMA16_ACC(acc[ai][1][m][0], B1[0][k], At[m][k]); MFMA16_ACC(acc[ai][1][m][1], B1[1][k], At[m][k]); \
            MFMA16_ACC(ace[ai][m], BE[k], At[m][k]); } \
        __builtin_amdgcn_s_setprio(0); } while (0)
            STA(0, 0, 0); STA(0, 1, 0); STA(1, 0, 1);
#pragma unroll
            for (int a = 0; a < 2; ++a)
#pragma unroll
                for (int m = 0; m < 4; ++m) { ace[a][m] = (f32x4){0.f, 0.f, 0.f, 0.f}; PIN(ace[a][m]);
#pragma unroll
                    for (int b = 0; b < 2; ++b)
#pragma unroll
                        for (int n = 0; n < 2; ++n) { acc[a][b][m][n] = (f32x4){0.f, 0.f, 0.f, 0.f}; PIN(acc[a][b][m][n]); } }
            bf16x8 At[4][2], B0[2][2], B1[2][2], BE[2];
            constexpr int nt = DM / 64;
            if (wr == 1) BAR;
            WAIT_V(4); BAR;
            WAIT_V(2); BAR;
    rb_ = (unsigned)__builtin_amdgcn_s_memrealtime(); asm volatile("" : "+s"(rb_));
            for (int t = 0; t < nt; t += 2) {
                const bool more = t + 2 < nt, far = t + 4 < nt;
                LDB(B0, 0, 0); LDB(B1, 0, 1); LDE(BE, 0); SCHED; LDA(At, 0, 0); STA(1, 1, t + 1);
                if (far) { TCH(t + 4); WAIT_V(11); } else { WAIT_V(9); }
                WAIT_L(0); BAR; MMA(0); BAR; SCHED;
                LDA(At, 0, 1);
                if (more) { STB(0, 0, t + 2); STB(0, 1, t + 2); STE(0, t + 2); STA(0, 0, t + 2); if (far) { WAIT_V(10); } else { WAIT_V(9); } } else { WAIT_V(2); }
                WAIT_L(0); BAR; MMA(1); BAR; SCHED;
                LDB(B0, 1, 0); LDB(B1, 1, 1); LDE(BE, 1); SCHED; LDA(At, 1, 0);
                if (more) { STA(0, 1, t + 2); if (far) { TCH(t + 5); WAIT_V(11); } else { WAIT_V(9); } } else { WAIT_V(0); }
                WAIT_L(0); BAR; MMA(0); BAR; SCHED;
                LDA(At, 1, 1);
                if (more) { STB(1, 0, t + 3); STB(1, 1, t + 3); STE(1, t + 3); STA(1, 0, t + 3); if (far) { WAIT_V(10); } else { WAIT_V(9); } }
                WAIT_L(0); BAR; MMA(1); if (more || wr == 0) BAR; SCHED;
            }
            asm volatile("s_nop 7"); SCHED;
#undef SA
#undef SB
#undef STA
#undef STB
#undef STE
#undef TCH
#undef LDA
#undef LDB
#undef LDE
#undef MMA
        }
        const int ln = opaque_tid() & 63, fr = ln & 15, fq = ln >> 4;
        if (qkv) {
            const int kind = ct >> 2;
            const float sc = kind == 0 ? QSCALE : 1.0f;
            const unsigned base = WS_Q + kind * (SEQ * DM * 2) + ((ct & 3) * 8 + wc) * 131072 + (rt * 256 + wr * 64) * 64;
            const unsigned lo0 = base + (kind == 2 ? fr * 64 + fq * 16 : fr * 16 + fq * 512), lo1 = lo0 + (kind == 2 ? 1024 : 256);
#pragma unroll
            for (int bj = 0; bj < 2; ++bj)
#pragma unroll
                for (int ai = 0; ai < 2; ++ai)
#pragma unroll
                    for (int m = 0; m < 4; ++m)
                        ST16(pack8(acc[ai][bj][m][0] * sc, acc[ai][bj][m][1] * sc), bj * 4 * 131072 + ai * 8192 + (m >> 1) * 2048 + ((m & 1) ? lo1 : lo0));
            WAIT_V(0); BAR;
            if (wid == 0 && ln == 0) flag_st(FL(F_QKV + rt * 32 + ct), tag);
        } else {
#pragma unroll
            for (int ai = 0; ai < 2; ++ai)
#pragma unroll
                for (int m = 0; m < 4; ++m) { f32x4 h0, h1;
#pragma unroll
                    for (int j = 0; j < 4; ++j) { h0[j] = silu_mul(acc[ai][0][m][0][j], acc[ai][1][m][0][j]); h1[j] = silu_mul(acc[ai][0][m][1][j], acc[ai][1][m][1][j]); }
                    hmain[ai * 4 + m] = pack8(h0, h1);
                }
        }
#pragma unroll
        for (int ai = 0; ai < 2; ++ai) { u32x2 e[2];
#pragma unroll
            for (int mp = 0; mp < 2; ++mp) { float h[4];
#pragma unroll
                for (int j = 0; j < 4; ++j) { auto r = __builtin_amdgcn_permlane32_swap(__float_as_uint(ace[ai][2 * mp][j]), __float_as_uint(ace[ai][2 * mp + 1][j]), false, false);
                    h[j] = silu_mul(__uint_as_float(r[0]), __uint_as_float(r[1])); }
                e[mp] = (u32x2){cvtpk(h[0], h[1]), cvtpk(h[2], h[3])}; }
            hext[ai] = widen(e[0], e[1]); }
    }

    const int kh = xcd & 1, rt2 = ((xcd >> 1) & 1) * 8 + (idx & 7), ct2 = (xcd >> 2) * 4 + (idx >> 3);
    G128 g2; g2.kh = kh; g2.a = ws + WS_AH + rt2 * 8 * KB_AH * 1024; g2.w2 = (const char*)w2 + ct2 * 256; g2.wo = (const char*)wo + ct2 * 256;
#define G2_OFFS(lw_, l_) const unsigned vA = ((lw_) ^ kh) * 2 * KB_AH * 1024 + (l_) * 16, vB = ((lw_) * 16 + ((l_) >> 3)) * (DM * 2) + ((((l_) & 7) ^ (2 * (((l_) >> 4) & 3))) * 16)

    {
        constexpr int AT_BASE = 65536, AT_STAGE = 32768;
        const int lane = opaque_tid() & 63, hi = lane >> 5;
        const int head = xcd * 4 + (idx >> 3), qb = idx & 7;
        if (wid == 0) flag_wait(FL(F_QKV + (lane < 4 ? (lane & 1) * 32 + 4 + (lane >> 1) * 4 : qb * 32) + (head >> 3)), tag);
        BAR; CBAR;
        const char* Kg = ws + WS_K + head * 131072; const char* Vg = ws + WS_V + head * 131072;
        const unsigned soff = wid * 2048 + lane * 16;
        const unsigned sdst = lds0 + AT_BASE + wid * 2048;
#define AT_ISSUE(s, slot) do { glds_pair_sc1<0>(Kg + (s) * 16384, soff, sdst + (slot)); glds_pair_sc1<16384>(Vg + (s) * 16384, soff, sdst + (slot)); } while (0)
        const unsigned qoff = WS_Q + (head * 64 + qb * 8 + wid) * 2048 + lane * 16;
        const bf16x8 q0 = __builtin_bit_cast(bf16x8, LD16(qoff)), q1 = __builtin_bit_cast(bf16x8, LD16(qoff + 1024));
        AT_ISSUE(0, 0); AT_ISSUE(1, AT_STAGE);
        const GAS u64* kvflag = FL(F_QKV + (lane & 7) * 32 + 4 + ((lane >> 3) & 1) * 4 + (head >> 3));
        u64 seen = flag_ld(kvflag);
        bf16x8 sel; { const short one = ((lane & 15) == ((lane >> 4) & 1)) ? (short)0x3f80 : (short)0;
#pragma unroll
            for (int i = 0; i < 8; ++i) sel[i] = one; }
        const LAS char* kb0 = lds + AT_BASE + lane * 16;
        const LAS char* vb0 = lds + AT_BASE + 16384 + (4 * hi + ((lane & 15) >> 2)) * 64 + ((lane >> 4) & 1) * 32 + (lane & 3) * 8;
        f32x16 O, negm, SA, SB; f32x4 L = {0.f, 0.f, 0.f, 0.f};
        bf16x8 k0, k1, v0, v1;
#pragma unroll
        for (int i = 0; i < 16; ++i) { O[i] = 0.f; negm[i] = 0.f; }
        WAIT_V(5); BAR;
        {
            k0 = *(const LAS bf16x8*)(kb0); k1 = *(const LAS bf16x8*)(kb0 + 1024);
            SA = MFMA32(k0, q0, negm); SA = MFMA32(k1, q1, SA);
            k0 = *(const LAS bf16x8*)(kb0 + 2048); k1 = *(const LAS bf16x8*)(kb0 + 3072);
            v0 = cat8(trd(vb0), trd(vb0 + 512)); v1 = cat8(trd(vb0 + 1024), trd(vb0 + 1536));
            float m = SA[0];
#pragma unroll
            for (int i = 1; i < 16; ++i) m = fmaxf(m, SA[i]);
            m = fmaxf(m, __shfl_xor(m, 32));
#pragma unroll
            for (int i = 0; i < 16; ++i) { negm[i] = -m; SA[i] -= m; }
            PIN(negm);
        }
#define EX(S, i) S[i] = __builtin_amdgcn_exp2f(S[i])
#define BC8(x) __builtin_bit_cast(bf16x8, x)
#define AT_STEP(SC, SN, kp, v1p, v0p) do { \
        O = MFMA32(v1, BC8(p1), O); SCHED; v1 = cat8(trd(v1p), trd((v1p) + 512)); EX(SC, 0); EX(SC, 1); EX(SC, 2); PIN(SC); SCHED; \
        SN = MFMA32(k0, q0, negm); SCHED; k0 = *(const LAS bf16x8*)(kp); EX(SC, 3); EX(SC, 4); EX(SC, 5); PIN(SC); SCHED; \
        L = MFMA16(sel, BC8(p1), L); SCHED; EX(SC, 6); EX(SC, 7); \
        p0[0] = cvtpk(SC[0], SC[1]); p0[1] = cvtpk(SC[2], SC[3]); p0[2] = cvtpk(SC[4], SC[5]); p0[3] = cvtpk(SC[6], SC[7]); PIN(p0); SCHED; \
        O = MFMA32(v0, BC8(p0), O); SCHED; v0 = cat8(trd(v0p), trd((v0p) + 512)); EX(SC, 8); EX(SC, 9); EX(SC, 10); PIN(SC); SCHED; \
        SN = MFMA32(k1, q1, SN); SCHED; k1 = *(const LAS bf16x8*)((kp) + 1024); EX(SC, 11); EX(SC, 12); EX(SC, 13); PIN(SC); SCHED; \
        L = MFMA16(sel, BC8(p0), L); SCHED; EX(SC, 14); EX(SC, 15); \
        p1[0] = cvtpk(SC[8], SC[9]); p1[1] = cvtpk(SC[10], SC[11]); p1[2] = cvtpk(SC[12], SC[13]); p1[3] = cvtpk(SC[14], SC[15]); PIN(p1); SCHED; } while (0)
        u32x4 p0, p1 = {0u, 0u, 0u, 0u};
        int cur = 0, nxt = AT_STAGE, fre = 2 * AT_STAGE;
        const GAS u64* hflag = FL(F_H + (rt2 >> 1) * 32 + (lane & 31));
        const unsigned hb = WS_AH + ((rt * 16 + (wid >> 2) * 4) * KB_AH + KB_X + (ct - 12) * 4 + (wid & 3)) * 1024 + lane * 16;
        const unsigned eb = WS_AH + ((rt * 16 + (wid >> 2) * 4 + 2 * ((lane >> 4) & 1) + hi) * KB_AH + KB_X + 80 + ct) * 1024 + ((wid & 3) * 16 + (lane & 15)) * 16;
#define HM(i) ST16(hmain[i], hb + (((i) >> 2) * 8 + ((i) & 3)) * KB_AH * 1024)
#define H_ST(j) do { if (s == 4) ST16(hext[j], eb + (j) * 8 * KB_AH * 1024); else if (!qkv && s < 4) { if (s == 0) HM(j); else if (s == 1) HM(2 + (j)); else if (s == 2) HM(4 + (j)); else HM(6 + (j)); } SCHED; } while (0)
        for (int s = 0; s < 8; ++s) {
            if (s == 1) { if (qkv) { WAIT_V(0); } else { WAIT_V(2); } } else if (s >= 2 && s <= 4) { if (qkv) { WAIT_V(1); } else { WAIT_V(3); } } else if (s == 5) { WAIT_V(3); } else { WAIT_V(0); }
            WAIT_L(0); BAR;
            if (s == 0) { if (!__all(seen == tag)) flag_wait(kvflag, tag); }
            if (s + 2 < 8) glds_pair_sc1<0>(Kg + (s + 2) * 16384, soff, sdst + fre);
            if (s == 6) { if (wid == 0 && lane == 0) flag_st(FL(F_H + rt * 32 + ct), tag); }
            if (s == 7) seen = flag_ld(hflag);
            if (wid >= 4) __builtin_amdgcn_s_setprio(1);
            const LAS char* kc = kb0 + cur; const LAS char* vc = vb0 + cur; const LAS char* kn = kb0 + nxt; const LAS char* vn = vb0 + nxt;
            AT_STEP(SA, SB, kc + 2 * 2048, vc + 1024, vc + 1 * 2048);
            if (s + 2 < 8) glds_pair_sc1<16384>(Vg + (s + 2) * 16384, soff, sdst + fre);
            SCHED;
            AT_STEP(SB, SA, kc + 3 * 2048, vc + 1 * 2048 + 1024, vc + 2 * 2048);
            H_ST(0);
            AT_STEP(SA, SB, kc + 4 * 2048, vc + 2 * 2048 + 1024, vc + 3 * 2048); AT_STEP(SB, SA, kc + 5 * 2048, vc + 3 * 2048 + 1024, vc + 4 * 2048);
            if (s >= 1 && s < 6) {
                const int r0 = ((s - 1) * 256 + idx * 8 + wid) * 2;
                const char* src = (kh ? (const char*)w2 + 1024 * (DM * 2) : r0 >= 1536 ? (const char*)wo - 1536 * (DM * 2) : (const char*)w2) + (size_t)r0 * (DM * 2) + (xcd >> 2) * 1024;
                touch<-AT_BASE>(src, ((lane >> 3) & 1) * (DM * 2) + (lane & 7) * 128, sdst);
            }
            if (s == 7 && wid >= 4) {
                if (!__all(seen == tag)) flag_wait(hflag, tag);
                G2_OFFS(wid - 4, lane);
                g128_issue<false>(lds0, g2, 0, wid - 4, vA, vB); g128_issue<false>(lds0, g2, 1, wid - 4, vA, vB);
            }
            SCHED;
            AT_STEP(SA, SB, kc + 6 * 2048, vc + 4 * 2048 + 1024, vc + 5 * 2048); __builtin_amdgcn_s_setprio(0); AT_STEP(SB, SA, kc + 7 * 2048, vc + 5 * 2048 + 1024, vc + 6 * 2048);
            H_ST(1);
            AT_STEP(SA, SB, kn, vc + 6 * 2048 + 1024, vc + 7 * 2048);            AT_STEP(SB, SA, kn + 2048, vc + 7 * 2048 + 1024, vn);
            const int tmp = cur; cur = nxt; nxt = fre; fre = tmp;
        }
        O = MFMA32(v1, BC8(p1), O); L = MFMA16(sel, BC8(p1), L);
        float lrow; { const float a = __shfl(L[0], lane & 15), b = __shfl(L[1], lane & 15); lrow = (lane & 16) ? b : a; }
        float oabs = 0.f;
#pragma unroll
        for (int i = 0; i < 16; ++i) oabs += fabsf(O[i]);
        if (__any(!(lrow < 1.0e30f) || !(oabs < 3.0e38f))) {
            __builtin_amdgcn_fence(__ATOMIC_ACQUIRE, "agent");
            float m = -INFINITY, l = 0.f;
#pragma unroll
            for (int i = 0; i < 16; ++i) { O[i] = 0.f; negm[i] = 0.f; }
            const u16* Vs = (const u16*)Vg;
            for (int t = 0; t < 64; ++t) { const int key0 = t * 32;
                k0 = *(const bf16x8*)(Kg + key0 * 64 + lane * 16); k1 = *(const bf16x8*)(Kg + key0 * 64 + 1024 + lane * 16);
#pragma unroll
                for (int j = 0; j < 8; ++j) { const int kk = key0 + 8 * (j >> 2) + 4 * hi + (j & 3); v0[j] = (short)Vs[kk * 32 + (lane & 31)]; v1[j] = (short)Vs[(kk + 16) * 32 + (lane & 31)]; }
                f32x16 S = MFMA32(k0, q0, negm); S = MFMA32(k1, q1, S);
                float mt = S[0];
#pragma unroll
                for (int i = 1; i < 16; ++i) mt = fmaxf(mt, S[i]);
                mt = fmaxf(mt, __shfl_xor(mt, 32));
                const float mn = fmaxf(m, mt), al = __builtin_amdgcn_exp2f(m - mn); m = mn;
                float ps = 0.f;
#pragma unroll
                for (int i = 0; i < 16; ++i) { S[i] = rbf(__builtin_amdgcn_exp2f(S[i] - mn)); ps += S[i]; O[i] *= al; }
                l = l * al + ps + __shfl_xor(ps, 32);
#pragma unroll
                for (int i = 0; i < 4; ++i) { p0[i] = cvtpk(S[2 * i], S[2 * i + 1]); p1[i] = cvtpk(S[8 + 2 * i], S[9 + 2 * i]); }
                O = MFMA32(v0, BC8(p0), O); O = MFMA32(v1, BC8(p1), O);
            }
            lrow = l;
        }
        {
            const float inv = 1.0f / lrow;
            unsigned pk[8];
#pragma unroll
            for (int i = 0; i < 8; ++i) pk[i] = cvtpk(O[2 * i] * inv, O[2 * i + 1] * inv);
            const unsigned dst = WS_AH + ((qb * 16 + wid * 2 + ((lane >> 4) & 1)) * KB_AH + head) * 1024 + (lane & 15) * 16 + hi * 256;
#pragma unroll
            for (int c = 0; c < 2; ++c) {
                auto r0 = __builtin_amdgcn_permlane32_swap(pk[4 * c + 0], pk[4 * c + 2], false, false);
                auto r1 = __builtin_amdgcn_permlane32_swap(pk[4 * c + 1], pk[4 * c + 3], false, false);
                ST16(((u32x4){r0[0], r1[0], r0[1], r1[1]}), dst + c * 512);
            }
        }
        if (wid >= 4) { WAIT_V(2); }
        WAIT_L(0); BAR;
#undef AT_ISSUE
#undef AT_STEP
#undef HM
#undef H_ST
#undef EX
#undef BC8
    }

    {
        const int tid = opaque_tid(), lane = tid & 63, tile = rt2 * 8 + ct2;
        GAS u64* myflag = FL(F_G2 + tile * 2 + kh); const GAS u64* pflag = FL(F_G2 + tile * 2 + (kh ^ 1));
        if (wid >= 4) {
            const int lw = wid - 4;
            G2_OFFS(lw, lane);
            const unsigned vH = ((lw >> 1) * 4 + 2 * kh + (lw & 1)) * KB_AH * 1024 + lane * 16;
            g128_issue<false>(lds0, g2, 2, lw, vA, vB); g128_issue<false>(lds0, g2, 3, lw, vA, vB);
            WAIT_V(16);
            for (int t = 0; t < 28; ++t) {
                BAR;
                if (t == 0 && wid == 4 && lane == 0) flag_st(FL(F_AT + (idx & 7) * 32 + xcd * 4 + (idx >> 3)), tag);
                if (t == 12 && kh == 0) flag_wait(FL(F_AT + (rt2 >> 1) * 32 + (lane & 31)), tag);
                g128_issue<false>(lds0, g2, t + 4, lw, vA, vB);
                WAIT_V(16);
            }
            BAR; g128_issue<true>(lds0, g2, 32, lw, vH, vB); WAIT_V(14);
            for (int t = 29; t < 35; ++t) {
                BAR;
                if (t == 34 && wid == 4 && lane == 0) flag_st(myflag, tag);
                g128_issue<true>(lds0, g2, t + 4, lw, vH, vB); WAIT_V(12);
            }
            BAR; g128_issue<true>(lds0, g2, 39, lw, vH, vB);
            const u64 seen = flag_ld(pflag);
            WAIT_V(13);
            BAR; WAIT_V(7);
            BAR; WAIT_V(0);
            const char* psrc = ws + WS_PART + tile * 32768 + (kh ^ 1) * 16384 + lw * 4096;
            const bool early = __all(seen == tag);
            if (early) { glds_pair_sc1<131072>(psrc, lane * 16, lds0 + lw * 4096); glds_pair_sc1<131072 + 2048>(psrc + 2048, lane * 16, lds0 + lw * 4096); }
            BAR;
            if (!early) { flag_wait(pflag, tag); glds_pair_sc1<131072>(psrc, lane * 16, lds0 + lw * 4096); glds_pair_sc1<131072 + 2048>(psrc + 2048, lane * 16, lds0 + lw * 4096); }
            WAIT_V(0); BAR;
            if (bid == 0 && wid == 4) {
#pragma unroll
                for (int i = 0; i < 4; ++i) flag_wait(FL(F_LN + i * 64 + lane), tag);
                if (lane == 0) flag_st(flags0, tag);
            }
        } else {
            const int wm = wid >> 1, wn = wid & 1, g = lane >> 4, q = (lane & 15) >> 2, p = lane & 3, swz = (2 * g + (q >> 1)) & 3;
            const int aoff = wm * 8192 + lane * 16;
            const int boff0 = 16384 + (4 * g + q) * 128 + (((2 * wn) ^ swz) * 32) + p * 8, boff1 = boff0 ^ 32;
            f32x4 acc[4][4]; bf16x8 AX[4], AY[4];
            s16x4 XL[4], XH[4], YL[4], YH[4];
            int pa[2] = {aoff, aoff + 65536}, pb0[2] = {boff0, boff0 + 65536}, pb1[2] = {boff1, boff1 + 65536};
            PIN(pa[1]); PIN(pb0[1]); PIN(pb1[1]);
#define G_BP(n, sl, kk) (lds + (((n) & 1) ? pb1[(sl) >> 1] : pb0[(sl) >> 1]) + ((sl) & 1) * 32768 + ((n) >> 1) * 8192 + (kk) * 4096)
#define G_BL(L_, n, sl, kk) do { L_[n] = trd(G_BP(n, sl, kk)); SCHED; } while (0)
#define G_BH(H_, n, sl, kk) do { H_[n] = trd(G_BP(n, sl, kk) + 2048); SCHED; } while (0)
#define G_A(A_, m, sl, kk) do { A_[m] = *(const LAS bf16x8*)(lds + pa[(sl) >> 1] + ((sl) & 1) * 32768 + ((m) * 2 + (kk)) * 1024); SCHED; } while (0)
#define G_M(A_, L_, H_, m, n) do { MFMA16_ACC(acc[m][n], cat8(L_[n], H_[n]), A_[m]); SCHED; } while (0)
#define G_REST(AC, CL, CH, AN, NL, NH, sl, kk) do { \
        G_BL(NL, 0, sl, kk); G_M(AC, CL, CH, 0, 2); G_BH(NH, 0, sl, kk); G_M(AC, CL, CH, 0, 3); G_A(AN, 0, sl, kk); G_M(AC, CL, CH, 1, 0); G_BL(NL, 1, sl, kk); G_M(AC, CL, CH, 1, 1); \
        G_BH(NH, 1, sl, kk); G_M(AC, CL, CH, 1, 2); G_BL(NL, 2, sl, kk); G_M(AC, CL, CH, 1, 3); G_BH(NH, 2, sl, kk); G_M(AC, CL, CH, 2, 0); G_BL(NL, 3, sl, kk); G_M(AC, CL, CH, 2, 1); \
        G_BH(NH, 3, sl, kk); G_M(AC, CL, CH, 2, 2); G_A(AN, 1, sl, kk); G_M(AC, CL, CH, 2, 3); G_A(AN, 2, sl, kk); G_M(AC, CL, CH, 3, 0); G_A(AN, 3, sl, kk); G_M(AC, CL, CH, 3, 1); \
        G_M(AC, CL, CH, 3, 2); G_M(AC, CL, CH, 3, 3); } while (0)
#define G_TILE(sl) do { G_M(AX, XL, XH, 0, 0); G_M(AX, XL, XH, 0, 1); G_REST(AX, XL, XH, AY, YL, YH, sl, 1); \
        G_M(AY, YL, YH, 0, 0); G_M(AY, YL, YH, 0, 1); \
        asm volatile("s_waitcnt vmcnt(0) lgkmcnt(0)" ::: "memory"); BAR; SCHED;        \
        G_REST(AY, YL, YH, AX, XL, XH, ((sl) + 1) & 3, 0); } while (0)
#pragma unroll
            for (int m = 0; m < 4; ++m)
#pragma unroll
                for (int n = 0; n < 4; ++n) { acc[m][n] = (f32x4){0.f, 0.f, 0.f, 0.f}; PIN(acc[m][n]); }
            SCHED;
#pragma unroll
            for (int i = 0; i < 4; ++i) { G_BL(XL, i, 0, 0); G_BH(XH, i, 0, 0); G_A(AX, i, 0, 0); }
            for (int t = 0; t < 32; t += 4) { G_TILE(0); G_TILE(1); G_TILE(2); G_TILE(3); }
            asm volatile("s_nop 7"); SCHED;
            const int fr = lane & 15, fq = lane >> 4;
            const size_t rowo = (size_t)(rt2 * 128 + wm * 64 + kh * 32 + fr) * DM + ct2 * 128 + wn * 32 + (fq & 1) * 16 + (fq >> 1) * 8;
            u32x4 xv[2][2];
#pragma unroll
            for (int m = 0; m < 2; ++m)
#pragma unroll
                for (int s = 0; s < 2; ++s) ST16(pack8(acc[2 + m][2 * s], acc[2 + m][2 * s + 1]), WS_PART + tile * 32768 + kh * 16384 + (m * 2 + s) * 4096 + tid * 16);
#pragma unroll
            for (int m = 0; m < 2; ++m)
#pragma unroll
                for (int s = 0; s < 2; ++s) xv[m][s] = *(const u32x4*)(x + rowo + (size_t)m * 16 * DM + s * 64);
            SCHED;
#define H_REST(AC, CL, CH, AN, NL, NH, sl, kk) do { \
        G_BL(NL, 0, sl, kk); G_BH(NH, 0, sl, kk); G_M(AC, CL, CH, 0, 2); G_A(AN, 0, sl, kk); G_BL(NL, 1, sl, kk); G_M(AC, CL, CH, 0, 3); G_BH(NH, 1, sl, kk); G_BL(NL, 2, sl, kk); G_M(AC, CL, CH, 1, 0); \
        G_BH(NH, 2, sl, kk); G_BL(NL, 3, sl, kk); G_M(AC, CL, CH, 1, 1); G_BH(NH, 3, sl, kk); G_M(AC, CL, CH, 1, 2); G_A(AN, 1, sl, kk); G_M(AC, CL, CH, 1, 3); } while (0)
#define H_TILE(sl, SYNC) do { G_M(AX, XL, XH, 0, 0); G_M(AX, XL, XH, 0, 1); H_REST(AX, XL, XH, AY, YL, YH, sl, 1); \
        G_M(AY, YL, YH, 0, 0); G_M(AY, YL, YH, 0, 1); SYNC; SCHED; H_REST(AY, YL, YH, AX, XL, XH, ((sl) + 1) & 3, 0); } while (0)
#define H_BAR do { WAIT_L(0); BAR; } while (0)
            H_TILE(0, H_BAR); H_TILE(1, H_BAR); H_TILE(2, WAIT_V(0); H_BAR); H_TILE(3, H_BAR);
            H_TILE(0, H_BAR); H_TILE(1, H_BAR); H_TILE(2, H_BAR); H_TILE(3, );
#undef G_BP
#undef G_BL
#undef G_BH
#undef G_A
#undef G_M
#undef G_REST
#undef G_TILE
#undef H_REST
#undef H_TILE
#undef H_BAR
            asm volatile("s_nop 7"); SCHED;
            BAR; CBAR;
            u32x4 pv[2][2];
#pragma unroll
            for (int m = 0; m < 2; ++m)
#pragma unroll
                for (int s = 0; s < 2; ++s) pv[m][s] = *(const LAS u32x4*)(lds + 131072 + (m * 2 + s) * 4096 + tid * 16);
#pragma unroll
            for (int m = 0; m < 2; ++m)
#pragma unroll
                for (int s = 0; s < 2; ++s) { float lo[4], hi4[4]; const u32x4 p_ = pv[m][s], x_ = xv[m][s];
                    const f32x4 a0 = acc[m][2 * s] + (f32x4){bf_lo(p_[0]), bf_hi(p_[0]), bf_lo(p_[1]), bf_hi(p_[1])}, a1 = acc[m][2 * s + 1] + (f32x4){bf_lo(p_[2]), bf_hi(p_[2]), bf_lo(p_[3]), bf_hi(p_[3])};
#pragma unroll
                    for (int i = 0; i < 4; ++i) { auto r = __builtin_amdgcn_permlane16_swap(__float_as_uint(a0[i]), __float_as_uint(a1[i]), false, false);
                        lo[i] = __uint_as_float(r[0]); hi4[i] = __uint_as_float(r[1]); }
                    u32x4 o; o[0] = cvtpk(lo[0] + bf_lo(x_[0]), lo[1] + bf_hi(x_[0])); o[1] = cvtpk(lo[2] + bf_lo(x_[1]), lo[3] + bf_hi(x_[1]));
                    o[2] = cvtpk(hi4[0] + bf_lo(x_[2]), hi4[1] + bf_hi(x_[2])); o[3] = cvtpk(hi4[2] + bf_lo(x_[3]), hi4[3] + bf_hi(x_[3]));
                    __builtin_nontemporal_store(o, (u32x4*)(out + rowo + (size_t)m * 16 * DM + s * 64));
                }
        }
    }
    { const unsigned e_ = (unsigned)__builtin_amdgcn_s_memrealtime(); while ((unsigned)__builtin_amdgcn_s_memrealtime() - e_ < 2 * (rb_ - ra_)) __builtin_amdgcn_s_sleep(8); }
}

extern "C" void kernel_launch(void* const* d_in, const int* in_sizes, int n_in, void* d_out, int out_size, void* d_ws, size_t ws_size, hipStream_t stream) {
    (void)hipFuncSetAttribute((const void*)k_block, hipFuncAttributeMaxDynamicSharedMemorySize, LDS_BYTES);
    k_block<<<256, 512, LDS_BYTES, stream>>>((const u16*)d_in[0], (const u16*)d_in[1], (const u16*)d_in[2], (const u16*)d_in[4], (const u16*)d_in[5], (const u16*)d_in[6], (char*)d_ws, (u16*)d_out);
}
```

```cpp
#include <hip/hip_runtime.h>
#include <hip/hip_bf16.h>
#include <stdint.h>

#define LAS __attribute__((address_space(3)))
#define GAS __attribute__((address_space(1)))
#define DI __device__ __forceinline__
typedef unsigned short u16;
typedef unsigned long long u64;
typedef short bf16x8 __attribute__((ext_vector_type(8)));
typedef short s16x4 __attribute__((ext_vector_type(4)));
typedef float f32x2 __attribute__((ext_vector_type(2)));
typedef float f32x4 __attribute__((ext_vector_type(4)));
typedef float f32x16 __attribute__((ext_vector_type(16)));
typedef unsigned u32x2 __attribute__((ext_vector_type(2)));
typedef unsigned u32x4 __attribute__((ext_vector_type(4)));
typedef __bf16 bf16x2_t __attribute__((ext_vector_type(2)));

constexpr int SEQ = 2048, DM = 1024, FF = 3584;
constexpr int KB_X = DM / 32;
constexpr int KB_AH = (DM + FF) / 32;
constexpr unsigned WS_XN = 0;
constexpr unsigned WS_Q = WS_XN + SEQ * DM * 2;
constexpr unsigned WS_K = WS_Q + SEQ * DM * 2;
constexpr unsigned WS_V = WS_K + SEQ * DM * 2;
constexpr unsigned WS_AH = WS_V + SEQ * DM * 2;
constexpr unsigned WS_PART = WS_AH + SEQ * (DM + FF) * 2;
constexpr unsigned WS_FLAG = WS_PART + 128 * 32768;
constexpr unsigned WS_END = WS_FLAG + 2048 * 128;
constexpr int F_LN = 32, F_QKV = F_LN + 256, F_H = F_QKV + 256, F_AT = F_H + 256, F_G2 = F_AT + 256;
constexpr int LDS_BYTES = 163840;

DI unsigned cvtpk(float lo, float hi) { f32x2 v = {lo, hi}; bf16x2_t b = __builtin_convertvector(v, bf16x2_t); return __builtin_bit_cast(unsigned, b); }
DI float bf_lo(unsigned u) { return __uint_as_float(u << 16); }
DI float bf_hi(unsigned u) { return __uint_as_float(u & 0xffff0000u); }
DI float rbf(float f) { return __uint_as_float(cvtpk(f, f) << 16); }
DI s16x4 trd(const LAS char* p) { return __builtin_amdgcn_ds_read_tr16_b64_v4i16((LAS s16x4*)p); }
DI bf16x8 cat8(s16x4 a, s16x4 b) { return __builtin_shufflevector(a, b, 0, 1, 2, 3, 4, 5, 6, 7); }
template <int OFF, int D> DI void glds(const char* base, unsigned voff, unsigned dst) {
    asm volatile("s_add_u32 m0, %2, %4\n\ts_nop 0\n\tglobal_load_lds_dwordx4 %0, %1 offset:%3" :: "v"(voff), "s"(base), "s"(dst), "i"(OFF), "i"(D - OFF) : "memory", "scc"); }
template <int D> DI void glds_pair_sc1(const char* base, unsigned voff, unsigned dst) {
    asm volatile("s_add_u32 m0, %2, %3\n\ts_nop 0\n\tglobal_load_lds_dwordx4 %0, %1 sc1\n\tglobal_load_lds_dwordx4 %0, %1 offset:1024 sc1" :: "v"(voff), "s"(base), "s"(dst), "i"(D) : "memory", "scc"); }
template <int D> DI void touch(const char* base, unsigned voff, unsigned dst) {
    asm volatile("s_add_u32 m0, %2, %3\n\ts_nop 0\n\tglobal_load_lds_dword %0, %1" :: "v"(voff), "s"(base), "s"(dst), "i"(D) : "memory", "scc"); }
#define WAIT_V(n) asm volatile("s_waitcnt vmcnt(" #n ")" ::: "memory")
#define WAIT_L(n) asm volatile("s_waitcnt lgkmcnt(" #n ")" ::: "memory")
#define BAR __builtin_amdgcn_s_barrier()
#define CBAR asm volatile("" ::: "memory")
#define SCHED __builtin_amdgcn_sched_barrier(0)
#define PIN(x) asm volatile("" : "+v"(x))
#define MFMA16(a, b, c) __builtin_amdgcn_mfma_f32_16x16x32_bf16(a, b, c, 0, 0, 0)
#define MFMA16_ACC(c, a, b) asm volatile("v_mfma_f32_16x16x32_bf16 %0, %1, %2, %0" : "+v"(c) : "v"(a), "v"(b))
#define MFMA32(a, b, c) __builtin_amdgcn_mfma_f32_32x32x16_bf16(a, b, c, 0, 0, 0)
#define ST16(v, off) __builtin_amdgcn_raw_buffer_store_b128(v, rs, (int)(off), 0, 16)
#define LD16(off) __builtin_amdgcn_raw_buffer_load_b128(rs, (int)(off), 0, 16)
DI u64 flag_ld(const GAS u64* p) { return __hip_atomic_load(p, __ATOMIC_RELAXED, __HIP_MEMORY_SCOPE_AGENT); }
DI void flag_st(GAS u64* p, u64 v) { __hip_atomic_store(p, v, __ATOMIC_RELAXED, __HIP_MEMORY_SCOPE_AGENT); }
DI void flag_wait(const GAS u64* p, u64 tag) {
    for (unsigned spins = 0; spins < (1u << 18); ++spins) { if (__all(flag_ld(p) == tag)) break; __builtin_amdgcn_s_sleep(1); }
    CBAR;
}
template <int N> DI float ror_add(float v) { return v + __builtin_bit_cast(float, __builtin_amdgcn_update_dpp(0, __builtin_bit_cast(int, v), 0x120 + N  , 0xf, 0xf, false)); }
DI float sum64(float v) {
    v = ror_add<1>(ror_add<2>(ror_add<4>(ror_add<8>(v))));
    { auto r = __builtin_amdgcn_permlane16_swap(__float_as_uint(v), __float_as_uint(v), false, false); v = __uint_as_float(r[0]) + __uint_as_float(r[1]); }
    { auto r = __builtin_amdgcn_permlane32_swap(__float_as_uint(v), __float_as_uint(v), false, false); v = __uint_as_float(r[0]) + __uint_as_float(r[1]); }
    return v;
}
DI u32x4 widen(u32x2 a, u32x2 b) { auto r0 = __builtin_amdgcn_permlane16_swap(a[0], b[0], false, false); auto r1 = __builtin_amdgcn_permlane16_swap(a[1], b[1], false, false);
    return (u32x4){r0[0], r1[0], r0[1], r1[1]}; }
DI int opaque_tid() { int t = threadIdx.x; asm volatile("" : "+v"(t)); return t; }

DI float silu_mul(float g, float u) { return g * u * __builtin_amdgcn_rcpf(1.0f + __builtin_amdgcn_exp2f(g * -1.4426950408889634f)); }
DI u32x4 pack8(const f32x4& a, const f32x4& b) { u32x4 o; o[0] = cvtpk(a[0], a[1]); o[1] = cvtpk(a[2], a[3]); o[2] = cvtpk(b[0], b[1]); o[3] = cvtpk(b[2], b[3]); return o; }
constexpr float QSCALE = 0.17677669529663687f * 1.4426950408889634f;


struct G128 { const char* a; const char* w2; const char* wo; int kh; };
template <bool HALF> DI void g128_issue(unsigned lds0, const G128& g, int t, int lw, unsigned voffA, unsigned voffB) {
    const unsigned so = lds0 + (t & 3) * 32768, sb = so + lw * 2048;
    const bool att = !HALF && g.kh == 0 && t >= 16;
    const int kt = HALF ? t - 16 : att ? t - 16 : g.kh * 24 + t;
    const char* a = g.a + (size_t)(att ? kt : 16 + kt) * 2048; const char* b = (att ? g.wo : g.w2) + (size_t)kt * (64 * DM * 2);
    if (HALF) glds_pair_sc1<0>(a, voffA, so + (lw >> 1) * 8192 + (lw & 1) * 2048);
    else { glds_pair_sc1<0>(a, voffA, so + lw * 4096); glds_pair_sc1<2048>(a + KB_AH * 1024, voffA, so + lw * 4096); }
    glds<0, 16384>(b, voffB, sb); glds<128, 24576>(b, voffB, sb);
    glds<0, 16384 + 1024>(b + 8 * DM * 2, voffB, sb); glds<128, 24576 + 1024>(b + 8 * DM * 2, voffB, sb);
}

__global__ __launch_bounds__(512, 2) void k_block(const u16* __restrict__ x, const u16* __restrict__ wqkv, const u16* __restrict__ wo, const u16* __restrict__ w1,
                                                  const u16* __restrict__ w3, const u16* __restrict__ w2, char* __restrict__ ws, u16* __restrict__ out) {
    extern __shared__ __attribute__((aligned(16))) char smem[];
    LAS char* lds = (LAS char*)smem;
    const unsigned lds0 = (unsigned)(size_t)lds;
    const int wid = __builtin_amdgcn_readfirstlane(threadIdx.x >> 6);
    const int bid = blockIdx.x, xcd = bid & 7, idx = bid >> 3;
    GAS u64* flags0 = (GAS u64*)(ws + WS_FLAG);
#define FL(i) (flags0 + (i) * 16)
    const __amdgpu_buffer_rsrc_t rs = __builtin_amdgcn_make_buffer_rsrc((void*)ws, 0, WS_END, 0x00020000);
    {
        const char* pc; asm volatile("s_getpc_b64 %0" : "=s"(pc));
        if (wid == 7) touch<148480>(pc + (idx & 7) * 4096, (threadIdx.x & 63) * 64, lds0 + 7 * 1024);
    }
    u64 tag;
    u32x4 xa, xb; u32x2 ep;
    { const char* xp = (const char*)x + (size_t)(bid * 8 + wid) * (DM * 2);
      asm volatile("global_load_dwordx4 %0, %3, %4\n\tglobal_load_dwordx4 %1, %3, %4 offset:1024\n\tglobal_load_dwordx2 %2, %5, %6 sc1"
                   : "=&v"(xa), "=&v"(xb), "=&v"(ep) : "v"((threadIdx.x & 63) * 16), "s"(xp), "v"(0), "s"(ws + WS_FLAG), "s"(wqkv), "s"(w1), "s"(w3) : "memory"); }
    const int cj = idx >> 2, ct = cj < 3 ? (xcd >> 1) * 3 + cj : 12 + (xcd >> 1) * 5 + (cj - 3), rt = (xcd & 1) * 4 + (idx & 3);
    const bool qkv = ct < 12;
    u32x4 hmain[8], hext[2];

    {
        const int tid = opaque_tid(), lane = tid & 63, wr = wid >> 2, wc = wid & 3;
        const unsigned ldb2 = qkv ? 3 * DM * 2 : FF * 2;
        const char* cB0 = qkv ? (const char*)wqkv + ct * 512 : (const char*)w1 + (ct - 12) * 256;
        const char* cB1 = qkv ? cB0 + 256 : (const char*)w3 + (ct - 12) * 256;
        const char* cA = ws + WS_XN + rt * 16 * KB_X * 1024;
        const unsigned voffA = wid * (KB_X * 1024) + lane * 16;
        const unsigned voffB = (unsigned)(wid * 8 + (lane >> 3)) * ldb2 + (((lane & 7) ^ (2 * ((lane >> 4) & 3))) * 16);
        const char* pE = ((lane & 1) ? (const char*)w3 : (const char*)w1) + (size_t)(wid * 8 + (lane >> 3)) * (FF * 2) + (2560 + ct * 32) * 2 + ((((lane >> 1) & 3) ^ ((lane >> 4) & 3)) * 16);
        const size_t kstepB = (size_t)64 * ldb2;
        const unsigned ldsA = lds0 + wid * 2048, ldsB = lds0 + wid * 1024;
        const char* cT = ((idx & 2) ? cB1 : cB0) + (idx & 1) * 128;
#define SA(b, h) (((b) * 2 + (h)) * 16384)
#define SB(b, h) (((b) * 2 + (h)) * 16384)
#define STA(b, h, kt) glds_pair_sc1<SA(b, h)>(cA + (h) * 8 * KB_X * 1024 + (kt) * 2048, voffA, ldsA)
#define STB(b, h, kt) do { const char* s_ = ((h) ? cB1 : cB0) + (size_t)(kt) * kstepB; \
        glds<0, 65536 + SB(b, h)>(s_, voffB, ldsB); glds<128, 65536 + SB(b, h) + 8192>(s_, voffB, ldsB); } while (0)
#define TCH(kt) touch<148480>(cT + (size_t)(kt) * kstepB, voffB, ldsB)
#define STE(b, kt) do { const char* s_ = pE + (size_t)(kt) * (64 * FF * 2); \
        asm volatile("s_add_u32 m0, %1, %2\n\ts_nop 0\n\tglobal_load_lds_dwordx4 %0, off" :: "v"(s_), "s"(ldsB), "i"(131072 + (b) * 8192) : "memory", "scc"); } while (0)
        {
            TCH(0); TCH(1);
            { const char* s_ = pE + (size_t)(idx & 1) * (64 * FF * 2) + (ptrdiff_t)(((idx >> 1) & 1) - (wid >> 2)) * (32 * FF * 2);
              asm volatile("s_add_u32 m0, %1, 148480\n\ts_nop 0\n\tglobal_load_lds_dword %0, off" :: "v"(s_), "s"(ldsB) : "memory", "scc"); }
            asm volatile("s_waitcnt vmcnt(3)" : "+v"(xa), "+v"(xb), "+v"(ep) :: "memory");
            tag = (((u64)(unsigned)__builtin_amdgcn_readfirstlane(ep[1]) << 32) | (unsigned)__builtin_amdgcn_readfirstlane(ep[0])) + 1;
            float f[16];
#pragma unroll
            for (int i = 0; i < 4; ++i) { f[2 * i] = bf_lo(xa[i]); f[2 * i + 1] = bf_hi(xa[i]); f[8 + 2 * i] = bf_lo(xb[i]); f[9 + 2 * i] = bf_hi(xb[i]); }
            float s = 0.f;
#pragma unroll
            for (int i = 0; i < 16; ++i) s += f[i];
            const float mean = sum64(s) * (1.0f / DM);
            float q = 0.f;
#pragma unroll
            for (int i = 0; i < 16; ++i) { const float d = f[i] - mean; q += d * d; }
            const float var = sum64(q) * (1.0f / DM);
            const float mb = rbf(mean), sd = rbf(__builtin_amdgcn_sqrtf(rbf(rbf(var) + 1e-5f))), inv = __builtin_amdgcn_rcpf(sd);
#pragma unroll
            for (int c = 0; c < 2; ++c) { u32x4 o;
#pragma unroll
                for (int i = 0; i < 4; ++i) o[i] = cvtpk(rbf(f[c * 8 + 2 * i] - mb) * inv, rbf(f[c * 8 + 2 * i + 1] - mb) * inv);
                *(LAS u32x4*)(lds + wid * 2080 + c * 1024 + lane * 16) = o; }
            WAIT_L(0); BAR; CBAR;
            const int r = lane & 7, g = (lane >> 3) & 3, row = bid * 8 + r;
#pragma unroll
            for (int c = 0; c < 2; ++c) { const int kb = 4 * wid + 2 * c + (lane >> 5); const LAS char* p = lds + r * 2080 + kb * 64 + g * 8;
                const u32x2 lo = *(const LAS u32x2*)p, hi = *(const LAS u32x2*)(p + 32);
                ST16(((u32x4){lo[0], lo[1], hi[0], hi[1]}), WS_XN + ((row >> 4) * KB_X + kb) * 1024 + (g * 16 + (row & 15)) * 16); }
            CBAR;
            STB(0, 0, 0); STB(0, 1, 0); STE(0, 0);
            WAIT_V(5); BAR;
            if (wid == 0) { if (lane == 0) flag_st(FL(F_LN + bid), tag); flag_wait(FL(F_LN + rt * 32 + (lane & 31)), tag); }
            STB(1, 0, 1); STB(1, 1, 1); STE(1, 1);
            if (wid != 0) { TCH(2); TCH(3); }
            BAR; CBAR;
        }
        f32x4 acc[2][2][4][2], ace[2][4];
        {
            const int g = lane >> 4, q = (lane & 15) >> 2, p = lane & 3, swz = (2 * g + (q >> 1)) & 3;
            const int aoff = wr * 8192 + lane * 16;
            const int boff0 = 65536 + (wc >> 1) * 8192 + (4 * g + q) * 128 + (((2 * (wc & 1)) ^ swz) * 32) + p * 8, boff1 = boff0 ^ 32;
            int eoff = 131072 + (4 * g + q) * 128 + ((((wc >> 1) + 2 * (p & 1)) ^ swz) * 32) + (p >> 1) * 16 + (wc & 1) * 8; PIN(eoff);
#define LDA(dst, b, h) do { _Pragma("unroll") for (int m = 0; m < 4; ++m) _Pragma("unroll") for (int k = 0; k < 2; ++k) \
        dst[m][k] = *(const LAS bf16x8*)(lds + aoff + SA(b, h) + (m * 2 + k) * 1024); } while (0)
#define LDB(dst, b, h) do { _Pragma("unroll") for (int n = 0; n < 2; ++n) _Pragma("unroll") for (int k = 0; k < 2; ++k) { \
        const LAS char* b_ = lds + (n ? boff1 : boff0) + SB(b, h) + k * 4096; dst[n][k] = cat8(trd(b_), trd(b_ + 2048)); } } while (0)
#define LDE(dst, b) do { _Pragma("unroll") for (int k = 0; k < 2; ++k) { const LAS char* b_ = lds + eoff + (b) * 8192 + k * 4096; dst[k] = cat8(trd(b_), trd(b_ + 2048)); } } while (0)
#define MMA(ai) do { __builtin_amdgcn_s_setprio(1); \
        _Pragma("unroll") for (int k = 0; k < 2; ++k) _Pragma("unroll") for (int m = 0; m < 4; ++m) { \
            MFMA16_ACC(acc[ai][0][m][0], B0[0][k], At[m][k]); MFMA16_ACC(acc[ai][0][m][1], B0[1][k], At[m][k]); \
            MFMA16_ACC(acc[ai][1][m][0], B1[0][k], At[m][k]); MFMA16_ACC(acc[ai][1][m][1], B1[1][k], At[m][k]); \
            MFMA16_ACC(ace[ai][m], BE[k], At[m][k]); } \
        __builtin_amdgcn_s_setprio(0); } while (0)
            STA(0, 0, 0); STA(0, 1, 0); STA(1, 0, 1);
#pragma unroll
            for (int a = 0; a < 2; ++a)
#pragma unroll
                for (int m = 0; m < 4; ++m) { ace[a][m] = (f32x4){0.f, 0.f, 0.f, 0.f}; PIN(ace[a][m]);
#pragma unroll
                    for (int b = 0; b < 2; ++b)
#pragma unroll
                        for (int n = 0; n < 2; ++n) { acc[a][b][m][n] = (f32x4){0.f, 0.f, 0.f, 0.f}; PIN(acc[a][b][m][n]); } }
            bf16x8 At[4][2], B0[2][2], B1[2][2], BE[2];
            constexpr int nt = DM / 64;
            if (wr == 1) BAR;
            WAIT_V(4); BAR;
            WAIT_V(2); BAR;
            for (int t = 0; t < nt; t += 2) {
                const bool more = t + 2 < nt, far = t + 4 < nt;
                LDB(B0, 0, 0); LDB(B1, 0, 1); LDE(BE, 0); SCHED; LDA(At, 0, 0); STA(1, 1, t + 1);
                if (far) { TCH(t + 4); WAIT_V(11); } else { WAIT_V(9); }
                WAIT_L(0); BAR; MMA(0); BAR; SCHED;
                LDA(At, 0, 1);
                if (more) { STB(0, 0, t + 2); STB(0, 1, t + 2); STE(0, t + 2); STA(0, 0, t + 2); if (far) { WAIT_V(10); } else { WAIT_V(9); } } else { WAIT_V(2); }
                WAIT_L(0); BAR; MMA(1); BAR; SCHED;
                LDB(B0, 1, 0); LDB(B1, 1, 1); LDE(BE, 1); SCHED; LDA(At, 1, 0);
                if (more) { STA(0, 1, t + 2); if (far) { TCH(t + 5); WAIT_V(11); } else { WAIT_V(9); } } else { WAIT_V(0); }
                WAIT_L(0); BAR; MMA(0); BAR; SCHED;
                LDA(At, 1, 1);
                if (more) { STB(1, 0, t + 3); STB(1, 1, t + 3); STE(1, t + 3); STA(1, 0, t + 3); if (far) { WAIT_V(10); } else { WAIT_V(9); } }
                WAIT_L(0); BAR; MMA(1); if (more || wr == 0) BAR; SCHED;
            }
            asm volatile("s_nop 7"); SCHED;
#undef SA
#undef SB
#undef STA
#undef STB
#undef STE
#undef TCH
#undef LDA
#undef LDB
#undef LDE
#undef MMA
        }
        const int ln = opaque_tid() & 63, fr = ln & 15, fq = ln >> 4;
        if (qkv) {
            const int kind = ct >> 2;
            const float sc = kind == 0 ? QSCALE : 1.0f;
            const unsigned base = WS_Q + kind * (SEQ * DM * 2) + ((ct & 3) * 8 + wc) * 131072 + (rt * 256 + wr * 64) * 64;
            const unsigned lo0 = base + (kind == 2 ? fr * 64 + fq * 16 : fr * 16 + fq * 512), lo1 = lo0 + (kind == 2 ? 1024 : 256);
#pragma unroll
            for (int bj = 0; bj < 2; ++bj)
#pragma unroll
                for (int ai = 0; ai < 2; ++ai)
#pragma unroll
                    for (int m = 0; m < 4; ++m)
                        ST16(pack8(acc[ai][bj][m][0] * sc, acc[ai][bj][m][1] * sc), bj * 4 * 131072 + ai * 8192 + (m >> 1) * 2048 + ((m & 1) ? lo1 : lo0));
            WAIT_V(0); BAR;
            if (wid == 0 && ln == 0) flag_st(FL(F_QKV + rt * 32 + ct), tag);
        } else {
#pragma unroll
            for (int ai = 0; ai < 2; ++ai)
#pragma unroll
                for (int m = 0; m < 4; ++m) { f32x4 h0, h1;
#pragma unroll
                    for (int j = 0; j < 4; ++j) { h0[j] = silu_mul(acc[ai][0][m][0][j], acc[ai][1][m][0][j]); h1[j] = silu_mul(acc[ai][0][m][1][j], acc[ai][1][m][1][j]); }
                    hmain[ai * 4 + m] = pack8(h0, h1);
                }
        }
#pragma unroll
        for (int ai = 0; ai < 2; ++ai) { u32x2 e[2];
#pragma unroll
            for (int mp = 0; mp < 2; ++mp) { float h[4];
#pragma unroll
                for (int j = 0; j < 4; ++j) { auto r = __builtin_amdgcn_permlane32_swap(__float_as_uint(ace[ai][2 * mp][j]), __float_as_uint(ace[ai][2 * mp + 1][j]), false, false);
                    h[j] = silu_mul(__uint_as_float(r[0]), __uint_as_float(r[1])); }
                e[mp] = (u32x2){cvtpk(h[0], h[1]), cvtpk(h[2], h[3])}; }
            hext[ai] = widen(e[0], e[1]); }
    }

    const int kh = xcd & 1, rt2 = ((xcd >> 1) & 1) * 8 + (idx & 7), ct2 = (xcd >> 2) * 4 + (idx >> 3);
    G128 g2; g2.kh = kh; g2.a = ws + WS_AH + rt2 * 8 * KB_AH * 1024; g2.w2 = (const char*)w2 + ct2 * 256; g2.wo = (const char*)wo + ct2 * 256;
#define G2_OFFS(lw_, l_) const unsigned vA = ((lw_) ^ kh) * 2 * KB_AH * 1024 + (l_) * 16, vB = ((lw_) * 16 + ((l_) >> 3)) * (DM * 2) + ((((l_) & 7) ^ (2 * (((l_) >> 4) & 3))) * 16)

    {
        constexpr int AT_BASE = 65536, AT_STAGE = 32768;
        const int lane = opaque_tid() & 63, hi = lane >> 5;
        const int head = xcd * 4 + (idx >> 3), qb = idx & 7;
        if (wid == 0) flag_wait(FL(F_QKV + (lane < 4 ? (lane & 1) * 32 + 4 + (lane >> 1) * 4 : qb * 32) + (head >> 3)), tag);
        BAR; CBAR;
        const char* Kg = ws + WS_K + head * 131072; const char* Vg = ws + WS_V + head * 131072;
        const unsigned soff = wid * 2048 + lane * 16;
        const unsigned sdst = lds0 + AT_BASE + wid * 2048;
#define AT_ISSUE(s, slot) do { glds_pair_sc1<0>(Kg + (s) * 16384, soff, sdst + (slot)); glds_pair_sc1<16384>(Vg + (s) * 16384, soff, sdst + (slot)); } while (0)
        const unsigned qoff = WS_Q + (head * 64 + qb * 8 + wid) * 2048 + lane * 16;
        const bf16x8 q0 = __builtin_bit_cast(bf16x8, LD16(qoff)), q1 = __builtin_bit_cast(bf16x8, LD16(qoff + 1024));
        AT_ISSUE(0, 0); AT_ISSUE(1, AT_STAGE);
        const GAS u64* kvflag = FL(F_QKV + (lane & 7) * 32 + 4 + ((lane >> 3) & 1) * 4 + (head >> 3));
        u64 seen = flag_ld(kvflag);
        bf16x8 sel; { const short one = ((lane & 15) == ((lane >> 4) & 1)) ? (short)0x3f80 : (short)0;
#pragma unroll
            for (int i = 0; i < 8; ++i) sel[i] = one; }
        const LAS char* kb0 = lds + AT_BASE + lane * 16;
        const LAS char* vb0 = lds + AT_BASE + 16384 + (4 * hi + ((lane & 15) >> 2)) * 64 + ((lane >> 4) & 1) * 32 + (lane & 3) * 8;
        f32x16 O, negm, SA, SB; f32x4 L = {0.f, 0.f, 0.f, 0.f};
        bf16x8 k0, k1, v0, v1;
#pragma unroll
        for (int i = 0; i < 16; ++i) { O[i] = 0.f; negm[i] = 0.f; }
        WAIT_V(5); BAR;
        {
            k0 = *(const LAS bf16x8*)(kb0); k1 = *(const LAS bf16x8*)(kb0 + 1024);
            SA = MFMA32(k0, q0, negm); SA = MFMA32(k1, q1, SA);
            k0 = *(const LAS bf16x8*)(kb0 + 2048); k1 = *(const LAS bf16x8*)(kb0 + 3072);
            v0 = cat8(trd(vb0), trd(vb0 + 512)); v1 = cat8(trd(vb0 + 1024), trd(vb0 + 1536));
            float m = SA[0];
#pragma unroll
            for (int i = 1; i < 16; ++i) m = fmaxf(m, SA[i]);
            m = fmaxf(m, __shfl_xor(m, 32));
#pragma unroll
            for (int i = 0; i < 16; ++i) { negm[i] = -m; SA[i] -= m; }
            PIN(negm);
        }
#define EX(S, i) S[i] = __builtin_amdgcn_exp2f(S[i])
#define BC8(x) __builtin_bit_cast(bf16x8, x)
#define AT_STEP(SC, SN, kp, v1p, v0p) do { \
        O = MFMA32(v1, BC8(p1), O); SCHED; v1 = cat8(trd(v1p), trd((v1p) + 512)); EX(SC, 0); EX(SC, 1); EX(SC, 2); PIN(SC); SCHED; \
        SN = MFMA32(k0, q0, negm); SCHED; k0 = *(const LAS bf16x8*)(kp); EX(SC, 3); EX(SC, 4); EX(SC, 5); PIN(SC); SCHED; \
        L = MFMA16(sel, BC8(p1), L); SCHED; EX(SC, 6); EX(SC, 7); \
        p0[0] = cvtpk(SC[0], SC[1]); p0[1] = cvtpk(SC[2], SC[3]); p0[2] = cvtpk(SC[4], SC[5]); p0[3] = cvtpk(SC[6], SC[7]); PIN(p0); SCHED; \
        O = MFMA32(v0, BC8(p0), O); SCHED; v0 = cat8(trd(v0p), trd((v0p) + 512)); EX(SC, 8); EX(SC, 9); EX(SC, 10); PIN(SC); SCHED; \
        SN = MFMA32(k1, q1, SN); SCHED; k1 = *(const LAS bf16x8*)((kp) + 1024); EX(SC, 11); EX(SC, 12); EX(SC, 13); PIN(SC); SCHED; \
        L = MFMA16(sel, BC8(p0), L); SCHED; EX(SC, 14); EX(SC, 15); \
        p1[0] = cvtpk(SC[8], SC[9]); p1[1] = cvtpk(SC[10], SC[11]); p1[2] = cvtpk(SC[12], SC[13]); p1[3] = cvtpk(SC[14], SC[15]); PIN(p1); SCHED; } while (0)
        u32x4 p0, p1 = {0u, 0u, 0u, 0u};
        int cur = 0, nxt = AT_STAGE, fre = 2 * AT_STAGE;
        const GAS u64* hflag = FL(F_H + (rt2 >> 1) * 32 + (lane & 31));
        const unsigned hb = WS_AH + ((rt * 16 + (wid >> 2) * 4) * KB_AH + KB_X + (ct - 12) * 4 + (wid & 3)) * 1024 + lane * 16;
        const unsigned eb = WS_AH + ((rt * 16 + (wid >> 2) * 4 + 2 * ((lane >> 4) & 1) + hi) * KB_AH + KB_X + 80 + ct) * 1024 + ((wid & 3) * 16 + (lane & 15)) * 16;
#define HM(i) ST16(hmain[i], hb + (((i) >> 2) * 8 + ((i) & 3)) * KB_AH * 1024)
#define H_ST(j) do { if (s == 4) ST16(hext[j], eb + (j) * 8 * KB_AH * 1024); else if (!qkv && s < 4) { if (s == 0) HM(j); else if (s == 1) HM(2 + (j)); else if (s == 2) HM(4 + (j)); else HM(6 + (j)); } SCHED; } while (0)
        for (int s = 0; s < 8; ++s) {
            if (s == 1) { if (qkv) { WAIT_V(0); } else { WAIT_V(2); } } else if (s >= 2 && s <= 4) { if (qkv) { WAIT_V(1); } else { WAIT_V(3); } } else if (s == 5) { WAIT_V(3); } else { WAIT_V(0); }
            WAIT_L(0); BAR;
            if (s == 0) { if (!__all(seen == tag)) flag_wait(kvflag, tag); }
            if (s + 2 < 8) glds_pair_sc1<0>(Kg + (s + 2) * 16384, soff, sdst + fre);
            if (s == 6) { if (wid == 0 && lane == 0) flag_st(FL(F_H + rt * 32 + ct), tag); }
            if (s == 7) seen = flag_ld(hflag);
            if (wid >= 4) __builtin_amdgcn_s_setprio(1);
            const LAS char* kc = kb0 + cur; const LAS char* vc = vb0 + cur; const LAS char* kn = kb0 + nxt; const LAS char* vn = vb0 + nxt;
            AT_STEP(SA, SB, kc + 2 * 2048, vc + 1024, vc + 1 * 2048);
            if (s + 2 < 8) glds_pair_sc1<16384>(Vg + (s + 2) * 16384, soff, sdst + fre);
            SCHED;
            AT_STEP(SB, SA, kc + 3 * 2048, vc + 1 * 2048 + 1024, vc + 2 * 2048);
            H_ST(0);
            AT_STEP(SA, SB, kc + 4 * 2048, vc + 2 * 2048 + 1024, vc + 3 * 2048); AT_STEP(SB, SA, kc + 5 * 2048, vc + 3 * 2048 + 1024, vc + 4 * 2048);
            if (s >= 1 && s < 6) {
                const int r0 = ((s - 1) * 256 + idx * 8 + wid) * 2;
                const char* src = (kh ? (const char*)w2 + 1024 * (DM * 2) : r0 >= 1536 ? (const char*)wo - 1536 * (DM * 2) : (const char*)w2) + (size_t)r0 * (DM * 2) + (xcd >> 2) * 1024;
                touch<-AT_BASE>(src, ((lane >> 3) & 1) * (DM * 2) + (lane & 7) * 128, sdst);
            }
            if (s == 7 && wid >= 4) {
                if (!__all(seen == tag)) flag_wait(hflag, tag);
                G2_OFFS(wid - 4, lane);
                g128_issue<false>(lds0, g2, 0, wid - 4, vA, vB); g128_issue<false>(lds0, g2, 1, wid - 4, vA, vB);
            }
            SCHED;
            AT_STEP(SA, SB, kc + 6 * 2048, vc + 4 * 2048 + 1024, vc + 5 * 2048); __builtin_amdgcn_s_setprio(0); AT_STEP(SB, SA, kc + 7 * 2048, vc + 5 * 2048 + 1024, vc + 6 * 2048);
            H_ST(1);
            AT_STEP(SA, SB, kn, vc + 6 * 2048 + 1024, vc + 7 * 2048);            AT_STEP(SB, SA, kn + 2048, vc + 7 * 2048 + 1024, vn);
            const int tmp = cur; cur = nxt; nxt = fre; fre = tmp;
        }
        O = MFMA32(v1, BC8(p1), O); L = MFMA16(sel, BC8(p1), L);
        float lrow; { const float a = __shfl(L[0], lane & 15), b = __shfl(L[1], lane & 15); lrow = (lane & 16) ? b : a; }
        float oabs = 0.f;
#pragma unroll
        for (int i = 0; i < 16; ++i) oabs += fabsf(O[i]);
        if (__any(!(lrow < 1.0e30f) || !(oabs < 3.0e38f))) {
            __builtin_amdgcn_fence(__ATOMIC_ACQUIRE, "agent");
            float m = -INFINITY, l = 0.f;
#pragma unroll
            for (int i = 0; i < 16; ++i) { O[i] = 0.f; negm[i] = 0.f; }
            const u16* Vs = (const u16*)Vg;
            for (int t = 0; t < 64; ++t) { const int key0 = t * 32;
                k0 = *(const bf16x8*)(Kg + key0 * 64 + lane * 16); k1 = *(const bf16x8*)(Kg + key0 * 64 + 1024 + lane * 16);
#pragma unroll
                for (int j = 0; j < 8; ++j) { const int kk = key0 + 8 * (j >> 2) + 4 * hi + (j & 3); v0[j] = (short)Vs[kk * 32 + (lane & 31)]; v1[j] = (short)Vs[(kk + 16) * 32 + (lane & 31)]; }
                f32x16 S = MFMA32(k0, q0, negm); S = MFMA32(k1, q1, S);
                float mt = S[0];
#pragma unroll
                for (int i = 1; i < 16; ++i) mt = fmaxf(mt, S[i]);
                mt = fmaxf(mt, __shfl_xor(mt, 32));
                const float mn = fmaxf(m, mt), al = __builtin_amdgcn_exp2f(m - mn); m = mn;
                float ps = 0.f;
#pragma unroll
                for (int i = 0; i < 16; ++i) { S[i] = rbf(__builtin_amdgcn_exp2f(S[i] - mn)); ps += S[i]; O[i] *= al; }
                l = l * al + ps + __shfl_xor(ps, 32);
#pragma unroll
                for (int i = 0; i < 4; ++i) { p0[i] = cvtpk(S[2 * i], S[2 * i + 1]); p1[i] = cvtpk(S[8 + 2 * i], S[9 + 2 * i]); }
                O = MFMA32(v0, BC8(p0), O); O = MFMA32(v1, BC8(p1), O);
            }
            lrow = l;
        }
        {
            const float inv = 1.0f / lrow;
            unsigned pk[8];
#pragma unroll
            for (int i = 0; i < 8; ++i) pk[i] = cvtpk(O[2 * i] * inv, O[2 * i + 1] * inv);
            const unsigned dst = WS_AH + ((qb * 16 + wid * 2 + ((lane >> 4) & 1)) * KB_AH + head) * 1024 + (lane & 15) * 16 + hi * 256;
#pragma unroll
            for (int c = 0; c < 2; ++c) {
                auto r0 = __builtin_amdgcn_permlane32_swap(pk[4 * c + 0], pk[4 * c + 2], false, false);
                auto r1 = __builtin_amdgcn_permlane32_swap(pk[4 * c + 1], pk[4 * c + 3], false, false);
                ST16(((u32x4){r0[0], r1[0], r0[1], r1[1]}), dst + c * 512);
            }
        }
        if (wid >= 4) { WAIT_V(2); }
        WAIT_L(0); BAR;
#undef AT_ISSUE
#undef AT_STEP
#undef HM
#undef H_ST
#undef EX
#undef BC8
    }

    {
        const int tid = opaque_tid(), lane = tid & 63, tile = rt2 * 8 + ct2;
        GAS u64* myflag = FL(F_G2 + tile * 2 + kh); const GAS u64* pflag = FL(F_G2 + tile * 2 + (kh ^ 1));
        if (wid >= 4) {
            const int lw = wid - 4;
            G2_OFFS(lw, lane);
            const unsigned vH = ((lw >> 1) * 4 + 2 * kh + (lw & 1)) * KB_AH * 1024 + lane * 16;
            g128_issue<false>(lds0, g2, 2, lw, vA, vB); g128_issue<false>(lds0, g2, 3, lw, vA, vB);
            WAIT_V(16);
            for (int t = 0; t < 28; ++t) {
                BAR;
                if (t == 0 && wid == 4 && lane == 0) flag_st(FL(F_AT + (idx & 7) * 32 + xcd * 4 + (idx >> 3)), tag);
                if (t == 12 && kh == 0) flag_wait(FL(F_AT + (rt2 >> 1) * 32 + (lane & 31)), tag);
                g128_issue<false>(lds0, g2, t + 4, lw, vA, vB);
                WAIT_V(16);
            }
            BAR; g128_issue<true>(lds0, g2, 32, lw, vH, vB); WAIT_V(14);
            for (int t = 29; t < 35; ++t) {
                BAR;
                if (t == 34 && wid == 4 && lane == 0) flag_st(myflag, tag);
                g128_issue<true>(lds0, g2, t + 4, lw, vH, vB); WAIT_V(12);
            }
            BAR; g128_issue<true>(lds0, g2, 39, lw, vH, vB);
            const u64 seen = flag_ld(pflag);
            WAIT_V(13);
            BAR; WAIT_V(7);
            BAR; WAIT_V(0);
            const char* psrc = ws + WS_PART + tile * 32768 + (kh ^ 1) * 16384 + lw * 4096;
            const bool early = __all(seen == tag);
            if (early) { glds_pair_sc1<131072>(psrc, lane * 16, lds0 + lw * 4096); glds_pair_sc1<131072 + 2048>(psrc + 2048, lane * 16, lds0 + lw * 4096); }
            BAR;
            if (!early) { flag_wait(pflag, tag); glds_pair_sc1<131072>(psrc, lane * 16, lds0 + lw * 4096); glds_pair_sc1<131072 + 2048>(psrc + 2048, lane * 16, lds0 + lw * 4096); }
            WAIT_V(0); BAR;
            if (bid == 0 && wid == 4) {
#pragma unroll
                for (int i = 0; i < 4; ++i) flag_wait(FL(F_LN + i * 64 + lane), tag);
                if (lane == 0) flag_st(flags0, tag);
            }
        } else {
            const int wm = wid >> 1, wn = wid & 1, g = lane >> 4, q = (lane & 15) >> 2, p = lane & 3, swz = (2 * g + (q >> 1)) & 3;
            const int aoff = wm * 8192 + lane * 16;
            const int boff0 = 16384 + (4 * g + q) * 128 + (((2 * wn) ^ swz) * 32) + p * 8, boff1 = boff0 ^ 32;
            f32x4 acc[4][4]; bf16x8 AX[4], AY[4];
            s16x4 XL[4], XH[4], YL[4], YH[4];
            int pa[2] = {aoff, aoff + 65536}, pb0[2] = {boff0, boff0 + 65536}, pb1[2] = {boff1, boff1 + 65536};
            PIN(pa[1]); PIN(pb0[1]); PIN(pb1[1]);
#define G_BP(n, sl, kk) (lds + (((n) & 1) ? pb1[(sl) >> 1] : pb0[(sl) >> 1]) + ((sl) & 1) * 32768 + ((n) >> 1) * 8192 + (kk) * 4096)
#define G_BL(L_, n, sl, kk) do { L_[n] = trd(G_BP(n, sl, kk)); SCHED; } while (0)
#define G_BH(H_, n, sl, kk) do { H_[n] = trd(G_BP(n, sl, kk) + 2048); SCHED; } while (0)
#define G_A(A_, m, sl, kk) do { A_[m] = *(const LAS bf16x8*)(lds + pa[(sl) >> 1] + ((sl) & 1) * 32768 + ((m) * 2 + (kk)) * 1024); SCHED; } while (0)
#define G_M(A_, L_, H_, m, n) do { MFMA16_ACC(acc[m][n], cat8(L_[n], H_[n]), A_[m]); SCHED; } while (0)
#define G_REST(AC, CL, CH, AN, NL, NH, sl, kk) do { \
        G_BL(NL, 0, sl, kk); G_M(AC, CL, CH, 0, 2); G_BH(NH, 0, sl, kk); G_M(AC, CL, CH, 0, 3); G_A(AN, 0, sl, kk); G_M(AC, CL, CH, 1, 0); G_BL(NL, 1, sl, kk); G_M(AC, CL, CH, 1, 1); \
        G_BH(NH, 1, sl, kk); G_M(AC, CL, CH, 1, 2); G_BL(NL, 2, sl, kk); G_M(AC, CL, CH, 1, 3); G_BH(NH, 2, sl, kk); G_M(AC, CL, CH, 2, 0); G_BL(NL, 3, sl, kk); G_M(AC, CL, CH, 2, 1); \
        G_BH(NH, 3, sl, kk); G_M(AC, CL, CH, 2, 2); G_A(AN, 1, sl, kk); G_M(AC, CL, CH, 2, 3); G_A(AN, 2, sl, kk); G_M(AC, CL, CH, 3, 0); G_A(AN, 3, sl, kk); G_M(AC, CL, CH, 3, 1); \
        G_M(AC, CL, CH, 3, 2); G_M(AC, CL, CH, 3, 3); } while (0)
#define G_TILE(sl) do { G_M(AX, XL, XH, 0, 0); G_M(AX, XL, XH, 0, 1); G_REST(AX, XL, XH, AY, YL, YH, sl, 1); \
        G_M(AY, YL, YH, 0, 0); G_M(AY, YL, YH, 0, 1); \
        asm volatile("s_waitcnt vmcnt(0) lgkmcnt(0)" ::: "memory"); BAR; SCHED;        \
        G_REST(AY, YL, YH, AX, XL, XH, ((sl) + 1) & 3, 0); } while (0)
#pragma unroll
            for (int m = 0; m < 4; ++m)
#pragma unroll
                for (int n = 0; n < 4; ++n) { acc[m][n] = (f32x4){0.f, 0.f, 0.f, 0.f}; PIN(acc[m][n]); }
            SCHED;
#pragma unroll
            for (int i = 0; i < 4; ++i) { G_BL(XL, i, 0, 0); G_BH(XH, i, 0, 0); G_A(AX, i, 0, 0); }
            for (int t = 0; t < 32; t += 4) { G_TILE(0); G_TILE(1); G_TILE(2); G_TILE(3); }
            asm volatile("s_nop 7"); SCHED;
            const int fr = lane & 15, fq = lane >> 4;
            const size_t rowo = (size_t)(rt2 * 128 + wm * 64 + kh * 32 + fr) * DM + ct2 * 128 + wn * 32 + (fq & 1) * 16 + (fq >> 1) * 8;
            u32x4 xv[2][2];
#pragma unroll
            for (int m = 0; m < 2; ++m)
#pragma unroll
                for (int s = 0; s < 2; ++s) ST16(pack8(acc[2 + m][2 * s], acc[2 + m][2 * s + 1]), WS_PART + tile * 32768 + kh * 16384 + (m * 2 + s) * 4096 + tid * 16);
#pragma unroll
            for (int m = 0; m < 2; ++m)
#pragma unroll
                for (int s = 0; s < 2; ++s) xv[m][s] = *(const u32x4*)(x + rowo + (size_t)m * 16 * DM + s * 64);
            SCHED;
#define H_REST(AC, CL, CH, AN, NL, NH, sl, kk) do { \
        G_BL(NL, 0, sl, kk); G_BH(NH, 0, sl, kk); G_M(AC, CL, CH, 0, 2); G_A(AN, 0, sl, kk); G_BL(NL, 1, sl, kk); G_M(AC, CL, CH, 0, 3); G_BH(NH, 1, sl, kk); G_BL(NL, 2, sl, kk); G_M(AC, CL, CH, 1, 0); \
        G_BH(NH, 2, sl, kk); G_BL(NL, 3, sl, kk); G_M(AC, CL, CH, 1, 1); G_BH(NH, 3, sl, kk); G_M(AC, CL, CH, 1, 2); G_A(AN, 1, sl, kk); G_M(AC, CL, CH, 1, 3); } while (0)
#define H_TILE(sl, SYNC) do { G_M(AX, XL, XH, 0, 0); G_M(AX, XL, XH, 0, 1); H_REST(AX, XL, XH, AY, YL, YH, sl, 1); \
        G_M(AY, YL, YH, 0, 0); G_M(AY, YL, YH, 0, 1); SYNC; SCHED; H_REST(AY, YL, YH, AX, XL, XH, ((sl) + 1) & 3, 0); } while (0)
#define H_BAR do { WAIT_L(0); BAR; } while (0)
            H_TILE(0, H_BAR); H_TILE(1, H_BAR); H_TILE(2, WAIT_V(0); H_BAR); H_TILE(3, H_BAR);
            H_TILE(0, H_BAR); H_TILE(1, H_BAR); H_TILE(2, H_BAR); H_TILE(3, );
#undef G_BP
#undef G_BL
#undef G_BH
#undef G_A
#undef G_M
#undef G_REST
#undef G_TILE
#undef H_REST
#undef H_TILE
#undef H_BAR
            asm volatile("s_nop 7"); SCHED;
            BAR; CBAR;
            u32x4 pv[2][2];
#pragma unroll
            for (int m = 0; m < 2; ++m)
#pragma unroll
                for (int s = 0; s < 2; ++s) pv[m][s] = *(const LAS u32x4*)(lds + 131072 + (m * 2 + s) * 4096 + tid * 16);
#pragma unroll
            for (int m = 0; m < 2; ++m)
#pragma unroll
                for (int s = 0; s < 2; ++s) { float lo[4], hi4[4]; const u32x4 p_ = pv[m][s], x_ = xv[m][s];
                    const f32x4 a0 = acc[m][2 * s] + (f32x4){bf_lo(p_[0]), bf_hi(p_[0]), bf_lo(p_[1]), bf_hi(p_[1])}, a1 = acc[m][2 * s + 1] + (f32x4){bf_lo(p_[2]), bf_hi(p_[2]), bf_lo(p_[3]), bf_hi(p_[3])};
#pragma unroll
                    for (int i = 0; i < 4; ++i) { auto r = __builtin_amdgcn_permlane16_swap(__float_as_uint(a0[i]), __float_as_uint(a1[i]), false, false);
                        lo[i] = __uint_as_float(r[0]); hi4[i] = __uint_as_float(r[1]); }
                    u32x4 o; o[0] = cvtpk(lo[0] + bf_lo(x_[0]), lo[1] + bf_hi(x_[0])); o[1] = cvtpk(lo[2] + bf_lo(x_[1]), lo[3] + bf_hi(x_[1]));
                    o[2] = cvtpk(hi4[0] + bf_lo(x_[2]), hi4[1] + bf_hi(x_[2])); o[3] = cvtpk(hi4[2] + bf_lo(x_[3]), hi4[3] + bf_hi(x_[3]));
                    asm volatile("global_store_dwordx4 %0, %1, off sc0 sc1\n\ts_nop 1" :: "v"(out + rowo + (size_t)m * 16 * DM + s * 64), "v"(o) : "memory");
                }
        }
    }
}

extern "C" void kernel_launch(void* const* d_in, const int* in_sizes, int n_in, void* d_out, int out_size, void* d_ws, size_t ws_size, hipStream_t stream) {
    (void)hipFuncSetAttribute((const void*)k_block, hipFuncAttributeMaxDynamicSharedMemorySize, LDS_BYTES);
    k_block<<<256, 512, LDS_BYTES, stream>>>((const u16*)d_in[0], (const u16*)d_in[1], (const u16*)d_in[2], (const u16*)d_in[4], (const u16*)d_in[5], (const u16*)d_in[6], (char*)d_ws, (u16*)d_out);
}
```

```cpp
#include <hip/hip_runtime.h>
#include <hip/hip_bf16.h>
#include <stdint.h>

#define LAS __attribute__((address_space(3)))
#define GAS __attribute__((address_space(1)))
#define DI __device__ __forceinline__
typedef unsigned short u16;
typedef unsigned long long u64;
typedef short bf16x8 __attribute__((ext_vector_type(8)));
typedef short s16x4 __attribute__((ext_vector_type(4)));
typedef float f32x2 __attribute__((ext_vector_type(2)));
typedef float f32x4 __attribute__((ext_vector_type(4)));
typedef float f32x16 __attribute__((ext_vector_type(16)));
typedef unsigned u32x2 __attribute__((ext_vector_type(2)));
typedef unsigned u32x4 __attribute__((ext_vector_type(4)));
typedef __bf16 bf16x2_t __attribute__((ext_vector_type(2)));

constexpr int SEQ = 2048, DM = 1024, FF = 3584;
constexpr int KB_X = DM / 32;
constexpr int KB_AH = (DM + FF) / 32;
constexpr unsigned WS_XN = 0;
constexpr unsigned WS_Q = WS_XN + SEQ * DM * 2;
constexpr unsigned WS_K = WS_Q + SEQ * DM * 2;
constexpr unsigned WS_V = WS_K + SEQ * DM * 2;
constexpr unsigned WS_AH = WS_V + SEQ * DM * 2;
constexpr unsigned WS_PART = WS_AH + SEQ * (DM + FF) * 2;
constexpr unsigned WS_FLAG = WS_PART + 128 * 32768;
constexpr unsigned WS_END = WS_FLAG + 2048 * 128;
constexpr int F_LN = 32, F_QKV = F_LN + 256, F_H = F_QKV + 256, F_AT = F_H + 256, F_G2 = F_AT + 256;
constexpr int LDS_BYTES = 163840;

DI unsigned cvtpk(float lo, float hi) { f32x2 v = {lo, hi}; bf16x2_t b = __builtin_convertvector(v, bf16x2_t); return __builtin_bit_cast(unsigned, b); }
DI float bf_lo(unsigned u) { return __uint_as_float(u << 16); }
DI float bf_hi(unsigned u) { return __uint_as_float(u & 0xffff0000u); }
DI float rbf(float f) { return __uint_as_float(cvtpk(f, f) << 16); }
DI s16x4 trd(const LAS char* p) { return __builtin_amdgcn_ds_read_tr16_b64_v4i16((LAS s16x4*)p); }
DI bf16x8 cat8(s16x4 a, s16x4 b) { return __builtin_shufflevector(a, b, 0, 1, 2, 3, 4, 5, 6, 7); }
template <int OFF, int D> DI void glds(const char* base, unsigned voff, unsigned dst) {
    asm volatile("s_add_u32 m0, %2, %4\n\ts_nop 0\n\tglobal_load_lds_dwordx4 %0, %1 offset:%3" :: "v"(voff), "s"(base), "s"(dst), "i"(OFF), "i"(D - OFF) : "memory", "scc"); }
template <int D> DI void glds_pair_sc1(const char* base, unsigned voff, unsigned dst) {
    asm volatile("s_add_u32 m0, %2, %3\n\ts_nop 0\n\tglobal_load_lds_dwordx4 %0, %1 sc1\n\tglobal_load_lds_dwordx4 %0, %1 offset:1024 sc1" :: "v"(voff), "s"(base), "s"(dst), "i"(D) : "memory", "scc"); }
template <int D> DI void touch(const char* base, unsigned voff, unsigned dst) {
    asm volatile("s_add_u32 m0, %2, %3\n\ts_nop 0\n\tglobal_load_lds_dword %0, %1" :: "v"(voff), "s"(base), "s"(dst), "i"(D) : "memory", "scc"); }
#define WAIT_V(n) asm volatile("s_waitcnt vmcnt(" #n ")" ::: "memory")
#define WAIT_L(n) asm volatile("s_waitcnt lgkmcnt(" #n ")" ::: "memory")
#define BAR __builtin_amdgcn_s_barrier()
#define CBAR asm volatile("" ::: "memory")
#define SCHED __builtin_amdgcn_sched_barrier(0)
#define PIN(x) asm volatile("" : "+v"(x))
#define MFMA16(a, b, c) __builtin_amdgcn_mfma_f32_16x16x32_bf16(a, b, c, 0, 0, 0)
#define MFMA16_ACC(c, a, b) asm volatile("v_mfma_f32_16x16x32_bf16 %0, %1, %2, %0" : "+v"(c) : "v"(a), "v"(b))
#define MFMA32(a, b, c) __builtin_amdgcn_mfma_f32_32x32x16_bf16(a, b, c, 0, 0, 0)
#define ST16(v, off) __builtin_amdgcn_raw_buffer_store_b128(v, rs, (int)(off), 0, 16)
#define LD16(off) __builtin_amdgcn_raw_buffer_load_b128(rs, (int)(off), 0, 16)
DI u64 flag_ld(const GAS u64* p) { return __hip_atomic_load(p, __ATOMIC_RELAXED, __HIP_MEMORY_SCOPE_AGENT); }
DI void flag_st(GAS u64* p, u64 v) { __hip_atomic_store(p, v, __ATOMIC_RELAXED, __HIP_MEMORY_SCOPE_AGENT); }
DI void flag_wait(const GAS u64* p, u64 tag) {
    for (unsigned spins = 0; spins < (1u << 18); ++spins) { if (__all(flag_ld(p) == tag)) break; __builtin_amdgcn_s_sleep(1); }
    CBAR;
}
template <int N> DI float ror_add(float v) { return v + __builtin_bit_cast(float, __builtin_amdgcn_update_dpp(0, __builtin_bit_cast(int, v), 0x120 + N  , 0xf, 0xf, false)); }
DI float sum64(float v) {
    v = ror_add<1>(ror_add<2>(ror_add<4>(ror_add<8>(v))));
    { auto r = __builtin_amdgcn_permlane16_swap(__float_as_uint(v), __float_as_uint(v), false, false); v = __uint_as_float(r[0]) + __uint_as_float(r[1]); }
    { auto r = __builtin_amdgcn_permlane32_swap(__float_as_uint(v), __float_as_uint(v), false, false); v = __uint_as_float(r[0]) + __uint_as_float(r[1]); }
    return v;
}
DI u32x4 widen(u32x2 a, u32x2 b) { auto r0 = __builtin_amdgcn_permlane16_swap(a[0], b[0], false, false); auto r1 = __builtin_amdgcn_permlane16_swap(a[1], b[1], false, false);
    return (u32x4){r0[0], r1[0], r0[1], r1[1]}; }
DI int opaque_tid() { int t = threadIdx.x; asm volatile("" : "+v"(t)); return t; }

DI float silu_mul(float g, float u) { return g * u * __builtin_amdgcn_rcpf(1.0f + __builtin_amdgcn_exp2f(g * -1.4426950408889634f)); }
DI u32x4 pack8(const f32x4& a, const f32x4& b) { u32x4 o; o[0] = cvtpk(a[0], a[1]); o[1] = cvtpk(a[2], a[3]); o[2] = cvtpk(b[0], b[1]); o[3] = cvtpk(b[2], b[3]); return o; }
constexpr float QSCALE = 0.17677669529663687f * 1.4426950408889634f;


struct G128 { const char* a; const char* w2; const char* wo; int kh; };
template <bool HALF> DI void g128_issue(unsigned lds0, const G128& g, int t, int lw, unsigned voffA, unsigned voffB) {
    const unsigned so = lds0 + (t & 3) * 32768, sb = so + lw * 2048;
    const bool att = !HALF && t >= 24;
    const int kt = HALF ? t - 8 : att ? g.kh * 8 + t - 24 : g.kh * 32 + t;
    const char* a = g.a + (size_t)(att ? kt : 16 + kt) * 2048; const char* b = (att ? g.wo : g.w2) + (size_t)kt * (64 * DM * 2);
    if (HALF) glds_pair_sc1<0>(a, voffA, so + (lw >> 1) * 8192 + (lw & 1) * 2048);
    else { glds_pair_sc1<0>(a, voffA, so + lw * 4096); glds_pair_sc1<2048>(a + KB_AH * 1024, voffA, so + lw * 4096); }
    glds<0, 16384>(b, voffB, sb); glds<128, 24576>(b, voffB, sb);
    glds<0, 16384 + 1024>(b + 8 * DM * 2, voffB, sb); glds<128, 24576 + 1024>(b + 8 * DM * 2, voffB, sb);
}

__global__ __launch_bounds__(512, 2) void k_block(const u16* __restrict__ x, const u16* __restrict__ wqkv, const u16* __restrict__ wo, const u16* __restrict__ w1,
                                                  const u16* __restrict__ w3, const u16* __restrict__ w2, char* __restrict__ ws, u16* __restrict__ out) {
    extern __shared__ __attribute__((aligned(16))) char smem[];
    LAS char* lds = (LAS char*)smem;
    const unsigned lds0 = (unsigned)(size_t)lds;
    const int wid = __builtin_amdgcn_readfirstlane(threadIdx.x >> 6);
    const int bid = blockIdx.x, xcd = bid & 7, idx = bid >> 3;
    GAS u64* flags0 = (GAS u64*)(ws + WS_FLAG);
#define FL(i) (flags0 + (i) * 16)
    const __amdgpu_buffer_rsrc_t rs = __builtin_amdgcn_make_buffer_rsrc((void*)ws, 0, WS_END, 0x00020000);
    {
        const char* pc; asm volatile("s_getpc_b64 %0" : "=s"(pc));
        if (wid == 7) touch<148480>(pc + (idx & 7) * 4096, (threadIdx.x & 63) * 64, lds0 + 7 * 1024);
    }
    u64 tag;
    u32x4 xa, xb; u32x2 ep;
    { const char* xp = (const char*)x + (size_t)(bid * 8 + wid) * (DM * 2);
      asm volatile("global_load_dwordx4 %0, %3, %4\n\tglobal_load_dwordx4 %1, %3, %4 offset:1024\n\tglobal_load_dwordx2 %2, %5, %6 sc1"
                   : "=&v"(xa), "=&v"(xb), "=&v"(ep) : "v"((threadIdx.x & 63) * 16), "s"(xp), "v"(0), "s"(ws + WS_FLAG), "s"(wqkv), "s"(w1), "s"(w3) : "memory"); }
    const int cj = idx >> 2, ct = cj < 3 ? (xcd >> 1) * 3 + cj : 12 + (xcd >> 1) * 5 + (cj - 3), rt = (xcd & 1) * 4 + (idx & 3);
    const bool qkv = ct < 12;
    u32x4 hmain[8], hext[2];

    {
        const int tid = opaque_tid(), lane = tid & 63, wr = wid >> 2, wc = wid & 3;
        const unsigned ldb2 = qkv ? 3 * DM * 2 : FF * 2;
        const char* cB0 = qkv ? (const char*)wqkv + ct * 512 : (const char*)w1 + (ct - 12) * 256;
        const char* cB1 = qkv ? cB0 + 256 : (const char*)w3 + (ct - 12) * 256;
        const char* cA = ws + WS_XN + rt * 16 * KB_X * 1024;
        const unsigned voffA = wid * (KB_X * 1024) + lane * 16;
        const unsigned voffB = (unsigned)(wid * 8 + (lane >> 3)) * ldb2 + (((lane & 7) ^ (2 * ((lane >> 4) & 3))) * 16);
        const char* pE = ((lane & 1) ? (const char*)w3 : (const char*)w1) + (size_t)(wid * 8 + (lane >> 3)) * (FF * 2) + (2560 + ct * 32) * 2 + ((((lane >> 1) & 3) ^ ((lane >> 4) & 3)) * 16);
        const size_t kstepB = (size_t)64 * ldb2;
        const unsigned ldsA = lds0 + wid * 2048, ldsB = lds0 + wid * 1024;
        const char* cT = ((idx & 2) ? cB1 : cB0) + (idx & 1) * 128;
#define SA(b, h) (((b) * 2 + (h)) * 16384)
#define SB(b, h) (((b) * 2 + (h)) * 16384)
#define STA(b, h, kt) glds_pair_sc1<SA(b, h)>(cA + (h) * 8 * KB_X * 1024 + (kt) * 2048, voffA, ldsA)
#define STB(b, h, kt) do { const char* s_ = ((h) ? cB1 : cB0) + (size_t)(kt) * kstepB; \
        glds<0, 65536 + SB(b, h)>(s_, voffB, ldsB); glds<128, 65536 + SB(b, h) + 8192>(s_, voffB, ldsB); } while (0)
#define TCH(kt) touch<148480>(cT + (size_t)(kt) * kstepB, voffB, ldsB)
#define STE(b, kt) do { const char* s_ = pE + (size_t)(kt) * (64 * FF * 2); \
        asm volatile("s_add_u32 m0, %1, %2\n\ts_nop 0\n\tglobal_load_lds_dwordx4 %0, off" :: "v"(s_), "s"(ldsB), "i"(131072 + (b) * 8192) : "memory", "scc"); } while (0)
        {
            TCH(0); TCH(1);
            { const char* s_ = pE + (size_t)(idx & 1) * (64 * FF * 2) + (ptrdiff_t)(((idx >> 1) & 1) - (wid >> 2)) * (32 * FF * 2);
              asm volatile("s_add_u32 m0, %1, 148480\n\ts_nop 0\n\tglobal_load_lds_dword %0, off" :: "v"(s_), "s"(ldsB) : "memory", "scc"); }
            asm volatile("s_waitcnt vmcnt(3)" : "+v"(xa), "+v"(xb), "+v"(ep) :: "memory");
            tag = (((u64)(unsigned)__builtin_amdgcn_readfirstlane(ep[1]) << 32) | (unsigned)__builtin_amdgcn_readfirstlane(ep[0])) + 1;
            float f[16];
#pragma unroll
            for (int i = 0; i < 4; ++i) { f[2 * i] = bf_lo(xa[i]); f[2 * i + 1] = bf_hi(xa[i]); f[8 + 2 * i] = bf_lo(xb[i]); f[9 + 2 * i] = bf_hi(xb[i]); }
            float s = 0.f;
#pragma unroll
            for (int i = 0; i < 16; ++i) s += f[i];
            const float mean = sum64(s) * (1.0f / DM);
            float q = 0.f;
#pragma unroll
            for (int i = 0; i < 16; ++i) { const float d = f[i] - mean; q += d * d; }
            const float var = sum64(q) * (1.0f / DM);
            const float mb = rbf(mean), sd = rbf(__builtin_amdgcn_sqrtf(rbf(rbf(var) + 1e-5f))), inv = __builtin_amdgcn_rcpf(sd);
#pragma unroll
            for (int c = 0; c < 2; ++c) { u32x4 o;
#pragma unroll
                for (int i = 0; i < 4; ++i) o[i] = cvtpk(rbf(f[c * 8 + 2 * i] - mb) * inv, rbf(f[c * 8 + 2 * i + 1] - mb) * inv);
                *(LAS u32x4*)(lds + wid * 2080 + c * 1024 + lane * 16) = o; }
            WAIT_L(0); BAR; CBAR;
            const int r = lane & 7, g = (lane >> 3) & 3, row = bid * 8 + r;
#pragma unroll
            for (int c = 0; c < 2; ++c) { const int kb = 4 * wid + 2 * c + (lane >> 5); const LAS char* p = lds + r * 2080 + kb * 64 + g * 8;
                const u32x2 lo = *(const LAS u32x2*)p, hi = *(const LAS u32x2*)(p + 32);
                ST16(((u32x4){lo[0], lo[1], hi[0], hi[1]}), WS_XN + ((row >> 4) * KB_X + kb) * 1024 + (g * 16 + (row & 15)) * 16); }
            CBAR;
            STB(0, 0, 0); STB(0, 1, 0); STE(0, 0);
            WAIT_V(5); BAR;
            if (wid == 0) { if (lane == 0) flag_st(FL(F_LN + bid), tag); flag_wait(FL(F_LN + rt * 32 + (lane & 31)), tag); }
            STB(1, 0, 1); STB(1, 1, 1); STE(1, 1);
            if (wid != 0) { TCH(2); TCH(3); }
            BAR; CBAR;
        }
        f32x4 acc[2][2][4][2], ace[2][4];
        {
            const int g = lane >> 4, q = (lane & 15) >> 2, p = lane & 3, swz = (2 * g + (q >> 1)) & 3;
            const int aoff = wr * 8192 + lane * 16;
            const int boff0 = 65536 + (wc >> 1) * 8192 + (4 * g + q) * 128 + (((2 * (wc & 1)) ^ swz) * 32) + p * 8, boff1 = boff0 ^ 32;
            int eoff = 131072 + (4 * g + q) * 128 + ((((wc >> 1) + 2 * (p & 1)) ^ swz) * 32) + (p >> 1) * 16 + (wc & 1) * 8; PIN(eoff);
#define LDA(dst, b, h) do { _Pragma("unroll") for (int m = 0; m < 4; ++m) _Pragma("unroll") for (int k = 0; k < 2; ++k) \
        dst[m][k] = *(const LAS bf16x8*)(lds + aoff + SA(b, h) + (m * 2 + k) * 1024); } while (0)
#define LDB(dst, b, h) do { _Pragma("unroll") for (int n = 0; n < 2; ++n) _Pragma("unroll") for (int k = 0; k < 2; ++k) { \
        const LAS char* b_ = lds + (n ? boff1 : boff0) + SB(b, h) + k * 4096; dst[n][k] = cat8(trd(b_), trd(b_ + 2048)); } } while (0)
#define LDE(dst, b) do { _Pragma("unroll") for (int k = 0; k < 2; ++k) { const LAS char* b_ = lds + eoff + (b) * 8192 + k * 4096; dst[k] = cat8(trd(b_), trd(b_ + 2048)); } } while (0)
#define MMA(ai) do { __builtin_amdgcn_s_setprio(1); \
        _Pragma("unroll") for (int k = 0; k < 2; ++k) _Pragma("unroll") for (int m = 0; m < 4; ++m) { \
            MFMA16_ACC(acc[ai][0][m][0], B0[0][k], At[m][k]); MFMA16_ACC(acc[ai][0][m][1], B0[1][k], At[m][k]); \
            MFMA16_ACC(acc[ai][1][m][0], B1[0][k], At[m][k]); MFMA16_ACC(acc[ai][1][m][1], B1[1][k], At[m][k]); \
            MFMA16_ACC(ace[ai][m], BE[k], At[m][k]); } \
        __builtin_amdgcn_s_setprio(0); } while (0)
            STA(0, 0, 0); STA(0, 1, 0); STA(1, 0, 1);
#pragma unroll
            for (int a = 0; a < 2; ++a)
#pragma unroll
                for (int m = 0; m < 4; ++m) { ace[a][m] = (f32x4){0.f, 0.f, 0.f, 0.f}; PIN(ace[a][m]);
#pragma unroll
                    for (int b = 0; b < 2; ++b)
#pragma unroll
                        for (int n = 0; n < 2; ++n) { acc[a][b][m][n] = (f32x4){0.f, 0.f, 0.f, 0.f}; PIN(acc[a][b][m][n]); } }
            bf16x8 At[4][2], B0[2][2], B1[2][2], BE[2];
            constexpr int nt = DM / 64;
            if (wr == 1) BAR;
            WAIT_V(4); BAR;
            WAIT_V(2); BAR;
            for (int t = 0; t < nt; t += 2) {
                const bool more = t + 2 < nt, far = t + 4 < nt;
                LDB(B0, 0, 0); LDB(B1, 0, 1); LDE(BE, 0); SCHED; LDA(At, 0, 0); STA(1, 1, t + 1);
                if (far) { TCH(t + 4); WAIT_V(11); } else { WAIT_V(9); }
                WAIT_L(0); BAR; MMA(0); BAR; SCHED;
                LDA(At, 0, 1);
                if (more) { STB(0, 0, t + 2); STB(0, 1, t + 2); STE(0, t + 2); STA(0, 0, t + 2); if (far) { WAIT_V(10); } else { WAIT_V(9); } } else { WAIT_V(2); }
                WAIT_L(0); BAR; MMA(1); BAR; SCHED;
                LDB(B0, 1, 0); LDB(B1, 1, 1); LDE(BE, 1); SCHED; LDA(At, 1, 0);
                if (more) { STA(0, 1, t + 2); if (far) { TCH(t + 5); WAIT_V(11); } else { WAIT_V(9); } } else { WAIT_V(0); }
                WAIT_L(0); BAR; MMA(0); BAR; SCHED;
                LDA(At, 1, 1);
                if (more) { STB(1, 0, t + 3); STB(1, 1, t + 3); STE(1, t + 3); STA(1, 0, t + 3); if (far) { WAIT_V(10); } else { WAIT_V(9); } }
                WAIT_L(0); BAR; MMA(1); if (more || wr == 0) BAR; SCHED;
            }
            asm volatile("s_nop 7"); SCHED;
#undef SA
#undef SB
#undef STA
#undef STB
#undef STE
#undef TCH
#undef LDA
#undef LDB
#undef LDE
#undef MMA
        }
        const int ln = opaque_tid() & 63, fr = ln & 15, fq = ln >> 4;
        if (qkv) {
            const int kind = ct >> 2;
            const float sc = kind == 0 ? QSCALE : 1.0f;
            const unsigned base = WS_Q + kind * (SEQ * DM * 2) + ((ct & 3) * 8 + wc) * 131072 + (rt * 256 + wr * 64) * 64;
            const unsigned lo0 = base + (kind == 2 ? fr * 64 + fq * 16 : fr * 16 + fq * 512), lo1 = lo0 + (kind == 2 ? 1024 : 256);
#pragma unroll
            for (int bj = 0; bj < 2; ++bj)
#pragma unroll
                for (int ai = 0; ai < 2; ++ai)
#pragma unroll
                    for (int m = 0; m < 4; ++m)
                        ST16(pack8(acc[ai][bj][m][0] * sc, acc[ai][bj][m][1] * sc), bj * 4 * 131072 + ai * 8192 + (m >> 1) * 2048 + ((m & 1) ? lo1 : lo0));
            WAIT_V(0); BAR;
            if (wid == 0 && ln == 0) flag_st(FL(F_QKV + rt * 32 + ct), tag);
        } else {
#pragma unroll
            for (int ai = 0; ai < 2; ++ai)
#pragma unroll
                for (int m = 0; m < 4; ++m) { f32x4 h0, h1;
#pragma unroll
                    for (int j = 0; j < 4; ++j) { h0[j] = silu_mul(acc[ai][0][m][0][j], acc[ai][1][m][0][j]); h1[j] = silu_mul(acc[ai][0][m][1][j], acc[ai][1][m][1][j]); }
                    hmain[ai * 4 + m] = pack8(h0, h1);
                }
        }
#pragma unroll
        for (int ai = 0; ai < 2; ++ai) { u32x2 e[2];
#pragma unroll
            for (int mp = 0; mp < 2; ++mp) { float h[4];
#pragma unroll
                for (int j = 0; j < 4; ++j) { auto r = __builtin_amdgcn_permlane32_swap(__float_as_uint(ace[ai][2 * mp][j]), __float_as_uint(ace[ai][2 * mp + 1][j]), false, false);
                    h[j] = silu_mul(__uint_as_float(r[0]), __uint_as_float(r[1])); }
                e[mp] = (u32x2){cvtpk(h[0], h[1]), cvtpk(h[2], h[3])}; }
            hext[ai] = widen(e[0], e[1]); }
    }

    const int kh = xcd & 1, rt2 = ((xcd >> 1) & 1) * 8 + (idx & 7), ct2 = (xcd >> 2) * 4 + (idx >> 3);
    G128 g2; g2.kh = kh; g2.a = ws + WS_AH + rt2 * 8 * KB_AH * 1024; g2.w2 = (const char*)w2 + ct2 * 256; g2.wo = (const char*)wo + ct2 * 256;
#define G2_OFFS(lw_, l_) const unsigned vA = ((lw_) ^ kh) * 2 * KB_AH * 1024 + (l_) * 16, vB = ((lw_) * 16 + ((l_) >> 3)) * (DM * 2) + ((((l_) & 7) ^ (2 * (((l_) >> 4) & 3))) * 16)

    {
        constexpr int AT_BASE = 65536, AT_STAGE = 32768;
        const int lane = opaque_tid() & 63, hi = lane >> 5;
        const int head = xcd * 4 + (idx >> 3), qb = idx & 7;
        if (wid == 0) flag_wait(FL(F_QKV + (lane < 4 ? (lane & 1) * 32 + 4 + (lane >> 1) * 4 : qb * 32) + (head >> 3)), tag);
        BAR; CBAR;
        const char* Kg = ws + WS_K + head * 131072; const char* Vg = ws + WS_V + head * 131072;
        const unsigned soff = wid * 2048 + lane * 16;
        const unsigned sdst = lds0 + AT_BASE + wid * 2048;
#define AT_ISSUE(s, slot) do { glds_pair_sc1<0>(Kg + (s) * 16384, soff, sdst + (slot)); glds_pair_sc1<16384>(Vg + (s) * 16384, soff, sdst + (slot)); } while (0)
        const unsigned qoff = WS_Q + (head * 64 + qb * 8 + wid) * 2048 + lane * 16;
        const bf16x8 q0 = __builtin_bit_cast(bf16x8, LD16(qoff)), q1 = __builtin_bit_cast(bf16x8, LD16(qoff + 1024));
        AT_ISSUE(0, 0); AT_ISSUE(1, AT_STAGE);
        const GAS u64* kvflag = FL(F_QKV + (lane & 7) * 32 + 4 + ((lane >> 3) & 1) * 4 + (head >> 3));
        u64 seen = flag_ld(kvflag);
        bf16x8 sel; { const short one = ((lane & 15) == ((lane >> 4) & 1)) ? (short)0x3f80 : (short)0;
#pragma unroll
            for (int i = 0; i < 8; ++i) sel[i] = one; }
        const LAS char* kb0 = lds + AT_BASE + lane * 16;
        const LAS char* vb0 = lds + AT_BASE + 16384 + (4 * hi + ((lane & 15) >> 2)) * 64 + ((lane >> 4) & 1) * 32 + (lane & 3) * 8;
        f32x16 O, negm, SA, SB; f32x4 L = {0.f, 0.f, 0.f, 0.f};
        bf16x8 k0, k1, v0, v1;
#pragma unroll
        for (int i = 0; i < 16; ++i) { O[i] = 0.f; negm[i] = 0.f; }
        WAIT_V(7); BAR;
        {
            k0 = *(const LAS bf16x8*)(kb0); k1 = *(const LAS bf16x8*)(kb0 + 1024);
            SA = MFMA32(k0, q0, negm); SA = MFMA32(k1, q1, SA);
            k0 = *(const LAS bf16x8*)(kb0 + 2048); k1 = *(const LAS bf16x8*)(kb0 + 3072);
            v0 = k0; v1 = k1;
            float m = SA[0];
#pragma unroll
            for (int i = 1; i < 16; ++i) m = fmaxf(m, SA[i]);
            m = fmaxf(m, __shfl_xor(m, 32));
#pragma unroll
            for (int i = 0; i < 16; ++i) { negm[i] = -m; SA[i] -= m; }
            PIN(negm);
        }
#define EX(S, i) S[i] = __builtin_amdgcn_exp2f(S[i])
#define BC8(x) __builtin_bit_cast(bf16x8, x)
#define AT_STEP(SC, SN, kp, v1p, v0p) do { \
        O = MFMA32(v1, BC8(p1), O); SCHED; v1 = cat8(trd(v1p), trd((v1p) + 512)); EX(SC, 0); EX(SC, 1); EX(SC, 2); PIN(SC); SCHED; \
        SN = MFMA32(k0, q0, negm); SCHED; k0 = *(const LAS bf16x8*)(kp); EX(SC, 3); EX(SC, 4); EX(SC, 5); PIN(SC); SCHED; \
        L = MFMA16(sel, BC8(p1), L); SCHED; EX(SC, 6); EX(SC, 7); \
        p0[0] = cvtpk(SC[0], SC[1]); p0[1] = cvtpk(SC[2], SC[3]); p0[2] = cvtpk(SC[4], SC[5]); p0[3] = cvtpk(SC[6], SC[7]); PIN(p0); SCHED; \
        O = MFMA32(v0, BC8(p0), O); SCHED; v0 = cat8(trd(v0p), trd((v0p) + 512)); EX(SC, 8); EX(SC, 9); EX(SC, 10); PIN(SC); SCHED; \
        SN = MFMA32(k1, q1, SN); SCHED; k1 = *(const LAS bf16x8*)((kp) + 1024); EX(SC, 11); EX(SC, 12); EX(SC, 13); PIN(SC); SCHED; \
        L = MFMA16(sel, BC8(p0), L); SCHED; EX(SC, 14); EX(SC, 15); \
        p1[0] = cvtpk(SC[8], SC[9]); p1[1] = cvtpk(SC[10], SC[11]); p1[2] = cvtpk(SC[12], SC[13]); p1[3] = cvtpk(SC[14], SC[15]); PIN(p1); SCHED; } while (0)
        u32x4 p0, p1 = {0u, 0u, 0u, 0u};
        int cur = 0, nxt = AT_STAGE, fre = 2 * AT_STAGE;
        const GAS u64* hflag = FL(F_H + (rt2 >> 1) * 32 + (lane & 31));
        const unsigned hb = WS_AH + ((rt * 16 + (wid >> 2) * 4) * KB_AH + KB_X + (ct - 12) * 4 + (wid & 3)) * 1024 + lane * 16;
        const unsigned eb = WS_AH + ((rt * 16 + (wid >> 2) * 4 + 2 * ((lane >> 4) & 1) + hi) * KB_AH + KB_X + 80 + ct) * 1024 + ((wid & 3) * 16 + (lane & 15)) * 16;
#define HM(i) ST16(hmain[i], hb + (((i) >> 2) * 8 + ((i) & 3)) * KB_AH * 1024)
#define H_ST(j) do { if (s == 4) ST16(hext[j], eb + (j) * 8 * KB_AH * 1024); else if (!qkv && s < 4) { if (s == 0) HM(j); else if (s == 1) HM(2 + (j)); else if (s == 2) HM(4 + (j)); else HM(6 + (j)); } SCHED; } while (0)
        for (int s = 0; s < 8; ++s) {
            if (s == 1) { if (qkv) { WAIT_V(0); } else { WAIT_V(2); } } else if (s >= 2 && s <= 4) { if (qkv) { WAIT_V(1); } else { WAIT_V(3); } } else if (s == 5) { WAIT_V(3); } else { WAIT_V(0); }
            WAIT_L(0); BAR;
            if (s == 0) { v0 = cat8(trd(vb0), trd(vb0 + 512)); if (!__all(seen == tag)) flag_wait(kvflag, tag); }
            if (s + 2 < 8) glds_pair_sc1<0>(Kg + (s + 2) * 16384, soff, sdst + fre);
            if (s == 6) { if (wid == 0 && lane == 0) flag_st(FL(F_H + rt * 32 + ct), tag); }
            if (s == 7) seen = flag_ld(hflag);
            if (wid >= 4) __builtin_amdgcn_s_setprio(1);
            const LAS char* kc = kb0 + cur; const LAS char* vc = vb0 + cur; const LAS char* kn = kb0 + nxt; const LAS char* vn = vb0 + nxt;
            AT_STEP(SA, SB, kc + 2 * 2048, vc + 1024, vc + 1 * 2048);
            if (s + 2 < 8) glds_pair_sc1<16384>(Vg + (s + 2) * 16384, soff, sdst + fre);
            SCHED;
            AT_STEP(SB, SA, kc + 3 * 2048, vc + 1 * 2048 + 1024, vc + 2 * 2048);
            H_ST(0);
            AT_STEP(SA, SB, kc + 4 * 2048, vc + 2 * 2048 + 1024, vc + 3 * 2048); AT_STEP(SB, SA, kc + 5 * 2048, vc + 3 * 2048 + 1024, vc + 4 * 2048);
            if (s >= 1 && s < 6) {
                const int r0 = ((s - 1) * 256 + idx * 8 + wid) * 2;
                const char* src = (r0 >= 2048 ? (const char*)wo + (kh * 512 - 2048) * (DM * 2) : (const char*)w2 + kh * 1536 * (DM * 2)) + (size_t)r0 * (DM * 2) + (xcd >> 2) * 1024;
                touch<-AT_BASE>(src, ((lane >> 3) & 1) * (DM * 2) + (lane & 7) * 128, sdst);
            }
            if (s == 7 && wid >= 4) {
                if (!__all(seen == tag)) flag_wait(hflag, tag);
                G2_OFFS(wid - 4, lane);
                g128_issue<false>(lds0, g2, 0, wid - 4, vA, vB); g128_issue<false>(lds0, g2, 1, wid - 4, vA, vB);
            }
            SCHED;
            AT_STEP(SA, SB, kc + 6 * 2048, vc + 4 * 2048 + 1024, vc + 5 * 2048); __builtin_amdgcn_s_setprio(0); AT_STEP(SB, SA, kc + 7 * 2048, vc + 5 * 2048 + 1024, vc + 6 * 2048);
            H_ST(1);
            AT_STEP(SA, SB, kn, vc + 6 * 2048 + 1024, vc + 7 * 2048);            AT_STEP(SB, SA, kn + 2048, vc + 7 * 2048 + 1024, vn);
            const int tmp = cur; cur = nxt; nxt = fre; fre = tmp;
        }
        O = MFMA32(v1, BC8(p1), O); L = MFMA16(sel, BC8(p1), L);
        float lrow; { const float a = __shfl(L[0], lane & 15), b = __shfl(L[1], lane & 15); lrow = (lane & 16) ? b : a; }
        float oabs = 0.f;
#pragma unroll
        for (int i = 0; i < 16; ++i) oabs += fabsf(O[i]);
        if (__any(!(lrow < 1.0e30f) || !(oabs < 3.0e38f))) {
            __builtin_amdgcn_fence(__ATOMIC_ACQUIRE, "agent");
            float m = -INFINITY, l = 0.f;
#pragma unroll
            for (int i = 0; i < 16; ++i) { O[i] = 0.f; negm[i] = 0.f; }
            const u16* Vs = (const u16*)Vg;
            for (int t = 0; t < 64; ++t) { const int key0 = t * 32;
                k0 = *(const bf16x8*)(Kg + key0 * 64 + lane * 16); k1 = *(const bf16x8*)(Kg + key0 * 64 + 1024 + lane * 16);
#pragma unroll
                for (int j = 0; j < 8; ++j) { const int kk = key0 + 8 * (j >> 2) + 4 * hi + (j & 3); v0[j] = (short)Vs[kk * 32 + (lane & 31)]; v1[j] = (short)Vs[(kk + 16) * 32 + (lane & 31)]; }
                f32x16 S = MFMA32(k0, q0, negm); S = MFMA32(k1, q1, S);
                float mt = S[0];
#pragma unroll
                for (int i = 1; i < 16; ++i) mt = fmaxf(mt, S[i]);
                mt = fmaxf(mt, __shfl_xor(mt, 32));
                const float mn = fmaxf(m, mt), al = __builtin_amdgcn_exp2f(m - mn); m = mn;
                float ps = 0.f;
#pragma unroll
                for (int i = 0; i < 16; ++i) { S[i] = rbf(__builtin_amdgcn_exp2f(S[i] - mn)); ps += S[i]; O[i] *= al; }
                l = l * al + ps + __shfl_xor(ps, 32);
#pragma unroll
                for (int i = 0; i < 4; ++i) { p0[i] = cvtpk(S[2 * i], S[2 * i + 1]); p1[i] = cvtpk(S[8 + 2 * i], S[9 + 2 * i]); }
                O = MFMA32(v0, BC8(p0), O); O = MFMA32(v1, BC8(p1), O);
            }
            lrow = l;
        }
        {
            const float inv = 1.0f / lrow;
            unsigned pk[8];
#pragma unroll
            for (int i = 0; i < 8; ++i) pk[i] = cvtpk(O[2 * i] * inv, O[2 * i + 1] * inv);
            const unsigned dst = WS_AH + ((qb * 16 + wid * 2 + ((lane >> 4) & 1)) * KB_AH + head) * 1024 + (lane & 15) * 16 + hi * 256;
#pragma unroll
            for (int c = 0; c < 2; ++c) {
                auto r0 = __builtin_amdgcn_permlane32_swap(pk[4 * c + 0], pk[4 * c + 2], false, false);
                auto r1 = __builtin_amdgcn_permlane32_swap(pk[4 * c + 1], pk[4 * c + 3], false, false);
                ST16(((u32x4){r0[0], r1[0], r0[1], r1[1]}), dst + c * 512);
            }
        }
        if (wid >= 4) { WAIT_V(2); }
        WAIT_L(0); BAR;
#undef AT_ISSUE
#undef AT_STEP
#undef HM
#undef H_ST
#undef EX
#undef BC8
    }

    {
        const int tid = opaque_tid(), lane = tid & 63, tile = rt2 * 8 + ct2;
        GAS u64* myflag = FL(F_G2 + tile * 2 + kh); const GAS u64* pflag = FL(F_G2 + tile * 2 + (kh ^ 1));
        if (wid >= 4) {
            const int lw = wid - 4;
            G2_OFFS(lw, lane);
            const unsigned vH = ((lw >> 1) * 4 + 2 * kh + (lw & 1)) * KB_AH * 1024 + lane * 16;
            g128_issue<false>(lds0, g2, 2, lw, vA, vB); g128_issue<false>(lds0, g2, 3, lw, vA, vB);
            WAIT_V(16);
            for (int t = 0; t < 28; ++t) {
                BAR;
                if (t == 0 && wid == 4 && lane == 0) flag_st(FL(F_AT + (idx & 7) * 32 + xcd * 4 + (idx >> 3)), tag);
                if (t == 20) flag_wait(FL(F_AT + (rt2 >> 1) * 32 + kh * 16 + (lane & 15)), tag);
                g128_issue<false>(lds0, g2, t + 4, lw, vA, vB);
                WAIT_V(16);
            }
            BAR; g128_issue<true>(lds0, g2, 32, lw, vH, vB); WAIT_V(14);
            for (int t = 29; t < 35; ++t) {
                BAR;
                if (t == 34 && wid == 4 && lane == 0) flag_st(myflag, tag);
                g128_issue<true>(lds0, g2, t + 4, lw, vH, vB); WAIT_V(12);
            }
            BAR; g128_issue<true>(lds0, g2, 39, lw, vH, vB);
            const u64 seen = flag_ld(pflag);
            WAIT_V(13);
            BAR; WAIT_V(7);
            BAR; WAIT_V(0);
            const char* psrc = ws + WS_PART + tile * 32768 + (kh ^ 1) * 16384 + lw * 4096;
            const bool early = __all(seen == tag);
            if (early) { glds_pair_sc1<131072>(psrc, lane * 16, lds0 + lw * 4096); glds_pair_sc1<131072 + 2048>(psrc + 2048, lane * 16, lds0 + lw * 4096); }
            BAR;
            if (!early) { flag_wait(pflag, tag); glds_pair_sc1<131072>(psrc, lane * 16, lds0 + lw * 4096); glds_pair_sc1<131072 + 2048>(psrc + 2048, lane * 16, lds0 + lw * 4096); }
            WAIT_V(0); BAR;
            if (bid == 0 && wid == 4) {
#pragma unroll
                for (int i = 0; i < 4; ++i) flag_wait(FL(F_LN + i * 64 + lane), tag);
                if (lane == 0) flag_st(flags0, tag);
            }
        } else {
            const int wm = wid >> 1, wn = wid & 1, g = lane >> 4, q = (lane & 15) >> 2, p = lane & 3, swz = (2 * g + (q >> 1)) & 3;
            const int aoff = wm * 8192 + lane * 16;
            const int boff0 = 16384 + (4 * g + q) * 128 + (((2 * wn) ^ swz) * 32) + p * 8, boff1 = boff0 ^ 32;
            f32x4 acc[4][4]; bf16x8 AX[4], AY[4];
            s16x4 XL[4], XH[4], YL[4], YH[4];
            int pa[2] = {aoff, aoff + 65536}, pb0[2] = {boff0, boff0 + 65536}, pb1[2] = {boff1, boff1 + 65536};
            PIN(pa[1]); PIN(pb0[1]); PIN(pb1[1]);
#define G_BP(n, sl, kk) (lds + (((n) & 1) ? pb1[(sl) >> 1] : pb0[(sl) >> 1]) + ((sl) & 1) * 32768 + ((n) >> 1) * 8192 + (kk) * 4096)
#define G_BL(L_, n, sl, kk) do { L_[n] = trd(G_BP(n, sl, kk)); SCHED; } while (0)
#define G_BH(H_, n, sl, kk) do { H_[n] = trd(G_BP(n, sl, kk) + 2048); SCHED; } while (0)
#define G_A(A_, m, sl, kk) do { A_[m] = *(const LAS bf16x8*)(lds + pa[(sl) >> 1] + ((sl) & 1) * 32768 + ((m) * 2 + (kk)) * 1024); SCHED; } while (0)
#define G_M(A_, L_, H_, m, n) do { MFMA16_ACC(acc[m][n], cat8(L_[n], H_[n]), A_[m]); SCHED; } while (0)
#define G_REST(AC, CL, CH, AN, NL, NH, sl, kk) do { \
        G_BL(NL, 0, sl, kk); G_M(AC, CL, CH, 0, 2); G_BH(NH, 0, sl, kk); G_M(AC, CL, CH, 0, 3); G_A(AN, 0, sl, kk); G_M(AC, CL, CH, 1, 0); G_BL(NL, 1, sl, kk); G_M(AC, CL, CH, 1, 1); \
        G_BH(NH, 1, sl, kk); G_M(AC, CL, CH, 1, 2); G_BL(NL, 2, sl, kk); G_M(AC, CL, CH, 1, 3); G_BH(NH, 2, sl, kk); G_M(AC, CL, CH, 2, 0); G_BL(NL, 3, sl, kk); G_M(AC, CL, CH, 2, 1); \
        G_BH(NH, 3, sl, kk); G_M(AC, CL, CH, 2, 2); G_A(AN, 1, sl, kk); G_M(AC, CL, CH, 2, 3); G_A(AN, 2, sl, kk); G_M(AC, CL, CH, 3, 0); G_A(AN, 3, sl, kk); G_M(AC, CL, CH, 3, 1); \
        G_M(AC, CL, CH, 3, 2); G_M(AC, CL, CH, 3, 3); } while (0)
#define G_TILE(sl) do { G_M(AX, XL, XH, 0, 0); G_M(AX, XL, XH, 0, 1); G_REST(AX, XL, XH, AY, YL, YH, sl, 1); \
        G_M(AY, YL, YH, 0, 0); G_M(AY, YL, YH, 0, 1); \
        asm volatile("s_waitcnt vmcnt(0) lgkmcnt(0)" ::: "memory"); BAR; SCHED;        \
        G_REST(AY, YL, YH, AX, XL, XH, ((sl) + 1) & 3, 0); } while (0)
#pragma unroll
            for (int m = 0; m < 4; ++m)
#pragma unroll
                for (int n = 0; n < 4; ++n) { acc[m][n] = (f32x4){0.f, 0.f, 0.f, 0.f}; PIN(acc[m][n]); }
            SCHED;
#pragma unroll
            for (int i = 0; i < 4; ++i) { G_BL(XL, i, 0, 0); G_BH(XH, i, 0, 0); G_A(AX, i, 0, 0); }
            for (int t = 0; t < 32; t += 4) { G_TILE(0); G_TILE(1); G_TILE(2); G_TILE(3); }
            asm volatile("s_nop 7"); SCHED;
            const int fr = lane & 15, fq = lane >> 4;
            const size_t rowo = (size_t)(rt2 * 128 + wm * 64 + kh * 32 + fr) * DM + ct2 * 128 + wn * 32 + (fq & 1) * 16 + (fq >> 1) * 8;
            u32x4 xv[2][2];
#pragma unroll
            for (int m = 0; m < 2; ++m)
#pragma unroll
                for (int s = 0; s < 2; ++s) ST16(pack8(acc[2 + m][2 * s], acc[2 + m][2 * s + 1]), WS_PART + tile * 32768 + kh * 16384 + (m * 2 + s) * 4096 + tid * 16);
#pragma unroll
            for (int m = 0; m < 2; ++m)
#pragma unroll
                for (int s = 0; s < 2; ++s) xv[m][s] = *(const u32x4*)(x + rowo + (size_t)m * 16 * DM + s * 64);
            SCHED;
#define H_REST(AC, CL, CH, AN, NL, NH, sl, kk) do { \
        G_BL(NL, 0, sl, kk); G_BH(NH, 0, sl, kk); G_M(AC, CL, CH, 0, 2); G_A(AN, 0, sl, kk); G_BL(NL, 1, sl, kk); G_M(AC, CL, CH, 0, 3); G_BH(NH, 1, sl, kk); G_BL(NL, 2, sl, kk); G_M(AC, CL, CH, 1, 0); \
        G_BH(NH, 2, sl, kk); G_BL(NL, 3, sl, kk); G_M(AC, CL, CH, 1, 1); G_BH(NH, 3, sl, kk); G_M(AC, CL, CH, 1, 2); G_A(AN, 1, sl, kk); G_M(AC, CL, CH, 1, 3); } while (0)
#define H_TILE(sl, SYNC) do { G_M(AX, XL, XH, 0, 0); G_M(AX, XL, XH, 0, 1); H_REST(AX, XL, XH, AY, YL, YH, sl, 1); \
        G_M(AY, YL, YH, 0, 0); G_M(AY, YL, YH, 0, 1); SYNC; SCHED; H_REST(AY, YL, YH, AX, XL, XH, ((sl) + 1) & 3, 0); } while (0)
#define H_BAR do { WAIT_L(0); BAR; } while (0)
            H_TILE(0, H_BAR); H_TILE(1, H_BAR); H_TILE(2, WAIT_V(0); H_BAR); H_TILE(3, H_BAR);
            H_TILE(0, H_BAR); H_TILE(1, H_BAR); H_TILE(2, H_BAR); H_TILE(3, );
#undef G_BP
#undef G_BL
#undef G_BH
#undef G_A
#undef G_M
#undef G_REST
#undef G_TILE
#undef H_REST
#undef H_TILE
#undef H_BAR
            asm volatile("s_nop 7"); SCHED;
            BAR; CBAR;
            u32x4 pv[2][2];
#pragma unroll
            for (int m = 0; m < 2; ++m)
#pragma unroll
                for (int s = 0; s < 2; ++s) pv[m][s] = *(const LAS u32x4*)(lds + 131072 + (m * 2 + s) * 4096 + tid * 16);
#pragma unroll
            for (int m = 0; m < 2; ++m)
#pragma unroll
                for (int s = 0; s < 2; ++s) { float lo[4], hi4[4]; const u32x4 p_ = pv[m][s], x_ = xv[m][s];
                    const f32x4 a0 = acc[m][2 * s] + (f32x4){bf_lo(p_[0]), bf_hi(p_[0]), bf_lo(p_[1]), bf_hi(p_[1])}, a1 = acc[m][2 * s + 1] + (f32x4){bf_lo(p_[2]), bf_hi(p_[2]), bf_lo(p_[3]), bf_hi(p_[3])};
#pragma unroll
                    for (int i = 0; i < 4; ++i) { auto r = __builtin_amdgcn_permlane16_swap(__float_as_uint(a0[i]), __float_as_uint(a1[i]), false, false);
                        lo[i] = __uint_as_float(r[0]); hi4[i] = __uint_as_float(r[1]); }
                    u32x4 o; o[0] = cvtpk(lo[0] + bf_lo(x_[0]), lo[1] + bf_hi(x_[0])); o[1] = cvtpk(lo[2] + bf_lo(x_[1]), lo[3] + bf_hi(x_[1]));
                    o[2] = cvtpk(hi4[0] + bf_lo(x_[2]), hi4[1] + bf_hi(x_[2])); o[3] = cvtpk(hi4[2] + bf_lo(x_[3]), hi4[3] + bf_hi(x_[3]));
                    __builtin_nontemporal_store(o, (u32x4*)(out + rowo + (size_t)m * 16 * DM + s * 64));
                }
        }
    }
}

extern "C" void kernel_launch(void* const* d_in, const int* in_sizes, int n_in, void* d_out, int out_size, void* d_ws, size_t ws_size, hipStream_t stream) {
    (void)hipFuncSetAttribute((const void*)k_block, hipFuncAttributeMaxDynamicSharedMemorySize, LDS_BYTES);
    k_block<<<256, 512, LDS_BYTES, stream>>>((const u16*)d_in[0], (const u16*)d_in[1], (const u16*)d_in[2], (const u16*)d_in[4], (const u16*)d_in[5], (const u16*)d_in[6], (char*)d_ws, (u16*)d_out);
}
```
